```python
import torch

def _softmax(x, axis=-1):
    return torch.softmax(x, dim=axis)
def _gelu(x):
    return torch.nn.functional.gelu(x, approximate="tanh")
def _silu(x):
    return torch.nn.functional.silu(x)
def _sigmoid(x):
    return torch.sigmoid(x)
def _layernorm(x, axis=-1, eps=1e-5):
    m = x.mean(dim=axis, keepdim=True)
    v = x.var(dim=axis, keepdim=True, unbiased=False)
    return (x - m) / torch.sqrt(v + eps)
def _scatter_add(tgt, idx, src, axis=0):
    return tgt.clone().index_add_(axis, idx, src)
def _segment_sum(src, seg, n):
    out = torch.zeros((n,) + src.shape[1:], dtype=src.dtype)
    return out.index_add_(0, seg, src)
def _pad(x, pw):
    flat = tuple(v for pair in reversed(pw) for v in pair)
    return torch.nn.functional.pad(x, flat)

kernel_name = 'standalone__gqa_block_cached__near_ridge'

def setup_inputs():

    _w = (lambda a: (lambda t: t.to(torch.bfloat16) if t.is_floating_point() else t)(torch.as_tensor(a)))
    B, Sq, D = 16, 192, 512
    Hq, Hkv, Sk, Dh = 32, 32, 512, 128
    assert Hq % Hkv == 0, (Hq, Hkv)
    return {
        "x": _w((lambda *s: torch.randn(*s, dtype=torch.bfloat16) * 0.8)(B, Sq, D)),
        "k_cache": _w((lambda *s: torch.randn(*s, dtype=torch.bfloat16) * 0.8)(B, Hkv, Sk, Dh)),
        "v_cache": _w((lambda *s: torch.randn(*s, dtype=torch.bfloat16) * 0.05)(B, Hkv, Sk, Dh)),
        "wq": _w((lambda *s: torch.randn(*s, dtype=torch.bfloat16) * 0.8)(D, Hq * Dh) * (D ** -0.5)),
        "wo": _w((lambda *s: torch.randn(*s, dtype=torch.bfloat16) * 0.05)(Hq * Dh, D) * ((Hq * Dh) ** -0.5)),
    }

def reference(x, k_cache, v_cache, wq, wo):
    B, Sq, D = x.shape
    Hq, Dh = 32, 128
    Hkv = k_cache.shape[1]
    rep = Hq // Hkv
    q = (lambda x, *d: x.permute(*d))((x @ wq).reshape(B, Sq, Hq, Dh), 0, 2, 1, 3)
    k = (lambda x, n, axis: torch.repeat_interleave(x, n, dim=axis))(k_cache, rep, axis=1)
    v = (lambda x, n, axis: torch.repeat_interleave(x, n, dim=axis))(v_cache, rep, axis=1)
    scores = torch.einsum("bhqd,bhkd->bhqk", q, k) * (Dh ** -0.5)
    p = _softmax(scores, axis=-1)
    o = torch.einsum("bhqk,bhkd->bhqd", p, v)
    return (lambda x, *d: x.permute(*d))(o, 0, 2, 1, 3).reshape(B, Sq, Hq * Dh) @ wo
```

```cpp
#include <hip/hip_runtime.h>
#include <hip/hip_bf16.h>
typedef unsigned short u16;
__device__ __forceinline__ float bf2f(u16 x){ return __uint_as_float(((unsigned)x)<<16); }
__device__ __forceinline__ u16 f2bf(float f){ unsigned u=__float_as_uint(f); u += 0x7FFF + ((u>>16)&1); return (u16)(u>>16); }
constexpr int B=16,SQ=192,D=512,H=32,SK=512,DH=128;
__global__ void gemm_naive(const u16* A,const u16* Bm,u16* C,int M,int N,int K){
  __shared__ float As[32][33], Bs[32][33];
  int tx=threadIdx.x&31, ty=threadIdx.x>>5;
  int row0=blockIdx.y*32, col0=blockIdx.x*32;
  float acc[4]={0,0,0,0};
  for(int k0=0;k0<K;k0+=32){
    for(int i=ty;i<32;i+=8){ As[i][tx]=bf2f(A[(size_t)(row0+i)*K+k0+tx]); Bs[i][tx]=bf2f(Bm[(size_t)(k0+i)*N+col0+tx]); }
    __syncthreads();
    for(int k=0;k<32;k++){ float b=Bs[k][tx]; for(int r=0;r<4;r++) acc[r]+=As[ty+8*r][k]*b; }
    __syncthreads();
  }
  for(int r=0;r<4;r++) C[(size_t)(row0+ty+8*r)*N+col0+tx]=f2bf(acc[r]);
}
__global__ void attn_naive(const u16* Q,const u16* Kc,const u16* Vc,u16* O){
  int q=blockIdx.x, h=blockIdx.y, b=blockIdx.z; int t=threadIdx.x;
  __shared__ float qs[DH]; __shared__ float s[SK]; __shared__ float red[128];
  qs[t]=bf2f(Q[((size_t)(b*SQ+q))*(H*DH)+h*DH+t]);
  __syncthreads();
  const u16* Kb=Kc+((size_t)(b*H+h))*SK*DH; const u16* Vb=Vc+((size_t)(b*H+h))*SK*DH;
  float mx=-1e30f;
  for(int k=t;k<SK;k+=128){ float a=0; for(int d=0;d<DH;d++) a+=qs[d]*bf2f(Kb[k*DH+d]); a*=0.08838834764831845f; s[k]=a; mx=fmaxf(mx,a);}
  red[t]=mx; __syncthreads();
  for(int o=64;o>0;o>>=1){ if(t<o) red[t]=fmaxf(red[t],red[t+o]); __syncthreads(); }
  mx=red[0]; __syncthreads();
  float sum=0; for(int k=t;k<SK;k+=128){ float e=__expf(s[k]-mx); s[k]=e; sum+=e; }
  red[t]=sum; __syncthreads();
  for(int o=64;o>0;o>>=1){ if(t<o) red[t]+=red[t+o]; __syncthreads(); }
  sum=red[0];
  float a=0; for(int k=0;k<SK;k++) a+=s[k]*bf2f(Vb[k*DH+t]);
  O[((size_t)(b*SQ+q))*(H*DH)+h*DH+t]=f2bf(a/sum);
}
extern "C" void kernel_launch(void* const* d_in, const int* in_sizes, int n_in, void* d_out, int out_size, void* d_ws, size_t ws_size, hipStream_t stream) {
  const u16* x=(const u16*)d_in[0]; const u16* kc=(const u16*)d_in[1]; const u16* vc=(const u16*)d_in[2]; const u16* wq=(const u16*)d_in[3]; const u16* wo=(const u16*)d_in[4];
  u16* Q=(u16*)d_ws; u16* O=Q+(size_t)B*SQ*H*DH;
  gemm_naive<<<dim3(H*DH/32,B*SQ/32),256,0,stream>>>(x,wq,Q,B*SQ,H*DH,D);
  attn_naive<<<dim3(SQ,H,B),128,0,stream>>>(Q,kc,vc,O);
  gemm_naive<<<dim3(D/32,B*SQ/32),256,0,stream>>>(O,wo,(u16*)d_out,B*SQ,D,H*DH);
}
```

```cpp
#include <hip/hip_runtime.h>
#include <hip/hip_bf16.h>


__global__ __launch_bounds__(512, 2) void fused_attn(const void* x, const void* kc, const void* vc, const void* wq, const void* wo, void* out, void* ws) {
  __shared__ __attribute__((aligned(16))) char lds[163840];
  unsigned tid = threadIdx.x;
  asm volatile(
".Lstart:\n"
"s_load_dwordx8 s[4:11], %2, 0x0\n"
"s_load_dwordx4 s[12:15], %2, 0x20\n"
"s_load_dwordx2 s[16:17], %2, 0x30\n"
"v_lshrrev_b32 v1, 6, v0\n"
"v_and_b32 v0, 63, v0\n"
"v_readfirstlane_b32 s56, v1\n"
"s_and_b32 s70, %1, 7\n"
"s_lshr_b32 s71, %1, 3\n"
"s_lshr_b32 s57, s56, 2\n"
"s_and_b32 s56, s56, 3\n"
"s_and_b32 s59, s70, 3\n"
"s_lshr_b32 s70, s70, 2\n"
"s_lshl_b32 s70, s70, 3\n"
"s_lshr_b32 s58, s71, 2\n"
"s_and_b32 s60, s71, 3\n"
"s_lshl_b32 s63, s60, 1\n"
"s_add_u32 s63, s63, s58\n"
"s_add_u32 s63, s63, s59\n"
"s_and_b32 s63, s63, 0\n"
"s_add_u32 s58, s58, s70\n"
"s_lshl_b32 s69, s59, 3\n"
"s_lshl_b32 s70, s60, 1\n"
"s_add_u32 s69, s69, s70\n"
"v_bfe_u32 v1, v0, 2, 3\n"
"v_lshrrev_b32 v2, 5, v0\n"
"v_bfe_u32 v3, v0, 4, 1\n"
"v_lshlrev_b32 v5, 3, v3\n"
"v_lshlrev_b32 v3, 1, v3\n"
"v_and_b32 v4, 3, v0\n"
"v_xor_b32 v7, v4, v3\n"
"v_lshlrev_b32 v4, 4, v7\n"
"v_lshl_add_u32 v4, v2, 6, v4\n"
"v_lshl_add_u32 v254, v1, 8, v4\n"
"v_lshl_add_u32 v255, v1, 10, v4\n"
"v_bfe_u32 v6, v0, 2, 2\n"
"v_add_u32 v5, v5, v6\n"
"v_lshl_add_u32 v202, v5, 13, v4\n"
"s_waitcnt lgkmcnt(0)\n"
"s_mul_i32 s70, s58, 196608\n"
"s_mov_b64 s[28:29], s[4:5]\n"
"s_add_u32 s28, s28, s70\n"
"s_addc_u32 s29, s29, 0\n"
"s_and_b32 s29, s29, 65535\n"
"s_mov_b32 s30, 196608\n"
"s_mov_b32 s31, 131072\n"
"s_mov_b64 s[36:37], s[10:11]\n"
"s_and_b32 s37, s37, 65535\n"
"s_mov_b32 s38, 4194304\n"
"s_mov_b32 s39, 131072\n"
"s_lshl_b32 s64, s69, 8\n"
"s_and_b32 s70, s56, 1\n"
"s_lshl_b32 s70, s70, 17\n"
"s_add_u32 s64, s64, s70\n"
"s_lshr_b32 s70, s56, 1\n"
"s_lshl_b32 s70, s70, 15\n"
"s_add_u32 s64, s64, s70\n"
"s_lshl_b32 s62, s63, 19\n"
"s_lshl_b32 s63, s63, 7\n"
"s_mul_i32 s65, s56, 49152\n"
"s_lshl_b32 s61, s56, 11\n"
"s_mul_i32 s75, s56, 6144\n"
"s_cmp_eq_u32 s57, 0\n"
"s_cbranch_scc1 .LX\n"
"; step -19\n"
"s_setprio 0\n"
"s_add_u32 s71, s62, 0\n"
"s_add_u32 m0, s61, 98304\n"
"s_and_b32 s71, s71, 4194303\n"
"s_add_u32 s71, s71, s64\n"
"buffer_load_dwordx4 v202, s[36:39], s71 offen lds\n"
"s_add_u32 m0, s61, 99200\n"
"s_nop 0\n"
"buffer_load_dwordx4 v202, s[36:39], s71 offen offset:128 lds\n"
"s_add_u32 s71, s62, 0\n"
"s_add_u32 m0, s61, 106240\n"
"s_and_b32 s71, s71, 4194303\n"
"s_add_u32 s71, s71, s64\n"
"buffer_load_dwordx4 v202, s[36:39], s71 offen offset:256 lds\n"
"s_add_u32 m0, s61, 107136\n"
"s_nop 0\n"
"buffer_load_dwordx4 v202, s[36:39], s71 offen offset:384 lds\n"
"s_add_u32 s71, s62, 262144\n"
"s_add_u32 m0, s61, 0\n"
"s_and_b32 s71, s71, 4194303\n"
"s_add_u32 s71, s71, s64\n"
"buffer_load_dwordx4 v202, s[36:39], s71 offen lds\n"
"s_add_u32 m0, s61, 896\n"
"s_nop 0\n"
"buffer_load_dwordx4 v202, s[36:39], s71 offen offset:128 lds\n"
"s_add_u32 s71, s62, 262144\n"
"s_add_u32 m0, s61, 7936\n"
"s_and_b32 s71, s71, 4194303\n"
"s_add_u32 s71, s71, s64\n"
"buffer_load_dwordx4 v202, s[36:39], s71 offen offset:256 lds\n"
"s_add_u32 m0, s61, 8832\n"
"s_nop 0\n"
"buffer_load_dwordx4 v202, s[36:39], s71 offen offset:384 lds\n"
"s_add_u32 s71, s62, 524288\n"
"s_add_u32 m0, s61, 65536\n"
"s_and_b32 s71, s71, 4194303\n"
"s_add_u32 s71, s71, s64\n"
"buffer_load_dwordx4 v202, s[36:39], s71 offen lds\n"
"s_add_u32 m0, s61, 66432\n"
"s_nop 0\n"
"buffer_load_dwordx4 v202, s[36:39], s71 offen offset:128 lds\n"
"s_add_u32 s71, s62, 524288\n"
"s_add_u32 m0, s61, 16128\n"
"s_and_b32 s71, s71, 4194303\n"
"s_add_u32 s71, s71, s64\n"
"buffer_load_dwordx4 v202, s[36:39], s71 offen offset:256 lds\n"
"s_add_u32 m0, s61, 17024\n"
"s_nop 0\n"
"buffer_load_dwordx4 v202, s[36:39], s71 offen offset:384 lds\n"
"s_branch .LY\n"
".LX:\n"
"; step -19\n"
"s_setprio 0\n"
"s_add_u32 m0, s75, 114688\n"
"s_add_u32 s74, s63, 0\n"
"s_and_b32 s74, s74, 1023\n"
"s_add_u32 s74, s74, s65\n"
"buffer_load_dwordx4 v255, s[28:31], s74 offen lds\n"
"s_add_u32 m0, s75, 115712\n"
"s_add_u32 s74, s74, 8192\n"
"buffer_load_dwordx4 v255, s[28:31], s74 offen lds\n"
"s_add_u32 m0, s75, 116736\n"
"s_add_u32 s74, s74, 8192\n"
"buffer_load_dwordx4 v255, s[28:31], s74 offen lds\n"
"s_add_u32 m0, s75, 117760\n"
"s_add_u32 s74, s74, 8192\n"
"buffer_load_dwordx4 v255, s[28:31], s74 offen lds\n"
"s_add_u32 m0, s75, 118784\n"
"s_add_u32 s74, s74, 8192\n"
"buffer_load_dwordx4 v255, s[28:31], s74 offen lds\n"
"s_add_u32 m0, s75, 119808\n"
"s_add_u32 s74, s74, 8192\n"
"buffer_load_dwordx4 v255, s[28:31], s74 offen lds\n"
"s_lshl_b32 s70, s58, 5\n"
"s_add_u32 s70, s70, s69\n"
"s_lshl_b32 s71, s70, 17\n"
"s_mov_b64 s[20:21], s[6:7]\n"
"s_add_u32 s20, s20, s71\n"
"s_addc_u32 s21, s21, 0\n"
"s_and_b32 s21, s21, 65535\n"
"s_mov_b32 s22, 262144\n"
"s_mov_b32 s23, 131072\n"
"s_mov_b64 s[24:25], s[8:9]\n"
"s_add_u32 s24, s24, s71\n"
"s_addc_u32 s25, s25, 0\n"
"s_and_b32 s25, s25, 65535\n"
"s_mov_b32 s26, 262144\n"
"s_mov_b32 s27, 131072\n"
"s_add_u32 s70, s70, s57\n"
"s_mul_i32 s73, s70, 12\n"
"s_mul_i32 s70, s56, 3\n"
"s_add_u32 s73, s73, s70\n"
"s_lshl_b32 s73, s73, 12\n"
"s_mov_b64 s[40:41], s[12:13]\n"
"s_and_b32 s41, s41, 65535\n"
"s_mov_b32 s42, 4194304\n"
"s_mov_b32 s43, 131072\n"
"s_mov_b64 s[52:53], s[14:15]\n"
"s_and_b32 s53, s53, 65535\n"
"s_mov_b32 s54, 3145728\n"
"s_mov_b32 s55, 131072\n"
"s_mov_b64 s[78:79], s[16:17]\n"
"s_mov_b64 s[44:45], s[16:17]\n"
"s_add_u32 s44, s44, 65536\n"
"s_addc_u32 s45, s45, 0\n"
"s_and_b32 s45, s45, 65535\n"
"s_mov_b32 s46, 25165824\n"
"s_mov_b32 s47, 131072\n"
"s_mov_b64 s[48:49], s[16:17]\n"
"s_and_b32 s49, s49, 65535\n"
"s_mov_b32 s50, 37814272\n"
"s_mov_b32 s51, 131072\n"
"s_mov_b32 s76, 1040356334\n"
"s_lshl_b32 s77, s56, 11\n"
"s_lshl_b32 s66, s59, 20\n"
"s_lshl_b32 s71, s60, 8\n"
"s_add_u32 s66, s66, s71\n"
"s_lshl_b32 s71, s56, 13\n"
"s_add_u32 s66, s66, s71\n"
"s_lshl_b32 s67, s60, 18\n"
"s_xor_b32 s34, s59, 3\n"
"s_lshl_b32 s33, s34, 6\n"
"s_and_b32 s33, s33, 128\n"
"s_or_b32 s33, s33, s66\n"
"v_mov_b32 v1, 0\n"
"s_lshl_b32 s70, s58, 6\n"
"buffer_load_dwordx2 v[244:245], v1, s[48:51], s70 offen sc1\n"
"v_lshrrev_b32 v1, 4, v0\n"
"v_bfe_u32 v4, v0, 2, 1\n"
"v_lshlrev_b32 v4, 1, v4\n"
"v_xor_b32 v4, v4, v1\n"
"v_lshlrev_b32 v4, 4, v4\n"
"v_and_b32 v5, 7, v0\n"
"v_lshl_add_u32 v4, v5, 6, v4\n"
"v_bfe_u32 v5, v0, 3, 1\n"
"v_lshl_add_u32 v248, v5, 11, v4\n"
"v_lshl_add_u32 v201, v5, 10, v4\n"
"v_add_u32 v201, s75, v201\n"
"v_lshrrev_b32 v1, 5, v0\n"
"v_bfe_u32 v4, v0, 2, 3\n"
"v_lshlrev_b32 v4, 6, v4\n"
"v_lshl_add_u32 v4, v1, 11, v4\n"
"v_lshl_add_u32 v200, v7, 4, v4\n"
"v_bfe_u32 v5, v0, 4, 1\n"
"v_lshlrev_b32 v5, 1, v5\n"
"v_bfe_u32 v6, v0, 1, 1\n"
"v_xor_b32 v5, v5, v6\n"
"v_lshl_add_u32 v4, v5, 4, v4\n"
"v_and_b32 v6, 1, v0\n"
"v_lshl_add_u32 v249, v6, 3, v4\n"
"v_xor_b32 v250, 32, v249\n"
"v_lshlrev_b32 v253, 4, v0\n"
"s_add_u32 m0, s75, 139264\n"
"s_add_u32 s74, s63, 128\n"
"s_and_b32 s74, s74, 1023\n"
"s_add_u32 s74, s74, s65\n"
"buffer_load_dwordx4 v255, s[28:31], s74 offen lds\n"
"s_add_u32 m0, s75, 140288\n"
"s_add_u32 s74, s74, 8192\n"
"buffer_load_dwordx4 v255, s[28:31], s74 offen lds\n"
"s_add_u32 m0, s75, 141312\n"
"s_add_u32 s74, s74, 8192\n"
"buffer_load_dwordx4 v255, s[28:31], s74 offen lds\n"
"s_add_u32 m0, s75, 142336\n"
"s_add_u32 s74, s74, 8192\n"
"buffer_load_dwordx4 v255, s[28:31], s74 offen lds\n"
"s_add_u32 m0, s75, 143360\n"
"s_add_u32 s74, s74, 8192\n"
"buffer_load_dwordx4 v255, s[28:31], s74 offen lds\n"
"s_add_u32 m0, s75, 144384\n"
"s_add_u32 s74, s74, 8192\n"
"buffer_load_dwordx4 v255, s[28:31], s74 offen lds\n"
"s_waitcnt vmcnt(7)\n"
"s_barrier\n"
"; step -18\n"
"v_add_u32 v200, 98304, v200\n"
"ds_read_b64_tr_b16 v[204:205], v200 offset:0\n"
"ds_read_b64_tr_b16 v[206:207], v200 offset:4096\n"
"ds_read_b64_tr_b16 v[208:209], v200 offset:8\n"
"ds_read_b64_tr_b16 v[210:211], v200 offset:4104\n"
"ds_read_b64_tr_b16 v[212:213], v200 offset:512\n"
"ds_read_b64_tr_b16 v[214:215], v200 offset:4608\n"
"ds_read_b64_tr_b16 v[216:217], v200 offset:520\n"
"ds_read_b64_tr_b16 v[218:219], v200 offset:4616\n"
"ds_read_b64_tr_b16 v[220:221], v200 offset:1024\n"
"ds_read_b64_tr_b16 v[222:223], v200 offset:5120\n"
"ds_read_b64_tr_b16 v[224:225], v200 offset:1032\n"
"ds_read_b64_tr_b16 v[226:227], v200 offset:5128\n"
"ds_read_b64_tr_b16 v[228:229], v200 offset:1536\n"
"ds_read_b64_tr_b16 v[230:231], v200 offset:5632\n"
"ds_read_b64_tr_b16 v[232:233], v200 offset:1544\n"
"ds_read_b64_tr_b16 v[234:235], v200 offset:5640\n"
"v_add_u32 v201, 98304, v201\n"
"ds_read_b128 v[144:147], v201 offset:16384\n"
"ds_read_b128 v[148:151], v201 offset:18432\n"
"ds_read_b128 v[152:155], v201 offset:20480\n"
"ds_read_b128 v[156:159], v201 offset:16896\n"
"ds_read_b128 v[160:163], v201 offset:18944\n"
"ds_read_b128 v[164:167], v201 offset:20992\n"
"s_barrier\n"
"; step -17\n"
"s_waitcnt lgkmcnt(15)\n"
"s_waitcnt lgkmcnt(5)\n"
"v_mfma_f32_16x16x32_bf16 v[0:3], v[204:207], v[144:147], 0\n"
"s_waitcnt lgkmcnt(4)\n"
"v_mfma_f32_16x16x32_bf16 v[4:7], v[204:207], v[148:151], 0\n"
"s_add_u32 m0, s75, 40960\n"
"s_add_u32 s74, s63, 256\n"
"s_and_b32 s74, s74, 1023\n"
"s_add_u32 s74, s74, s65\n"
"buffer_load_dwordx4 v255, s[28:31], s74 offen lds\n"
"s_waitcnt lgkmcnt(3)\n"
"v_mfma_f32_16x16x32_bf16 v[8:11], v[204:207], v[152:155], 0\n"
"v_add_u32 v200, -98304, v200\n"
"ds_read_b64_tr_b16 v[204:205], v200 offset:0\n"
"ds_read_b64_tr_b16 v[206:207], v200 offset:4096\n"
"v_mfma_f32_16x16x32_bf16 v[12:15], v[208:211], v[144:147], 0\n"
"v_mfma_f32_16x16x32_bf16 v[16:19], v[208:211], v[148:151], 0\n"
"v_mfma_f32_16x16x32_bf16 v[20:23], v[208:211], v[152:155], 0\n"
"ds_read_b64_tr_b16 v[208:209], v200 offset:8\n"
"ds_read_b64_tr_b16 v[210:211], v200 offset:4104\n"
"s_add_u32 m0, s75, 41984\n"
"s_add_u32 s74, s74, 8192\n"
"buffer_load_dwordx4 v255, s[28:31], s74 offen lds\n"
"v_mfma_f32_16x16x32_bf16 v[24:27], v[212:215], v[144:147], 0\n"
"v_mfma_f32_16x16x32_bf16 v[28:31], v[212:215], v[148:151], 0\n"
"v_mfma_f32_16x16x32_bf16 v[32:35], v[212:215], v[152:155], 0\n"
"ds_read_b64_tr_b16 v[212:213], v200 offset:512\n"
"ds_read_b64_tr_b16 v[214:215], v200 offset:4608\n"
"v_mfma_f32_16x16x32_bf16 v[36:39], v[216:219], v[144:147], 0\n"
"s_add_u32 m0, s75, 43008\n"
"s_add_u32 s74, s74, 8192\n"
"buffer_load_dwordx4 v255, s[28:31], s74 offen lds\n"
"v_mfma_f32_16x16x32_bf16 v[40:43], v[216:219], v[148:151], 0\n"
"v_mfma_f32_16x16x32_bf16 v[44:47], v[216:219], v[152:155], 0\n"
"ds_read_b64_tr_b16 v[216:217], v200 offset:520\n"
"ds_read_b64_tr_b16 v[218:219], v200 offset:4616\n"
"v_mfma_f32_16x16x32_bf16 v[48:51], v[220:223], v[144:147], 0\n"
"v_mfma_f32_16x16x32_bf16 v[52:55], v[220:223], v[148:151], 0\n"
"s_add_u32 m0, s75, 44032\n"
"s_add_u32 s74, s74, 8192\n"
"buffer_load_dwordx4 v255, s[28:31], s74 offen lds\n"
"v_mfma_f32_16x16x32_bf16 v[56:59], v[220:223], v[152:155], 0\n"
"ds_read_b64_tr_b16 v[220:221], v200 offset:1024\n"
"ds_read_b64_tr_b16 v[222:223], v200 offset:5120\n"
"v_mfma_f32_16x16x32_bf16 v[60:63], v[224:227], v[144:147], 0\n"
"v_mfma_f32_16x16x32_bf16 v[64:67], v[224:227], v[148:151], 0\n"
"v_mfma_f32_16x16x32_bf16 v[68:71], v[224:227], v[152:155], 0\n"
"ds_read_b64_tr_b16 v[224:225], v200 offset:1032\n"
"ds_read_b64_tr_b16 v[226:227], v200 offset:5128\n"
"s_add_u32 m0, s75, 45056\n"
"s_add_u32 s74, s74, 8192\n"
"buffer_load_dwordx4 v255, s[28:31], s74 offen lds\n"
"v_mfma_f32_16x16x32_bf16 v[72:75], v[228:231], v[144:147], 0\n"
"v_mfma_f32_16x16x32_bf16 v[76:79], v[228:231], v[148:151], 0\n"
"v_mfma_f32_16x16x32_bf16 v[80:83], v[228:231], v[152:155], 0\n"
"ds_read_b64_tr_b16 v[228:229], v200 offset:1536\n"
"ds_read_b64_tr_b16 v[230:231], v200 offset:5632\n"
"v_mfma_f32_16x16x32_bf16 v[84:87], v[232:235], v[144:147], 0\n"
"s_add_u32 m0, s75, 46080\n"
"s_add_u32 s74, s74, 8192\n"
"buffer_load_dwordx4 v255, s[28:31], s74 offen lds\n"
"v_mfma_f32_16x16x32_bf16 v[88:91], v[232:235], v[148:151], 0\n"
"v_mfma_f32_16x16x32_bf16 v[92:95], v[232:235], v[152:155], 0\n"
"ds_read_b64_tr_b16 v[232:233], v200 offset:1544\n"
"ds_read_b64_tr_b16 v[234:235], v200 offset:5640\n"
"s_waitcnt vmcnt(6)\n"
"s_waitcnt lgkmcnt(15)\n"
"s_barrier\n"
"; step -16\n"
"s_waitcnt lgkmcnt(12)\n"
"v_mfma_f32_16x16x32_bf16 v[0:3], v[204:207], v[156:159], v[0:3]\n"
"ds_read_b128 v[144:147], v201 offset:40960\n"
"ds_read_b128 v[148:151], v201 offset:43008\n"
"ds_read_b128 v[152:155], v201 offset:45056\n"
"v_mfma_f32_16x16x32_bf16 v[4:7], v[204:207], v[160:163], v[4:7]\n"
"v_mfma_f32_16x16x32_bf16 v[8:11], v[204:207], v[164:167], v[8:11]\n"
"v_add_u32 v200, 65536, v200\n"
"ds_read_b64_tr_b16 v[204:205], v200 offset:0\n"
"ds_read_b64_tr_b16 v[206:207], v200 offset:4096\n"
"v_mfma_f32_16x16x32_bf16 v[12:15], v[208:211], v[156:159], v[12:15]\n"
"s_add_u32 m0, s75, 114688\n"
"s_add_u32 s74, s63, 384\n"
"s_and_b32 s74, s74, 1023\n"
"s_add_u32 s74, s74, s65\n"
"buffer_load_dwordx4 v255, s[28:31], s74 offen lds\n"
"v_mfma_f32_16x16x32_bf16 v[16:19], v[208:211], v[160:163], v[16:19]\n"
"v_mfma_f32_16x16x32_bf16 v[20:23], v[208:211], v[164:167], v[20:23]\n"
"ds_read_b64_tr_b16 v[208:209], v200 offset:8\n"
"ds_read_b64_tr_b16 v[210:211], v200 offset:4104\n"
"s_waitcnt lgkmcnt(15)\n"
"v_mfma_f32_16x16x32_bf16 v[24:27], v[212:215], v[156:159], v[24:27]\n"
"v_mfma_f32_16x16x32_bf16 v[28:31], v[212:215], v[160:163], v[28:31]\n"
"s_add_u32 m0, s75, 115712\n"
"s_add_u32 s74, s74, 8192\n"
"buffer_load_dwordx4 v255, s[28:31], s74 offen lds\n"
"v_mfma_f32_16x16x32_bf16 v[32:35], v[212:215], v[164:167], v[32:35]\n"
"ds_read_b64_tr_b16 v[212:213], v200 offset:512\n"
"ds_read_b64_tr_b16 v[214:215], v200 offset:4608\n"
"v_mfma_f32_16x16x32_bf16 v[36:39], v[216:219], v[156:159], v[36:39]\n"
"v_mfma_f32_16x16x32_bf16 v[40:43], v[216:219], v[160:163], v[40:43]\n"
"s_add_u32 m0, s75, 116736\n"
"s_add_u32 s74, s74, 8192\n"
"buffer_load_dwordx4 v255, s[28:31], s74 offen lds\n"
"v_mfma_f32_16x16x32_bf16 v[44:47], v[216:219], v[164:167], v[44:47]\n"
"ds_read_b64_tr_b16 v[216:217], v200 offset:520\n"
"ds_read_b64_tr_b16 v[218:219], v200 offset:4616\n"
"s_waitcnt lgkmcnt(15)\n"
"v_mfma_f32_16x16x32_bf16 v[48:51], v[220:223], v[156:159], v[48:51]\n"
"v_mfma_f32_16x16x32_bf16 v[52:55], v[220:223], v[160:163], v[52:55]\n"
"v_mfma_f32_16x16x32_bf16 v[56:59], v[220:223], v[164:167], v[56:59]\n"
"ds_read_b64_tr_b16 v[220:221], v200 offset:1024\n"
"ds_read_b64_tr_b16 v[222:223], v200 offset:5120\n"
"s_add_u32 m0, s75, 117760\n"
"s_add_u32 s74, s74, 8192\n"
"buffer_load_dwordx4 v255, s[28:31], s74 offen lds\n"
"v_mfma_f32_16x16x32_bf16 v[60:63], v[224:227], v[156:159], v[60:63]\n"
"v_mfma_f32_16x16x32_bf16 v[64:67], v[224:227], v[160:163], v[64:67]\n"
"v_mfma_f32_16x16x32_bf16 v[68:71], v[224:227], v[164:167], v[68:71]\n"
"ds_read_b64_tr_b16 v[224:225], v200 offset:1032\n"
"ds_read_b64_tr_b16 v[226:227], v200 offset:5128\n"
"s_waitcnt lgkmcnt(15)\n"
"v_mfma_f32_16x16x32_bf16 v[72:75], v[228:231], v[156:159], v[72:75]\n"
"s_add_u32 m0, s75, 118784\n"
"s_add_u32 s74, s74, 8192\n"
"buffer_load_dwordx4 v255, s[28:31], s74 offen lds\n"
"v_mfma_f32_16x16x32_bf16 v[76:79], v[228:231], v[160:163], v[76:79]\n"
"v_mfma_f32_16x16x32_bf16 v[80:83], v[228:231], v[164:167], v[80:83]\n"
"ds_read_b64_tr_b16 v[228:229], v200 offset:1536\n"
"ds_read_b64_tr_b16 v[230:231], v200 offset:5632\n"
"v_mfma_f32_16x16x32_bf16 v[84:87], v[232:235], v[156:159], v[84:87]\n"
"s_add_u32 m0, s75, 119808\n"
"s_add_u32 s74, s74, 8192\n"
"buffer_load_dwordx4 v255, s[28:31], s74 offen lds\n"
"v_mfma_f32_16x16x32_bf16 v[88:91], v[232:235], v[160:163], v[88:91]\n"
"v_mfma_f32_16x16x32_bf16 v[92:95], v[232:235], v[164:167], v[92:95]\n"
"ds_read_b64_tr_b16 v[232:233], v200 offset:1544\n"
"ds_read_b64_tr_b16 v[234:235], v200 offset:5640\n"
"ds_read_b128 v[156:159], v201 offset:41472\n"
"ds_read_b128 v[160:163], v201 offset:43520\n"
"ds_read_b128 v[164:167], v201 offset:45568\n"
"s_barrier\n"
"; step -15\n"
"s_waitcnt lgkmcnt(15)\n"
"v_mfma_f32_16x16x32_bf16 v[0:3], v[204:207], v[144:147], v[0:3]\n"
"v_mfma_f32_16x16x32_bf16 v[4:7], v[204:207], v[148:151], v[4:7]\n"
"v_mfma_f32_16x16x32_bf16 v[8:11], v[204:207], v[152:155], v[8:11]\n"
"ds_read_b64_tr_b16 v[204:205], v200 offset:8192\n"
"ds_read_b64_tr_b16 v[206:207], v200 offset:12288\n"
"v_mfma_f32_16x16x32_bf16 v[12:15], v[208:211], v[144:147], v[12:15]\n"
"v_mfma_f32_16x16x32_bf16 v[16:19], v[208:211], v[148:151], v[16:19]\n"
"v_mfma_f32_16x16x32_bf16 v[20:23], v[208:211], v[152:155], v[20:23]\n"
"ds_read_b64_tr_b16 v[208:209], v200 offset:8200\n"
"ds_read_b64_tr_b16 v[210:211], v200 offset:12296\n"
"s_waitcnt lgkmcnt(15)\n"
"v_mfma_f32_16x16x32_bf16 v[24:27], v[212:215], v[144:147], v[24:27]\n"
"v_mfma_f32_16x16x32_bf16 v[28:31], v[212:215], v[148:151], v[28:31]\n"
"v_mfma_f32_16x16x32_bf16 v[32:35], v[212:215], v[152:155], v[32:35]\n"
"ds_read_b64_tr_b16 v[212:213], v200 offset:8704\n"
"ds_read_b64_tr_b16 v[214:215], v200 offset:12800\n"
"v_mfma_f32_16x16x32_bf16 v[36:39], v[216:219], v[144:147], v[36:39]\n"
"v_mfma_f32_16x16x32_bf16 v[40:43], v[216:219], v[148:151], v[40:43]\n"
"v_mfma_f32_16x16x32_bf16 v[44:47], v[216:219], v[152:155], v[44:47]\n"
"ds_read_b64_tr_b16 v[216:217], v200 offset:8712\n"
"ds_read_b64_tr_b16 v[218:219], v200 offset:12808\n"
"s_waitcnt lgkmcnt(15)\n"
"v_mfma_f32_16x16x32_bf16 v[48:51], v[220:223], v[144:147], v[48:51]\n"
"v_mfma_f32_16x16x32_bf16 v[52:55], v[220:223], v[148:151], v[52:55]\n"
"v_mfma_f32_16x16x32_bf16 v[56:59], v[220:223], v[152:155], v[56:59]\n"
"ds_read_b64_tr_b16 v[220:221], v200 offset:9216\n"
"ds_read_b64_tr_b16 v[222:223], v200 offset:13312\n"
"v_mfma_f32_16x16x32_bf16 v[60:63], v[224:227], v[144:147], v[60:63]\n"
"v_mfma_f32_16x16x32_bf16 v[64:67], v[224:227], v[148:151], v[64:67]\n"
"v_mfma_f32_16x16x32_bf16 v[68:71], v[224:227], v[152:155], v[68:71]\n"
"ds_read_b64_tr_b16 v[224:225], v200 offset:9224\n"
"ds_read_b64_tr_b16 v[226:227], v200 offset:13320\n"
"s_waitcnt lgkmcnt(15)\n"
"v_mfma_f32_16x16x32_bf16 v[72:75], v[228:231], v[144:147], v[72:75]\n"
"v_mfma_f32_16x16x32_bf16 v[76:79], v[228:231], v[148:151], v[76:79]\n"
"v_mfma_f32_16x16x32_bf16 v[80:83], v[228:231], v[152:155], v[80:83]\n"
"ds_read_b64_tr_b16 v[228:229], v200 offset:9728\n"
"ds_read_b64_tr_b16 v[230:231], v200 offset:13824\n"
"v_mfma_f32_16x16x32_bf16 v[84:87], v[232:235], v[144:147], v[84:87]\n"
"v_mfma_f32_16x16x32_bf16 v[88:91], v[232:235], v[148:151], v[88:91]\n"
"v_mfma_f32_16x16x32_bf16 v[92:95], v[232:235], v[152:155], v[92:95]\n"
"ds_read_b64_tr_b16 v[232:233], v200 offset:9736\n"
"ds_read_b64_tr_b16 v[234:235], v200 offset:13832\n"
"s_waitcnt vmcnt(6)\n"
"s_waitcnt lgkmcnt(15)\n"
"s_barrier\n"
"; step -14\n"
"s_waitcnt lgkmcnt(12)\n"
"v_mfma_f32_16x16x32_bf16 v[0:3], v[204:207], v[156:159], v[0:3]\n"
"v_add_u32 v201, -98304, v201\n"
"ds_read_b128 v[144:147], v201 offset:40960\n"
"ds_read_b128 v[148:151], v201 offset:43008\n"
"ds_read_b128 v[152:155], v201 offset:45056\n"
"v_mfma_f32_16x16x32_bf16 v[4:7], v[204:207], v[160:163], v[4:7]\n"
"v_mfma_f32_16x16x32_bf16 v[8:11], v[204:207], v[164:167], v[8:11]\n"
"ds_read_b64_tr_b16 v[204:205], v200 offset:16384\n"
"ds_read_b64_tr_b16 v[206:207], v200 offset:20480\n"
"v_mfma_f32_16x16x32_bf16 v[12:15], v[208:211], v[156:159], v[12:15]\n"
"s_add_u32 m0, s75, 139264\n"
"s_add_u32 s74, s63, 512\n"
"s_and_b32 s74, s74, 1023\n"
"s_add_u32 s74, s74, s65\n"
"buffer_load_dwordx4 v255, s[28:31], s74 offen lds\n"
"v_mfma_f32_16x16x32_bf16 v[16:19], v[208:211], v[160:163], v[16:19]\n"
"v_mfma_f32_16x16x32_bf16 v[20:23], v[208:211], v[164:167], v[20:23]\n"
"ds_read_b64_tr_b16 v[208:209], v200 offset:16392\n"
"ds_read_b64_tr_b16 v[210:211], v200 offset:20488\n"
"s_waitcnt lgkmcnt(15)\n"
"v_mfma_f32_16x16x32_bf16 v[24:27], v[212:215], v[156:159], v[24:27]\n"
"v_mfma_f32_16x16x32_bf16 v[28:31], v[212:215], v[160:163], v[28:31]\n"
"s_add_u32 m0, s75, 140288\n"
"s_add_u32 s74, s74, 8192\n"
"buffer_load_dwordx4 v255, s[28:31], s74 offen lds\n"
"v_mfma_f32_16x16x32_bf16 v[32:35], v[212:215], v[164:167], v[32:35]\n"
"ds_read_b64_tr_b16 v[212:213], v200 offset:16896\n"
"ds_read_b64_tr_b16 v[214:215], v200 offset:20992\n"
"v_mfma_f32_16x16x32_bf16 v[36:39], v[216:219], v[156:159], v[36:39]\n"
"v_mfma_f32_16x16x32_bf16 v[40:43], v[216:219], v[160:163], v[40:43]\n"
"s_add_u32 m0, s75, 141312\n"
"s_add_u32 s74, s74, 8192\n"
"buffer_load_dwordx4 v255, s[28:31], s74 offen lds\n"
"v_mfma_f32_16x16x32_bf16 v[44:47], v[216:219], v[164:167], v[44:47]\n"
"ds_read_b64_tr_b16 v[216:217], v200 offset:16904\n"
"ds_read_b64_tr_b16 v[218:219], v200 offset:21000\n"
"s_waitcnt lgkmcnt(15)\n"
"v_mfma_f32_16x16x32_bf16 v[48:51], v[220:223], v[156:159], v[48:51]\n"
"v_mfma_f32_16x16x32_bf16 v[52:55], v[220:223], v[160:163], v[52:55]\n"
"v_mfma_f32_16x16x32_bf16 v[56:59], v[220:223], v[164:167], v[56:59]\n"
"ds_read_b64_tr_b16 v[220:221], v200 offset:17408\n"
"ds_read_b64_tr_b16 v[222:223], v200 offset:21504\n"
"s_add_u32 m0, s75, 142336\n"
"s_add_u32 s74, s74, 8192\n"
"buffer_load_dwordx4 v255, s[28:31], s74 offen lds\n"
"v_mfma_f32_16x16x32_bf16 v[60:63], v[224:227], v[156:159], v[60:63]\n"
"v_mfma_f32_16x16x32_bf16 v[64:67], v[224:227], v[160:163], v[64:67]\n"
"v_mfma_f32_16x16x32_bf16 v[68:71], v[224:227], v[164:167], v[68:71]\n"
"ds_read_b64_tr_b16 v[224:225], v200 offset:17416\n"
"ds_read_b64_tr_b16 v[226:227], v200 offset:21512\n"
"s_waitcnt lgkmcnt(15)\n"
"v_mfma_f32_16x16x32_bf16 v[72:75], v[228:231], v[156:159], v[72:75]\n"
"s_add_u32 m0, s75, 143360\n"
"s_add_u32 s74, s74, 8192\n"
"buffer_load_dwordx4 v255, s[28:31], s74 offen lds\n"
"v_mfma_f32_16x16x32_bf16 v[76:79], v[228:231], v[160:163], v[76:79]\n"
"v_mfma_f32_16x16x32_bf16 v[80:83], v[228:231], v[164:167], v[80:83]\n"
"ds_read_b64_tr_b16 v[228:229], v200 offset:17920\n"
"ds_read_b64_tr_b16 v[230:231], v200 offset:22016\n"
"v_mfma_f32_16x16x32_bf16 v[84:87], v[232:235], v[156:159], v[84:87]\n"
"s_add_u32 m0, s75, 144384\n"
"s_add_u32 s74, s74, 8192\n"
"buffer_load_dwordx4 v255, s[28:31], s74 offen lds\n"
"v_mfma_f32_16x16x32_bf16 v[88:91], v[232:235], v[160:163], v[88:91]\n"
"v_mfma_f32_16x16x32_bf16 v[92:95], v[232:235], v[164:167], v[92:95]\n"
"ds_read_b64_tr_b16 v[232:233], v200 offset:17928\n"
"ds_read_b64_tr_b16 v[234:235], v200 offset:22024\n"
"ds_read_b128 v[156:159], v201 offset:41472\n"
"ds_read_b128 v[160:163], v201 offset:43520\n"
"ds_read_b128 v[164:167], v201 offset:45568\n"
"s_barrier\n"
"; step -13\n"
"s_waitcnt lgkmcnt(15)\n"
"v_mfma_f32_16x16x32_bf16 v[0:3], v[204:207], v[144:147], v[0:3]\n"
"v_mfma_f32_16x16x32_bf16 v[4:7], v[204:207], v[148:151], v[4:7]\n"
"v_mfma_f32_16x16x32_bf16 v[8:11], v[204:207], v[152:155], v[8:11]\n"
"ds_read_b64_tr_b16 v[204:205], v200 offset:32768\n"
"ds_read_b64_tr_b16 v[206:207], v200 offset:36864\n"
"v_mfma_f32_16x16x32_bf16 v[12:15], v[208:211], v[144:147], v[12:15]\n"
"v_mfma_f32_16x16x32_bf16 v[16:19], v[208:211], v[148:151], v[16:19]\n"
"v_mfma_f32_16x16x32_bf16 v[20:23], v[208:211], v[152:155], v[20:23]\n"
"ds_read_b64_tr_b16 v[208:209], v200 offset:32776\n"
"ds_read_b64_tr_b16 v[210:211], v200 offset:36872\n"
"s_waitcnt lgkmcnt(15)\n"
"v_mfma_f32_16x16x32_bf16 v[24:27], v[212:215], v[144:147], v[24:27]\n"
"v_mfma_f32_16x16x32_bf16 v[28:31], v[212:215], v[148:151], v[28:31]\n"
"v_mfma_f32_16x16x32_bf16 v[32:35], v[212:215], v[152:155], v[32:35]\n"
"ds_read_b64_tr_b16 v[212:213], v200 offset:33280\n"
"ds_read_b64_tr_b16 v[214:215], v200 offset:37376\n"
"v_mfma_f32_16x16x32_bf16 v[36:39], v[216:219], v[144:147], v[36:39]\n"
"v_mfma_f32_16x16x32_bf16 v[40:43], v[216:219], v[148:151], v[40:43]\n"
"v_mfma_f32_16x16x32_bf16 v[44:47], v[216:219], v[152:155], v[44:47]\n"
"ds_read_b64_tr_b16 v[216:217], v200 offset:33288\n"
"ds_read_b64_tr_b16 v[218:219], v200 offset:37384\n"
"s_waitcnt lgkmcnt(15)\n"
"v_mfma_f32_16x16x32_bf16 v[48:51], v[220:223], v[144:147], v[48:51]\n"
"v_mfma_f32_16x16x32_bf16 v[52:55], v[220:223], v[148:151], v[52:55]\n"
"v_mfma_f32_16x16x32_bf16 v[56:59], v[220:223], v[152:155], v[56:59]\n"
"ds_read_b64_tr_b16 v[220:221], v200 offset:33792\n"
"ds_read_b64_tr_b16 v[222:223], v200 offset:37888\n"
"v_mfma_f32_16x16x32_bf16 v[60:63], v[224:227], v[144:147], v[60:63]\n"
"v_mfma_f32_16x16x32_bf16 v[64:67], v[224:227], v[148:151], v[64:67]\n"
"v_mfma_f32_16x16x32_bf16 v[68:71], v[224:227], v[152:155], v[68:71]\n"
"ds_read_b64_tr_b16 v[224:225], v200 offset:33800\n"
"ds_read_b64_tr_b16 v[226:227], v200 offset:37896\n"
"s_waitcnt lgkmcnt(15)\n"
"v_mfma_f32_16x16x32_bf16 v[72:75], v[228:231], v[144:147], v[72:75]\n"
"v_mfma_f32_16x16x32_bf16 v[76:79], v[228:231], v[148:151], v[76:79]\n"
"v_mfma_f32_16x16x32_bf16 v[80:83], v[228:231], v[152:155], v[80:83]\n"
"ds_read_b64_tr_b16 v[228:229], v200 offset:34304\n"
"ds_read_b64_tr_b16 v[230:231], v200 offset:38400\n"
"v_mfma_f32_16x16x32_bf16 v[84:87], v[232:235], v[144:147], v[84:87]\n"
"v_mfma_f32_16x16x32_bf16 v[88:91], v[232:235], v[148:151], v[88:91]\n"
"v_mfma_f32_16x16x32_bf16 v[92:95], v[232:235], v[152:155], v[92:95]\n"
"ds_read_b64_tr_b16 v[232:233], v200 offset:34312\n"
"ds_read_b64_tr_b16 v[234:235], v200 offset:38408\n"
"s_waitcnt vmcnt(6)\n"
"s_waitcnt lgkmcnt(15)\n"
"s_barrier\n"
"; step -12\n"
"s_waitcnt lgkmcnt(12)\n"
"v_mfma_f32_16x16x32_bf16 v[0:3], v[204:207], v[156:159], v[0:3]\n"
"v_add_u32 v201, 98304, v201\n"
"ds_read_b128 v[144:147], v201 offset:16384\n"
"ds_read_b128 v[148:151], v201 offset:18432\n"
"ds_read_b128 v[152:155], v201 offset:20480\n"
"v_mfma_f32_16x16x32_bf16 v[4:7], v[204:207], v[160:163], v[4:7]\n"
"v_mfma_f32_16x16x32_bf16 v[8:11], v[204:207], v[164:167], v[8:11]\n"
"v_add_u32 v200, -65536, v200\n"
"ds_read_b64_tr_b16 v[204:205], v200 offset:0\n"
"ds_read_b64_tr_b16 v[206:207], v200 offset:4096\n"
"v_mfma_f32_16x16x32_bf16 v[12:15], v[208:211], v[156:159], v[12:15]\n"
"s_add_u32 m0, s75, 40960\n"
"s_add_u32 s74, s63, 640\n"
"s_and_b32 s74, s74, 1023\n"
"s_add_u32 s74, s74, s65\n"
"buffer_load_dwordx4 v255, s[28:31], s74 offen lds\n"
"v_mfma_f32_16x16x32_bf16 v[16:19], v[208:211], v[160:163], v[16:19]\n"
"v_mfma_f32_16x16x32_bf16 v[20:23], v[208:211], v[164:167], v[20:23]\n"
"ds_read_b64_tr_b16 v[208:209], v200 offset:8\n"
"ds_read_b64_tr_b16 v[210:211], v200 offset:4104\n"
"s_waitcnt lgkmcnt(15)\n"
"v_mfma_f32_16x16x32_bf16 v[24:27], v[212:215], v[156:159], v[24:27]\n"
"v_mfma_f32_16x16x32_bf16 v[28:31], v[212:215], v[160:163], v[28:31]\n"
"s_add_u32 m0, s75, 41984\n"
"s_add_u32 s74, s74, 8192\n"
"buffer_load_dwordx4 v255, s[28:31], s74 offen lds\n"
"v_mfma_f32_16x16x32_bf16 v[32:35], v[212:215], v[164:167], v[32:35]\n"
"ds_read_b64_tr_b16 v[212:213], v200 offset:512\n"
"ds_read_b64_tr_b16 v[214:215], v200 offset:4608\n"
"v_mfma_f32_16x16x32_bf16 v[36:39], v[216:219], v[156:159], v[36:39]\n"
"v_mfma_f32_16x16x32_bf16 v[40:43], v[216:219], v[160:163], v[40:43]\n"
"s_add_u32 m0, s75, 43008\n"
"s_add_u32 s74, s74, 8192\n"
"buffer_load_dwordx4 v255, s[28:31], s74 offen lds\n"
"v_mfma_f32_16x16x32_bf16 v[44:47], v[216:219], v[164:167], v[44:47]\n"
"ds_read_b64_tr_b16 v[216:217], v200 offset:520\n"
"ds_read_b64_tr_b16 v[218:219], v200 offset:4616\n"
"s_waitcnt lgkmcnt(15)\n"
"v_mfma_f32_16x16x32_bf16 v[48:51], v[220:223], v[156:159], v[48:51]\n"
"v_mfma_f32_16x16x32_bf16 v[52:55], v[220:223], v[160:163], v[52:55]\n"
"v_mfma_f32_16x16x32_bf16 v[56:59], v[220:223], v[164:167], v[56:59]\n"
"ds_read_b64_tr_b16 v[220:221], v200 offset:1024\n"
"ds_read_b64_tr_b16 v[222:223], v200 offset:5120\n"
"s_add_u32 m0, s75, 44032\n"
"s_add_u32 s74, s74, 8192\n"
"buffer_load_dwordx4 v255, s[28:31], s74 offen lds\n"
"v_mfma_f32_16x16x32_bf16 v[60:63], v[224:227], v[156:159], v[60:63]\n"
"v_mfma_f32_16x16x32_bf16 v[64:67], v[224:227], v[160:163], v[64:67]\n"
"v_mfma_f32_16x16x32_bf16 v[68:71], v[224:227], v[164:167], v[68:71]\n"
"ds_read_b64_tr_b16 v[224:225], v200 offset:1032\n"
"ds_read_b64_tr_b16 v[226:227], v200 offset:5128\n"
"s_waitcnt lgkmcnt(15)\n"
"v_mfma_f32_16x16x32_bf16 v[72:75], v[228:231], v[156:159], v[72:75]\n"
"s_add_u32 m0, s75, 45056\n"
"s_add_u32 s74, s74, 8192\n"
"buffer_load_dwordx4 v255, s[28:31], s74 offen lds\n"
"v_mfma_f32_16x16x32_bf16 v[76:79], v[228:231], v[160:163], v[76:79]\n"
"v_mfma_f32_16x16x32_bf16 v[80:83], v[228:231], v[164:167], v[80:83]\n"
"ds_read_b64_tr_b16 v[228:229], v200 offset:1536\n"
"ds_read_b64_tr_b16 v[230:231], v200 offset:5632\n"
"v_mfma_f32_16x16x32_bf16 v[84:87], v[232:235], v[156:159], v[84:87]\n"
"s_add_u32 m0, s75, 46080\n"
"s_add_u32 s74, s74, 8192\n"
"buffer_load_dwordx4 v255, s[28:31], s74 offen lds\n"
"v_mfma_f32_16x16x32_bf16 v[88:91], v[232:235], v[160:163], v[88:91]\n"
"v_mfma_f32_16x16x32_bf16 v[92:95], v[232:235], v[164:167], v[92:95]\n"
"ds_read_b64_tr_b16 v[232:233], v200 offset:1544\n"
"ds_read_b64_tr_b16 v[234:235], v200 offset:5640\n"
"ds_read_b128 v[156:159], v201 offset:16896\n"
"ds_read_b128 v[160:163], v201 offset:18944\n"
"ds_read_b128 v[164:167], v201 offset:20992\n"
"s_barrier\n"
"; step -11\n"
"s_waitcnt lgkmcnt(15)\n"
"v_mfma_f32_16x16x32_bf16 v[0:3], v[204:207], v[144:147], v[0:3]\n"
"v_mfma_f32_16x16x32_bf16 v[4:7], v[204:207], v[148:151], v[4:7]\n"
"v_mfma_f32_16x16x32_bf16 v[8:11], v[204:207], v[152:155], v[8:11]\n"
"v_add_u32 v200, 65536, v200\n"
"ds_read_b64_tr_b16 v[204:205], v200 offset:0\n"
"ds_read_b64_tr_b16 v[206:207], v200 offset:4096\n"
"v_mfma_f32_16x16x32_bf16 v[12:15], v[208:211], v[144:147], v[12:15]\n"
"v_mfma_f32_16x16x32_bf16 v[16:19], v[208:211], v[148:151], v[16:19]\n"
"v_mfma_f32_16x16x32_bf16 v[20:23], v[208:211], v[152:155], v[20:23]\n"
"ds_read_b64_tr_b16 v[208:209], v200 offset:8\n"
"ds_read_b64_tr_b16 v[210:211], v200 offset:4104\n"
"s_waitcnt lgkmcnt(15)\n"
"v_mfma_f32_16x16x32_bf16 v[24:27], v[212:215], v[144:147], v[24:27]\n"
"v_mfma_f32_16x16x32_bf16 v[28:31], v[212:215], v[148:151], v[28:31]\n"
"v_mfma_f32_16x16x32_bf16 v[32:35], v[212:215], v[152:155], v[32:35]\n"
"ds_read_b64_tr_b16 v[212:213], v200 offset:512\n"
"ds_read_b64_tr_b16 v[214:215], v200 offset:4608\n"
"v_mfma_f32_16x16x32_bf16 v[36:39], v[216:219], v[144:147], v[36:39]\n"
"v_mfma_f32_16x16x32_bf16 v[40:43], v[216:219], v[148:151], v[40:43]\n"
"v_mfma_f32_16x16x32_bf16 v[44:47], v[216:219], v[152:155], v[44:47]\n"
"ds_read_b64_tr_b16 v[216:217], v200 offset:520\n"
"ds_read_b64_tr_b16 v[218:219], v200 offset:4616\n"
"s_waitcnt lgkmcnt(15)\n"
"v_mfma_f32_16x16x32_bf16 v[48:51], v[220:223], v[144:147], v[48:51]\n"
"v_mfma_f32_16x16x32_bf16 v[52:55], v[220:223], v[148:151], v[52:55]\n"
"v_mfma_f32_16x16x32_bf16 v[56:59], v[220:223], v[152:155], v[56:59]\n"
"ds_read_b64_tr_b16 v[220:221], v200 offset:1024\n"
"ds_read_b64_tr_b16 v[222:223], v200 offset:5120\n"
"v_mfma_f32_16x16x32_bf16 v[60:63], v[224:227], v[144:147], v[60:63]\n"
"v_mfma_f32_16x16x32_bf16 v[64:67], v[224:227], v[148:151], v[64:67]\n"
"v_mfma_f32_16x16x32_bf16 v[68:71], v[224:227], v[152:155], v[68:71]\n"
"ds_read_b64_tr_b16 v[224:225], v200 offset:1032\n"
"ds_read_b64_tr_b16 v[226:227], v200 offset:5128\n"
"s_waitcnt lgkmcnt(15)\n"
"v_mfma_f32_16x16x32_bf16 v[72:75], v[228:231], v[144:147], v[72:75]\n"
"v_mfma_f32_16x16x32_bf16 v[76:79], v[228:231], v[148:151], v[76:79]\n"
"v_mfma_f32_16x16x32_bf16 v[80:83], v[228:231], v[152:155], v[80:83]\n"
"ds_read_b64_tr_b16 v[228:229], v200 offset:1536\n"
"ds_read_b64_tr_b16 v[230:231], v200 offset:5632\n"
"v_mfma_f32_16x16x32_bf16 v[84:87], v[232:235], v[144:147], v[84:87]\n"
"v_mfma_f32_16x16x32_bf16 v[88:91], v[232:235], v[148:151], v[88:91]\n"
"v_mfma_f32_16x16x32_bf16 v[92:95], v[232:235], v[152:155], v[92:95]\n"
"ds_read_b64_tr_b16 v[232:233], v200 offset:1544\n"
"ds_read_b64_tr_b16 v[234:235], v200 offset:5640\n"
"s_waitcnt vmcnt(6)\n"
"s_waitcnt lgkmcnt(15)\n"
"s_barrier\n"
"; step -10\n"
"s_waitcnt lgkmcnt(12)\n"
"v_mfma_f32_16x16x32_bf16 v[0:3], v[204:207], v[156:159], v[0:3]\n"
"ds_read_b128 v[144:147], v201 offset:40960\n"
"ds_read_b128 v[148:151], v201 offset:43008\n"
"ds_read_b128 v[152:155], v201 offset:45056\n"
"v_mfma_f32_16x16x32_bf16 v[4:7], v[204:207], v[160:163], v[4:7]\n"
"v_mfma_f32_16x16x32_bf16 v[8:11], v[204:207], v[164:167], v[8:11]\n"
"ds_read_b64_tr_b16 v[204:205], v200 offset:8192\n"
"ds_read_b64_tr_b16 v[206:207], v200 offset:12288\n"
"v_mfma_f32_16x16x32_bf16 v[12:15], v[208:211], v[156:159], v[12:15]\n"
"s_add_u32 m0, s75, 114688\n"
"s_add_u32 s74, s63, 768\n"
"s_and_b32 s74, s74, 1023\n"
"s_add_u32 s74, s74, s65\n"
"buffer_load_dwordx4 v255, s[28:31], s74 offen lds\n"
"v_mfma_f32_16x16x32_bf16 v[16:19], v[208:211], v[160:163], v[16:19]\n"
"v_mfma_f32_16x16x32_bf16 v[20:23], v[208:211], v[164:167], v[20:23]\n"
"ds_read_b64_tr_b16 v[208:209], v200 offset:8200\n"
"ds_read_b64_tr_b16 v[210:211], v200 offset:12296\n"
"s_waitcnt lgkmcnt(15)\n"
"v_mfma_f32_16x16x32_bf16 v[24:27], v[212:215], v[156:159], v[24:27]\n"
"v_mfma_f32_16x16x32_bf16 v[28:31], v[212:215], v[160:163], v[28:31]\n"
"s_add_u32 m0, s75, 115712\n"
"s_add_u32 s74, s74, 8192\n"
"buffer_load_dwordx4 v255, s[28:31], s74 offen lds\n"
"v_mfma_f32_16x16x32_bf16 v[32:35], v[212:215], v[164:167], v[32:35]\n"
"ds_read_b64_tr_b16 v[212:213], v200 offset:8704\n"
"ds_read_b64_tr_b16 v[214:215], v200 offset:12800\n"
"v_mfma_f32_16x16x32_bf16 v[36:39], v[216:219], v[156:159], v[36:39]\n"
"v_mfma_f32_16x16x32_bf16 v[40:43], v[216:219], v[160:163], v[40:43]\n"
"s_add_u32 m0, s75, 116736\n"
"s_add_u32 s74, s74, 8192\n"
"buffer_load_dwordx4 v255, s[28:31], s74 offen lds\n"
"v_mfma_f32_16x16x32_bf16 v[44:47], v[216:219], v[164:167], v[44:47]\n"
"ds_read_b64_tr_b16 v[216:217], v200 offset:8712\n"
"ds_read_b64_tr_b16 v[218:219], v200 offset:12808\n"
"s_waitcnt lgkmcnt(15)\n"
"v_mfma_f32_16x16x32_bf16 v[48:51], v[220:223], v[156:159], v[48:51]\n"
"v_mfma_f32_16x16x32_bf16 v[52:55], v[220:223], v[160:163], v[52:55]\n"
"v_mfma_f32_16x16x32_bf16 v[56:59], v[220:223], v[164:167], v[56:59]\n"
"ds_read_b64_tr_b16 v[220:221], v200 offset:9216\n"
"ds_read_b64_tr_b16 v[222:223], v200 offset:13312\n"
"s_add_u32 m0, s75, 117760\n"
"s_add_u32 s74, s74, 8192\n"
"buffer_load_dwordx4 v255, s[28:31], s74 offen lds\n"
"v_mfma_f32_16x16x32_bf16 v[60:63], v[224:227], v[156:159], v[60:63]\n"
"v_mfma_f32_16x16x32_bf16 v[64:67], v[224:227], v[160:163], v[64:67]\n"
"v_mfma_f32_16x16x32_bf16 v[68:71], v[224:227], v[164:167], v[68:71]\n"
"ds_read_b64_tr_b16 v[224:225], v200 offset:9224\n"
"ds_read_b64_tr_b16 v[226:227], v200 offset:13320\n"
"s_waitcnt lgkmcnt(15)\n"
"v_mfma_f32_16x16x32_bf16 v[72:75], v[228:231], v[156:159], v[72:75]\n"
"s_add_u32 m0, s75, 118784\n"
"s_add_u32 s74, s74, 8192\n"
"buffer_load_dwordx4 v255, s[28:31], s74 offen lds\n"
"v_mfma_f32_16x16x32_bf16 v[76:79], v[228:231], v[160:163], v[76:79]\n"
"v_mfma_f32_16x16x32_bf16 v[80:83], v[228:231], v[164:167], v[80:83]\n"
"ds_read_b64_tr_b16 v[228:229], v200 offset:9728\n"
"ds_read_b64_tr_b16 v[230:231], v200 offset:13824\n"
"v_mfma_f32_16x16x32_bf16 v[84:87], v[232:235], v[156:159], v[84:87]\n"
"s_add_u32 m0, s75, 119808\n"
"s_add_u32 s74, s74, 8192\n"
"buffer_load_dwordx4 v255, s[28:31], s74 offen lds\n"
"v_mfma_f32_16x16x32_bf16 v[88:91], v[232:235], v[160:163], v[88:91]\n"
"v_mfma_f32_16x16x32_bf16 v[92:95], v[232:235], v[164:167], v[92:95]\n"
"ds_read_b64_tr_b16 v[232:233], v200 offset:9736\n"
"ds_read_b64_tr_b16 v[234:235], v200 offset:13832\n"
"ds_read_b128 v[156:159], v201 offset:41472\n"
"ds_read_b128 v[160:163], v201 offset:43520\n"
"ds_read_b128 v[164:167], v201 offset:45568\n"
"s_barrier\n"
"; step -9\n"
"s_waitcnt lgkmcnt(15)\n"
"v_mfma_f32_16x16x32_bf16 v[0:3], v[204:207], v[144:147], v[0:3]\n"
"v_mfma_f32_16x16x32_bf16 v[4:7], v[204:207], v[148:151], v[4:7]\n"
"v_mfma_f32_16x16x32_bf16 v[8:11], v[204:207], v[152:155], v[8:11]\n"
"ds_read_b64_tr_b16 v[204:205], v200 offset:16384\n"
"ds_read_b64_tr_b16 v[206:207], v200 offset:20480\n"
"v_mfma_f32_16x16x32_bf16 v[12:15], v[208:211], v[144:147], v[12:15]\n"
"v_mfma_f32_16x16x32_bf16 v[16:19], v[208:211], v[148:151], v[16:19]\n"
"v_mfma_f32_16x16x32_bf16 v[20:23], v[208:211], v[152:155], v[20:23]\n"
"ds_read_b64_tr_b16 v[208:209], v200 offset:16392\n"
"ds_read_b64_tr_b16 v[210:211], v200 offset:20488\n"
"s_waitcnt lgkmcnt(15)\n"
"v_mfma_f32_16x16x32_bf16 v[24:27], v[212:215], v[144:147], v[24:27]\n"
"v_mfma_f32_16x16x32_bf16 v[28:31], v[212:215], v[148:151], v[28:31]\n"
"v_mfma_f32_16x16x32_bf16 v[32:35], v[212:215], v[152:155], v[32:35]\n"
"ds_read_b64_tr_b16 v[212:213], v200 offset:16896\n"
"ds_read_b64_tr_b16 v[214:215], v200 offset:20992\n"
"v_mfma_f32_16x16x32_bf16 v[36:39], v[216:219], v[144:147], v[36:39]\n"
"v_mfma_f32_16x16x32_bf16 v[40:43], v[216:219], v[148:151], v[40:43]\n"
"v_mfma_f32_16x16x32_bf16 v[44:47], v[216:219], v[152:155], v[44:47]\n"
"ds_read_b64_tr_b16 v[216:217], v200 offset:16904\n"
"ds_read_b64_tr_b16 v[218:219], v200 offset:21000\n"
"s_waitcnt lgkmcnt(15)\n"
"v_mfma_f32_16x16x32_bf16 v[48:51], v[220:223], v[144:147], v[48:51]\n"
"v_mfma_f32_16x16x32_bf16 v[52:55], v[220:223], v[148:151], v[52:55]\n"
"v_mfma_f32_16x16x32_bf16 v[56:59], v[220:223], v[152:155], v[56:59]\n"
"ds_read_b64_tr_b16 v[220:221], v200 offset:17408\n"
"ds_read_b64_tr_b16 v[222:223], v200 offset:21504\n"
"v_mfma_f32_16x16x32_bf16 v[60:63], v[224:227], v[144:147], v[60:63]\n"
"v_mfma_f32_16x16x32_bf16 v[64:67], v[224:227], v[148:151], v[64:67]\n"
"v_mfma_f32_16x16x32_bf16 v[68:71], v[224:227], v[152:155], v[68:71]\n"
"ds_read_b64_tr_b16 v[224:225], v200 offset:17416\n"
"ds_read_b64_tr_b16 v[226:227], v200 offset:21512\n"
"s_waitcnt lgkmcnt(15)\n"
"v_mfma_f32_16x16x32_bf16 v[72:75], v[228:231], v[144:147], v[72:75]\n"
"v_mfma_f32_16x16x32_bf16 v[76:79], v[228:231], v[148:151], v[76:79]\n"
"v_mfma_f32_16x16x32_bf16 v[80:83], v[228:231], v[152:155], v[80:83]\n"
"ds_read_b64_tr_b16 v[228:229], v200 offset:17920\n"
"ds_read_b64_tr_b16 v[230:231], v200 offset:22016\n"
"v_mfma_f32_16x16x32_bf16 v[84:87], v[232:235], v[144:147], v[84:87]\n"
"v_mfma_f32_16x16x32_bf16 v[88:91], v[232:235], v[148:151], v[88:91]\n"
"v_mfma_f32_16x16x32_bf16 v[92:95], v[232:235], v[152:155], v[92:95]\n"
"ds_read_b64_tr_b16 v[232:233], v200 offset:17928\n"
"ds_read_b64_tr_b16 v[234:235], v200 offset:22024\n"
"s_waitcnt vmcnt(6)\n"
"s_waitcnt lgkmcnt(15)\n"
"s_barrier\n"
"; step -8\n"
"s_waitcnt lgkmcnt(12)\n"
"v_mfma_f32_16x16x32_bf16 v[0:3], v[204:207], v[156:159], v[0:3]\n"
"v_add_u32 v201, -98304, v201\n"
"ds_read_b128 v[144:147], v201 offset:40960\n"
"ds_read_b128 v[148:151], v201 offset:43008\n"
"ds_read_b128 v[152:155], v201 offset:45056\n"
"v_mfma_f32_16x16x32_bf16 v[4:7], v[204:207], v[160:163], v[4:7]\n"
"v_mfma_f32_16x16x32_bf16 v[8:11], v[204:207], v[164:167], v[8:11]\n"
"ds_read_b64_tr_b16 v[204:205], v200 offset:32768\n"
"ds_read_b64_tr_b16 v[206:207], v200 offset:36864\n"
"v_mfma_f32_16x16x32_bf16 v[12:15], v[208:211], v[156:159], v[12:15]\n"
"s_add_u32 m0, s75, 139264\n"
"s_add_u32 s74, s63, 896\n"
"s_and_b32 s74, s74, 1023\n"
"s_add_u32 s74, s74, s65\n"
"buffer_load_dwordx4 v255, s[28:31], s74 offen lds\n"
"v_mfma_f32_16x16x32_bf16 v[16:19], v[208:211], v[160:163], v[16:19]\n"
"v_mfma_f32_16x16x32_bf16 v[20:23], v[208:211], v[164:167], v[20:23]\n"
"ds_read_b64_tr_b16 v[208:209], v200 offset:32776\n"
"ds_read_b64_tr_b16 v[210:211], v200 offset:36872\n"
"s_waitcnt lgkmcnt(15)\n"
"v_mfma_f32_16x16x32_bf16 v[24:27], v[212:215], v[156:159], v[24:27]\n"
"v_mfma_f32_16x16x32_bf16 v[28:31], v[212:215], v[160:163], v[28:31]\n"
"s_add_u32 m0, s75, 140288\n"
"s_add_u32 s74, s74, 8192\n"
"buffer_load_dwordx4 v255, s[28:31], s74 offen lds\n"
"v_mfma_f32_16x16x32_bf16 v[32:35], v[212:215], v[164:167], v[32:35]\n"
"ds_read_b64_tr_b16 v[212:213], v200 offset:33280\n"
"ds_read_b64_tr_b16 v[214:215], v200 offset:37376\n"
"v_mfma_f32_16x16x32_bf16 v[36:39], v[216:219], v[156:159], v[36:39]\n"
"v_mfma_f32_16x16x32_bf16 v[40:43], v[216:219], v[160:163], v[40:43]\n"
"s_add_u32 m0, s75, 141312\n"
"s_add_u32 s74, s74, 8192\n"
"buffer_load_dwordx4 v255, s[28:31], s74 offen lds\n"
"v_mfma_f32_16x16x32_bf16 v[44:47], v[216:219], v[164:167], v[44:47]\n"
"ds_read_b64_tr_b16 v[216:217], v200 offset:33288\n"
"ds_read_b64_tr_b16 v[218:219], v200 offset:37384\n"
"s_waitcnt lgkmcnt(15)\n"
"v_mfma_f32_16x16x32_bf16 v[48:51], v[220:223], v[156:159], v[48:51]\n"
"v_mfma_f32_16x16x32_bf16 v[52:55], v[220:223], v[160:163], v[52:55]\n"
"v_mfma_f32_16x16x32_bf16 v[56:59], v[220:223], v[164:167], v[56:59]\n"
"ds_read_b64_tr_b16 v[220:221], v200 offset:33792\n"
"ds_read_b64_tr_b16 v[222:223], v200 offset:37888\n"
"s_add_u32 m0, s75, 142336\n"
"s_add_u32 s74, s74, 8192\n"
"buffer_load_dwordx4 v255, s[28:31], s74 offen lds\n"
"v_mfma_f32_16x16x32_bf16 v[60:63], v[224:227], v[156:159], v[60:63]\n"
"v_mfma_f32_16x16x32_bf16 v[64:67], v[224:227], v[160:163], v[64:67]\n"
"v_mfma_f32_16x16x32_bf16 v[68:71], v[224:227], v[164:167], v[68:71]\n"
"ds_read_b64_tr_b16 v[224:225], v200 offset:33800\n"
"ds_read_b64_tr_b16 v[226:227], v200 offset:37896\n"
"s_waitcnt lgkmcnt(15)\n"
"v_mfma_f32_16x16x32_bf16 v[72:75], v[228:231], v[156:159], v[72:75]\n"
"s_add_u32 m0, s75, 143360\n"
"s_add_u32 s74, s74, 8192\n"
"buffer_load_dwordx4 v255, s[28:31], s74 offen lds\n"
"v_mfma_f32_16x16x32_bf16 v[76:79], v[228:231], v[160:163], v[76:79]\n"
"v_mfma_f32_16x16x32_bf16 v[80:83], v[228:231], v[164:167], v[80:83]\n"
"ds_read_b64_tr_b16 v[228:229], v200 offset:34304\n"
"ds_read_b64_tr_b16 v[230:231], v200 offset:38400\n"
"v_mfma_f32_16x16x32_bf16 v[84:87], v[232:235], v[156:159], v[84:87]\n"
"s_add_u32 m0, s75, 144384\n"
"s_add_u32 s74, s74, 8192\n"
"buffer_load_dwordx4 v255, s[28:31], s74 offen lds\n"
"v_mfma_f32_16x16x32_bf16 v[88:91], v[232:235], v[160:163], v[88:91]\n"
"v_mfma_f32_16x16x32_bf16 v[92:95], v[232:235], v[164:167], v[92:95]\n"
"ds_read_b64_tr_b16 v[232:233], v200 offset:34312\n"
"ds_read_b64_tr_b16 v[234:235], v200 offset:38408\n"
"ds_read_b128 v[156:159], v201 offset:41472\n"
"ds_read_b128 v[160:163], v201 offset:43520\n"
"ds_read_b128 v[164:167], v201 offset:45568\n"
"s_barrier\n"
"; step -7\n"
"s_waitcnt lgkmcnt(15)\n"
"v_mfma_f32_16x16x32_bf16 v[0:3], v[204:207], v[144:147], v[0:3]\n"
"v_mfma_f32_16x16x32_bf16 v[4:7], v[204:207], v[148:151], v[4:7]\n"
"v_mfma_f32_16x16x32_bf16 v[8:11], v[204:207], v[152:155], v[8:11]\n"
"v_add_u32 v200, -65536, v200\n"
"ds_read_b64_tr_b16 v[204:205], v200 offset:0\n"
"ds_read_b64_tr_b16 v[206:207], v200 offset:4096\n"
"v_mfma_f32_16x16x32_bf16 v[12:15], v[208:211], v[144:147], v[12:15]\n"
"v_mfma_f32_16x16x32_bf16 v[16:19], v[208:211], v[148:151], v[16:19]\n"
"v_mfma_f32_16x16x32_bf16 v[20:23], v[208:211], v[152:155], v[20:23]\n"
"ds_read_b64_tr_b16 v[208:209], v200 offset:8\n"
"ds_read_b64_tr_b16 v[210:211], v200 offset:4104\n"
"s_waitcnt lgkmcnt(15)\n"
"v_mfma_f32_16x16x32_bf16 v[24:27], v[212:215], v[144:147], v[24:27]\n"
"v_mfma_f32_16x16x32_bf16 v[28:31], v[212:215], v[148:151], v[28:31]\n"
"v_mfma_f32_16x16x32_bf16 v[32:35], v[212:215], v[152:155], v[32:35]\n"
"ds_read_b64_tr_b16 v[212:213], v200 offset:512\n"
"ds_read_b64_tr_b16 v[214:215], v200 offset:4608\n"
"v_mfma_f32_16x16x32_bf16 v[36:39], v[216:219], v[144:147], v[36:39]\n"
"v_mfma_f32_16x16x32_bf16 v[40:43], v[216:219], v[148:151], v[40:43]\n"
"v_mfma_f32_16x16x32_bf16 v[44:47], v[216:219], v[152:155], v[44:47]\n"
"ds_read_b64_tr_b16 v[216:217], v200 offset:520\n"
"ds_read_b64_tr_b16 v[218:219], v200 offset:4616\n"
"s_waitcnt lgkmcnt(15)\n"
"v_mfma_f32_16x16x32_bf16 v[48:51], v[220:223], v[144:147], v[48:51]\n"
"v_mfma_f32_16x16x32_bf16 v[52:55], v[220:223], v[148:151], v[52:55]\n"
"v_mfma_f32_16x16x32_bf16 v[56:59], v[220:223], v[152:155], v[56:59]\n"
"ds_read_b64_tr_b16 v[220:221], v200 offset:1024\n"
"ds_read_b64_tr_b16 v[222:223], v200 offset:5120\n"
"v_mfma_f32_16x16x32_bf16 v[60:63], v[224:227], v[144:147], v[60:63]\n"
"v_mfma_f32_16x16x32_bf16 v[64:67], v[224:227], v[148:151], v[64:67]\n"
"v_mfma_f32_16x16x32_bf16 v[68:71], v[224:227], v[152:155], v[68:71]\n"
"ds_read_b64_tr_b16 v[224:225], v200 offset:1032\n"
"ds_read_b64_tr_b16 v[226:227], v200 offset:5128\n"
"s_waitcnt lgkmcnt(15)\n"
"v_mfma_f32_16x16x32_bf16 v[72:75], v[228:231], v[144:147], v[72:75]\n"
"v_mfma_f32_16x16x32_bf16 v[76:79], v[228:231], v[148:151], v[76:79]\n"
"v_mfma_f32_16x16x32_bf16 v[80:83], v[228:231], v[152:155], v[80:83]\n"
"ds_read_b64_tr_b16 v[228:229], v200 offset:1536\n"
"ds_read_b64_tr_b16 v[230:231], v200 offset:5632\n"
"v_mfma_f32_16x16x32_bf16 v[84:87], v[232:235], v[144:147], v[84:87]\n"
"v_mfma_f32_16x16x32_bf16 v[88:91], v[232:235], v[148:151], v[88:91]\n"
"v_mfma_f32_16x16x32_bf16 v[92:95], v[232:235], v[152:155], v[92:95]\n"
"ds_read_b64_tr_b16 v[232:233], v200 offset:1544\n"
"ds_read_b64_tr_b16 v[234:235], v200 offset:5640\n"
"s_waitcnt vmcnt(6)\n"
"s_waitcnt lgkmcnt(15)\n"
"s_barrier\n"
"; step -6\n"
"s_waitcnt lgkmcnt(12)\n"
"v_mfma_f32_16x16x32_bf16 v[0:3], v[204:207], v[156:159], v[0:3]\n"
"v_mfma_f32_16x16x32_bf16 v[4:7], v[204:207], v[160:163], v[4:7]\n"
"v_mfma_f32_16x16x32_bf16 v[8:11], v[204:207], v[164:167], v[8:11]\n"
"v_add_u32 v200, 65536, v200\n"
"ds_read_b64_tr_b16 v[204:205], v200 offset:0\n"
"ds_read_b64_tr_b16 v[206:207], v200 offset:4096\n"
"v_mfma_f32_16x16x32_bf16 v[12:15], v[208:211], v[156:159], v[12:15]\n"
"v_mfma_f32_16x16x32_bf16 v[16:19], v[208:211], v[160:163], v[16:19]\n"
"v_add_u32 v201, 98304, v201\n"
"ds_read_b128 v[144:147], v201 offset:16384\n"
"ds_read_b128 v[148:151], v201 offset:18432\n"
"ds_read_b128 v[152:155], v201 offset:20480\n"
"v_mfma_f32_16x16x32_bf16 v[20:23], v[208:211], v[164:167], v[20:23]\n"
"ds_read_b64_tr_b16 v[208:209], v200 offset:8\n"
"ds_read_b64_tr_b16 v[210:211], v200 offset:4104\n"
"s_waitcnt lgkmcnt(15)\n"
"v_mfma_f32_16x16x32_bf16 v[24:27], v[212:215], v[156:159], v[24:27]\n"
"v_mfma_f32_16x16x32_bf16 v[28:31], v[212:215], v[160:163], v[28:31]\n"
"v_mfma_f32_16x16x32_bf16 v[32:35], v[212:215], v[164:167], v[32:35]\n"
"ds_read_b64_tr_b16 v[212:213], v200 offset:512\n"
"ds_read_b64_tr_b16 v[214:215], v200 offset:4608\n"
"v_mfma_f32_16x16x32_bf16 v[36:39], v[216:219], v[156:159], v[36:39]\n"
"v_mfma_f32_16x16x32_bf16 v[40:43], v[216:219], v[160:163], v[40:43]\n"
"v_mfma_f32_16x16x32_bf16 v[44:47], v[216:219], v[164:167], v[44:47]\n"
"ds_read_b64_tr_b16 v[216:217], v200 offset:520\n"
"ds_read_b64_tr_b16 v[218:219], v200 offset:4616\n"
"s_waitcnt lgkmcnt(15)\n"
"v_mfma_f32_16x16x32_bf16 v[48:51], v[220:223], v[156:159], v[48:51]\n"
"v_mfma_f32_16x16x32_bf16 v[52:55], v[220:223], v[160:163], v[52:55]\n"
"v_mfma_f32_16x16x32_bf16 v[56:59], v[220:223], v[164:167], v[56:59]\n"
"ds_read_b64_tr_b16 v[220:221], v200 offset:1024\n"
"ds_read_b64_tr_b16 v[222:223], v200 offset:5120\n"
"v_mfma_f32_16x16x32_bf16 v[60:63], v[224:227], v[156:159], v[60:63]\n"
"v_mfma_f32_16x16x32_bf16 v[64:67], v[224:227], v[160:163], v[64:67]\n"
"v_mfma_f32_16x16x32_bf16 v[68:71], v[224:227], v[164:167], v[68:71]\n"
"ds_read_b64_tr_b16 v[224:225], v200 offset:1032\n"
"ds_read_b64_tr_b16 v[226:227], v200 offset:5128\n"
"s_waitcnt lgkmcnt(15)\n"
"v_mfma_f32_16x16x32_bf16 v[72:75], v[228:231], v[156:159], v[72:75]\n"
"v_mfma_f32_16x16x32_bf16 v[76:79], v[228:231], v[160:163], v[76:79]\n"
"v_mfma_f32_16x16x32_bf16 v[80:83], v[228:231], v[164:167], v[80:83]\n"
"ds_read_b64_tr_b16 v[228:229], v200 offset:1536\n"
"ds_read_b64_tr_b16 v[230:231], v200 offset:5632\n"
"v_mfma_f32_16x16x32_bf16 v[84:87], v[232:235], v[156:159], v[84:87]\n"
"v_mfma_f32_16x16x32_bf16 v[88:91], v[232:235], v[160:163], v[88:91]\n"
"v_mfma_f32_16x16x32_bf16 v[92:95], v[232:235], v[164:167], v[92:95]\n"
"ds_read_b64_tr_b16 v[232:233], v200 offset:1544\n"
"ds_read_b64_tr_b16 v[234:235], v200 offset:5640\n"
"ds_read_b128 v[156:159], v201 offset:16896\n"
"ds_read_b128 v[160:163], v201 offset:18944\n"
"ds_read_b128 v[164:167], v201 offset:20992\n"
"s_barrier\n"
"; step -5\n"
"s_waitcnt lgkmcnt(15)\n"
"v_mfma_f32_16x16x32_bf16 v[0:3], v[204:207], v[144:147], v[0:3]\n"
"v_mfma_f32_16x16x32_bf16 v[4:7], v[204:207], v[148:151], v[4:7]\n"
"v_mfma_f32_16x16x32_bf16 v[8:11], v[204:207], v[152:155], v[8:11]\n"
"ds_read_b64_tr_b16 v[204:205], v200 offset:8192\n"
"ds_read_b64_tr_b16 v[206:207], v200 offset:12288\n"
"v_mfma_f32_16x16x32_bf16 v[12:15], v[208:211], v[144:147], v[12:15]\n"
"v_mfma_f32_16x16x32_bf16 v[16:19], v[208:211], v[148:151], v[16:19]\n"
"v_mfma_f32_16x16x32_bf16 v[20:23], v[208:211], v[152:155], v[20:23]\n"
"ds_read_b64_tr_b16 v[208:209], v200 offset:8200\n"
"ds_read_b64_tr_b16 v[210:211], v200 offset:12296\n"
"s_waitcnt lgkmcnt(15)\n"
"v_mfma_f32_16x16x32_bf16 v[24:27], v[212:215], v[144:147], v[24:27]\n"
"v_mfma_f32_16x16x32_bf16 v[28:31], v[212:215], v[148:151], v[28:31]\n"
"v_mfma_f32_16x16x32_bf16 v[32:35], v[212:215], v[152:155], v[32:35]\n"
"ds_read_b64_tr_b16 v[212:213], v200 offset:8704\n"
"ds_read_b64_tr_b16 v[214:215], v200 offset:12800\n"
"v_mfma_f32_16x16x32_bf16 v[36:39], v[216:219], v[144:147], v[36:39]\n"
"v_mfma_f32_16x16x32_bf16 v[40:43], v[216:219], v[148:151], v[40:43]\n"
"v_mfma_f32_16x16x32_bf16 v[44:47], v[216:219], v[152:155], v[44:47]\n"
"ds_read_b64_tr_b16 v[216:217], v200 offset:8712\n"
"ds_read_b64_tr_b16 v[218:219], v200 offset:12808\n"
"s_waitcnt lgkmcnt(15)\n"
"v_mfma_f32_16x16x32_bf16 v[48:51], v[220:223], v[144:147], v[48:51]\n"
"v_mfma_f32_16x16x32_bf16 v[52:55], v[220:223], v[148:151], v[52:55]\n"
"v_mfma_f32_16x16x32_bf16 v[56:59], v[220:223], v[152:155], v[56:59]\n"
"ds_read_b64_tr_b16 v[220:221], v200 offset:9216\n"
"ds_read_b64_tr_b16 v[222:223], v200 offset:13312\n"
"v_mfma_f32_16x16x32_bf16 v[60:63], v[224:227], v[144:147], v[60:63]\n"
"v_mfma_f32_16x16x32_bf16 v[64:67], v[224:227], v[148:151], v[64:67]\n"
"v_mfma_f32_16x16x32_bf16 v[68:71], v[224:227], v[152:155], v[68:71]\n"
"ds_read_b64_tr_b16 v[224:225], v200 offset:9224\n"
"ds_read_b64_tr_b16 v[226:227], v200 offset:13320\n"
"s_waitcnt lgkmcnt(15)\n"
"v_mfma_f32_16x16x32_bf16 v[72:75], v[228:231], v[144:147], v[72:75]\n"
"v_mfma_f32_16x16x32_bf16 v[76:79], v[228:231], v[148:151], v[76:79]\n"
"v_mfma_f32_16x16x32_bf16 v[80:83], v[228:231], v[152:155], v[80:83]\n"
"ds_read_b64_tr_b16 v[228:229], v200 offset:9728\n"
"ds_read_b64_tr_b16 v[230:231], v200 offset:13824\n"
"v_mfma_f32_16x16x32_bf16 v[84:87], v[232:235], v[144:147], v[84:87]\n"
"v_mfma_f32_16x16x32_bf16 v[88:91], v[232:235], v[148:151], v[88:91]\n"
"v_mfma_f32_16x16x32_bf16 v[92:95], v[232:235], v[152:155], v[92:95]\n"
"ds_read_b64_tr_b16 v[232:233], v200 offset:9736\n"
"ds_read_b64_tr_b16 v[234:235], v200 offset:13832\n"
"s_waitcnt vmcnt(0)\n"
"s_waitcnt lgkmcnt(15)\n"
"s_barrier\n"
"; step -4\n"
"s_waitcnt lgkmcnt(12)\n"
"v_mfma_f32_16x16x32_bf16 v[0:3], v[204:207], v[156:159], v[0:3]\n"
"v_mfma_f32_16x16x32_bf16 v[4:7], v[204:207], v[160:163], v[4:7]\n"
"v_mfma_f32_16x16x32_bf16 v[8:11], v[204:207], v[164:167], v[8:11]\n"
"ds_read_b64_tr_b16 v[204:205], v200 offset:16384\n"
"ds_read_b64_tr_b16 v[206:207], v200 offset:20480\n"
"v_mfma_f32_16x16x32_bf16 v[12:15], v[208:211], v[156:159], v[12:15]\n"
"v_mfma_f32_16x16x32_bf16 v[16:19], v[208:211], v[160:163], v[16:19]\n"
"ds_read_b128 v[144:147], v201 offset:40960\n"
"ds_read_b128 v[148:151], v201 offset:43008\n"
"ds_read_b128 v[152:155], v201 offset:45056\n"
"v_mfma_f32_16x16x32_bf16 v[20:23], v[208:211], v[164:167], v[20:23]\n"
"ds_read_b64_tr_b16 v[208:209], v200 offset:16392\n"
"ds_read_b64_tr_b16 v[210:211], v200 offset:20488\n"
"s_waitcnt lgkmcnt(15)\n"
"v_mfma_f32_16x16x32_bf16 v[24:27], v[212:215], v[156:159], v[24:27]\n"
"v_mfma_f32_16x16x32_bf16 v[28:31], v[212:215], v[160:163], v[28:31]\n"
"v_mfma_f32_16x16x32_bf16 v[32:35], v[212:215], v[164:167], v[32:35]\n"
"ds_read_b64_tr_b16 v[212:213], v200 offset:16896\n"
"ds_read_b64_tr_b16 v[214:215], v200 offset:20992\n"
"v_mfma_f32_16x16x32_bf16 v[36:39], v[216:219], v[156:159], v[36:39]\n"
"v_mfma_f32_16x16x32_bf16 v[40:43], v[216:219], v[160:163], v[40:43]\n"
"v_mfma_f32_16x16x32_bf16 v[44:47], v[216:219], v[164:167], v[44:47]\n"
"ds_read_b64_tr_b16 v[216:217], v200 offset:16904\n"
"ds_read_b64_tr_b16 v[218:219], v200 offset:21000\n"
"s_waitcnt lgkmcnt(15)\n"
"v_mfma_f32_16x16x32_bf16 v[48:51], v[220:223], v[156:159], v[48:51]\n"
"v_mfma_f32_16x16x32_bf16 v[52:55], v[220:223], v[160:163], v[52:55]\n"
"v_mfma_f32_16x16x32_bf16 v[56:59], v[220:223], v[164:167], v[56:59]\n"
"ds_read_b64_tr_b16 v[220:221], v200 offset:17408\n"
"ds_read_b64_tr_b16 v[222:223], v200 offset:21504\n"
"v_mfma_f32_16x16x32_bf16 v[60:63], v[224:227], v[156:159], v[60:63]\n"
"v_mfma_f32_16x16x32_bf16 v[64:67], v[224:227], v[160:163], v[64:67]\n"
"v_mfma_f32_16x16x32_bf16 v[68:71], v[224:227], v[164:167], v[68:71]\n"
"ds_read_b64_tr_b16 v[224:225], v200 offset:17416\n"
"ds_read_b64_tr_b16 v[226:227], v200 offset:21512\n"
"s_waitcnt lgkmcnt(15)\n"
"v_mfma_f32_16x16x32_bf16 v[72:75], v[228:231], v[156:159], v[72:75]\n"
"v_mfma_f32_16x16x32_bf16 v[76:79], v[228:231], v[160:163], v[76:79]\n"
"v_mfma_f32_16x16x32_bf16 v[80:83], v[228:231], v[164:167], v[80:83]\n"
"ds_read_b64_tr_b16 v[228:229], v200 offset:17920\n"
"ds_read_b64_tr_b16 v[230:231], v200 offset:22016\n"
"v_mfma_f32_16x16x32_bf16 v[84:87], v[232:235], v[156:159], v[84:87]\n"
"v_mfma_f32_16x16x32_bf16 v[88:91], v[232:235], v[160:163], v[88:91]\n"
"v_mfma_f32_16x16x32_bf16 v[92:95], v[232:235], v[164:167], v[92:95]\n"
"ds_read_b64_tr_b16 v[232:233], v200 offset:17928\n"
"ds_read_b64_tr_b16 v[234:235], v200 offset:22024\n"
"ds_read_b128 v[156:159], v201 offset:41472\n"
"ds_read_b128 v[160:163], v201 offset:43520\n"
"ds_read_b128 v[164:167], v201 offset:45568\n"
"s_barrier\n"
"; step -3\n"
"s_waitcnt lgkmcnt(15)\n"
"v_mfma_f32_16x16x32_bf16 v[0:3], v[204:207], v[144:147], v[0:3]\n"
"v_mfma_f32_16x16x32_bf16 v[4:7], v[204:207], v[148:151], v[4:7]\n"
"v_mfma_f32_16x16x32_bf16 v[8:11], v[204:207], v[152:155], v[8:11]\n"
"ds_read_b64_tr_b16 v[204:205], v200 offset:32768\n"
"ds_read_b64_tr_b16 v[206:207], v200 offset:36864\n"
"v_mfma_f32_16x16x32_bf16 v[12:15], v[208:211], v[144:147], v[12:15]\n"
"v_mfma_f32_16x16x32_bf16 v[16:19], v[208:211], v[148:151], v[16:19]\n"
"v_mfma_f32_16x16x32_bf16 v[20:23], v[208:211], v[152:155], v[20:23]\n"
"ds_read_b64_tr_b16 v[208:209], v200 offset:32776\n"
"ds_read_b64_tr_b16 v[210:211], v200 offset:36872\n"
"s_waitcnt lgkmcnt(15)\n"
"v_mfma_f32_16x16x32_bf16 v[24:27], v[212:215], v[144:147], v[24:27]\n"
"v_mfma_f32_16x16x32_bf16 v[28:31], v[212:215], v[148:151], v[28:31]\n"
"v_mfma_f32_16x16x32_bf16 v[32:35], v[212:215], v[152:155], v[32:35]\n"
"ds_read_b64_tr_b16 v[212:213], v200 offset:33280\n"
"ds_read_b64_tr_b16 v[214:215], v200 offset:37376\n"
"v_mfma_f32_16x16x32_bf16 v[36:39], v[216:219], v[144:147], v[36:39]\n"
"v_mfma_f32_16x16x32_bf16 v[40:43], v[216:219], v[148:151], v[40:43]\n"
"v_mfma_f32_16x16x32_bf16 v[44:47], v[216:219], v[152:155], v[44:47]\n"
"ds_read_b64_tr_b16 v[216:217], v200 offset:33288\n"
"ds_read_b64_tr_b16 v[218:219], v200 offset:37384\n"
"s_waitcnt lgkmcnt(15)\n"
"v_mfma_f32_16x16x32_bf16 v[48:51], v[220:223], v[144:147], v[48:51]\n"
"v_mfma_f32_16x16x32_bf16 v[52:55], v[220:223], v[148:151], v[52:55]\n"
"v_mfma_f32_16x16x32_bf16 v[56:59], v[220:223], v[152:155], v[56:59]\n"
"ds_read_b64_tr_b16 v[220:221], v200 offset:33792\n"
"ds_read_b64_tr_b16 v[222:223], v200 offset:37888\n"
"v_mfma_f32_16x16x32_bf16 v[60:63], v[224:227], v[144:147], v[60:63]\n"
"v_mfma_f32_16x16x32_bf16 v[64:67], v[224:227], v[148:151], v[64:67]\n"
"v_mfma_f32_16x16x32_bf16 v[68:71], v[224:227], v[152:155], v[68:71]\n"
"ds_read_b64_tr_b16 v[224:225], v200 offset:33800\n"
"ds_read_b64_tr_b16 v[226:227], v200 offset:37896\n"
"s_waitcnt lgkmcnt(15)\n"
"v_mfma_f32_16x16x32_bf16 v[72:75], v[228:231], v[144:147], v[72:75]\n"
"v_mfma_f32_16x16x32_bf16 v[76:79], v[228:231], v[148:151], v[76:79]\n"
"v_mfma_f32_16x16x32_bf16 v[80:83], v[228:231], v[152:155], v[80:83]\n"
"ds_read_b64_tr_b16 v[228:229], v200 offset:34304\n"
"ds_read_b64_tr_b16 v[230:231], v200 offset:38400\n"
"v_mfma_f32_16x16x32_bf16 v[84:87], v[232:235], v[144:147], v[84:87]\n"
"v_mfma_f32_16x16x32_bf16 v[88:91], v[232:235], v[148:151], v[88:91]\n"
"v_mfma_f32_16x16x32_bf16 v[92:95], v[232:235], v[152:155], v[92:95]\n"
"ds_read_b64_tr_b16 v[232:233], v200 offset:34312\n"
"ds_read_b64_tr_b16 v[234:235], v200 offset:38408\n"
"s_waitcnt lgkmcnt(15)\n"
"s_barrier\n"
"; step -2\n"
"s_waitcnt lgkmcnt(12)\n"
"v_mfma_f32_16x16x32_bf16 v[0:3], v[204:207], v[156:159], v[0:3]\n"
"v_mfma_f32_16x16x32_bf16 v[4:7], v[204:207], v[160:163], v[4:7]\n"
"v_mfma_f32_16x16x32_bf16 v[8:11], v[204:207], v[164:167], v[8:11]\n"
"v_mfma_f32_16x16x32_bf16 v[12:15], v[208:211], v[156:159], v[12:15]\n"
"v_mfma_f32_16x16x32_bf16 v[16:19], v[208:211], v[160:163], v[16:19]\n"
"v_mfma_f32_16x16x32_bf16 v[20:23], v[208:211], v[164:167], v[20:23]\n"
"s_waitcnt lgkmcnt(8)\n"
"v_mfma_f32_16x16x32_bf16 v[24:27], v[212:215], v[156:159], v[24:27]\n"
"v_mfma_f32_16x16x32_bf16 v[28:31], v[212:215], v[160:163], v[28:31]\n"
"v_mfma_f32_16x16x32_bf16 v[32:35], v[212:215], v[164:167], v[32:35]\n"
"v_mfma_f32_16x16x32_bf16 v[36:39], v[216:219], v[156:159], v[36:39]\n"
"v_mfma_f32_16x16x32_bf16 v[40:43], v[216:219], v[160:163], v[40:43]\n"
"v_mfma_f32_16x16x32_bf16 v[44:47], v[216:219], v[164:167], v[44:47]\n"
"s_waitcnt lgkmcnt(4)\n"
"v_mfma_f32_16x16x32_bf16 v[48:51], v[220:223], v[156:159], v[48:51]\n"
"v_mfma_f32_16x16x32_bf16 v[52:55], v[220:223], v[160:163], v[52:55]\n"
"v_mfma_f32_16x16x32_bf16 v[56:59], v[220:223], v[164:167], v[56:59]\n"
"v_mfma_f32_16x16x32_bf16 v[60:63], v[224:227], v[156:159], v[60:63]\n"
"v_mfma_f32_16x16x32_bf16 v[64:67], v[224:227], v[160:163], v[64:67]\n"
"v_mfma_f32_16x16x32_bf16 v[68:71], v[224:227], v[164:167], v[68:71]\n"
"s_waitcnt lgkmcnt(0)\n"
"v_mfma_f32_16x16x32_bf16 v[72:75], v[228:231], v[156:159], v[72:75]\n"
"v_mfma_f32_16x16x32_bf16 v[76:79], v[228:231], v[160:163], v[76:79]\n"
"v_mfma_f32_16x16x32_bf16 v[80:83], v[228:231], v[164:167], v[80:83]\n"
"v_mfma_f32_16x16x32_bf16 v[84:87], v[232:235], v[156:159], v[84:87]\n"
"v_mfma_f32_16x16x32_bf16 v[88:91], v[232:235], v[160:163], v[88:91]\n"
"v_mfma_f32_16x16x32_bf16 v[92:95], v[232:235], v[164:167], v[92:95]\n"
"s_barrier\n"
"; step -1\n"
"s_setprio 1\n"
"v_readfirstlane_b32 s80, v244\n"
"v_readfirstlane_b32 s81, v245\n"
"s_add_u32 s80, s80, 1\n"
"s_addc_u32 s81, s81, 0\n"
"s_lshl_b32 s70, s34, 6\n"
"s_and_b32 s70, s70, 64\n"
"v_xor_b32 v255, s70, v255\n"
"v_add_u32 v249, 49152, v249\n"
"v_add_u32 v250, 49152, v250\n"
"s_mov_b32 s68, 5\n"
"s_mov_b32 s75, 8192\n"
"s_mov_b32 s69, 0\n"
"ds_read_b128 v[204:207], v248 offset:32768\n"
"ds_read_b128 v[208:211], v248 offset:36864\n"
"ds_read_b128 v[212:215], v248 offset:33280\n"
"ds_read_b128 v[216:219], v248 offset:37376\n"
"ds_read_b128 v[220:223], v248 offset:33792\n"
"ds_read_b128 v[224:227], v248 offset:37888\n"
"ds_read_b128 v[228:231], v248 offset:34304\n"
"ds_read_b128 v[232:235], v248 offset:38400\n"
"v_mul_f32 v0, s76, v0\n"
"v_mul_f32 v1, s76, v1\n"
"v_mul_f32 v2, s76, v2\n"
"v_mul_f32 v3, s76, v3\n"
"v_mul_f32 v12, s76, v12\n"
"v_mul_f32 v13, s76, v13\n"
"v_mul_f32 v14, s76, v14\n"
"v_mul_f32 v15, s76, v15\n"
"v_cvt_pk_bf16_f32 v96, v0, v1\n"
"v_cvt_pk_bf16_f32 v97, v2, v3\n"
"v_cvt_pk_bf16_f32 v98, v12, v13\n"
"v_cvt_pk_bf16_f32 v99, v14, v15\n"
"v_mul_f32 v4, s76, v4\n"
"v_mul_f32 v5, s76, v5\n"
"v_mul_f32 v6, s76, v6\n"
"v_mul_f32 v7, s76, v7\n"
"v_mul_f32 v16, s76, v16\n"
"v_mul_f32 v17, s76, v17\n"
"v_mul_f32 v18, s76, v18\n"
"v_mul_f32 v19, s76, v19\n"
"v_cvt_pk_bf16_f32 v112, v4, v5\n"
"v_cvt_pk_bf16_f32 v113, v6, v7\n"
"v_cvt_pk_bf16_f32 v114, v16, v17\n"
"v_cvt_pk_bf16_f32 v115, v18, v19\n"
"v_mul_f32 v8, s76, v8\n"
"v_mul_f32 v9, s76, v9\n"
"v_mul_f32 v10, s76, v10\n"
"v_mul_f32 v11, s76, v11\n"
"v_mul_f32 v20, s76, v20\n"
"v_mul_f32 v21, s76, v21\n"
"v_mul_f32 v22, s76, v22\n"
"v_mul_f32 v23, s76, v23\n"
"v_cvt_pk_bf16_f32 v128, v8, v9\n"
"v_cvt_pk_bf16_f32 v129, v10, v11\n"
"v_cvt_pk_bf16_f32 v130, v20, v21\n"
"v_cvt_pk_bf16_f32 v131, v22, v23\n"
"v_mul_f32 v24, s76, v24\n"
"v_mul_f32 v25, s76, v25\n"
"v_mul_f32 v26, s76, v26\n"
"v_mul_f32 v27, s76, v27\n"
"v_mul_f32 v36, s76, v36\n"
"v_mul_f32 v37, s76, v37\n"
"v_mul_f32 v38, s76, v38\n"
"v_mul_f32 v39, s76, v39\n"
"v_cvt_pk_bf16_f32 v100, v24, v25\n"
"v_cvt_pk_bf16_f32 v101, v26, v27\n"
"v_cvt_pk_bf16_f32 v102, v36, v37\n"
"v_cvt_pk_bf16_f32 v103, v38, v39\n"
"v_mul_f32 v28, s76, v28\n"
"v_mul_f32 v29, s76, v29\n"
"v_mul_f32 v30, s76, v30\n"
"v_mul_f32 v31, s76, v31\n"
"v_mul_f32 v40, s76, v40\n"
"v_mul_f32 v41, s76, v41\n"
"v_mul_f32 v42, s76, v42\n"
"v_mul_f32 v43, s76, v43\n"
"v_cvt_pk_bf16_f32 v116, v28, v29\n"
"v_cvt_pk_bf16_f32 v117, v30, v31\n"
"v_cvt_pk_bf16_f32 v118, v40, v41\n"
"v_cvt_pk_bf16_f32 v119, v42, v43\n"
"v_mul_f32 v32, s76, v32\n"
"v_mul_f32 v33, s76, v33\n"
"v_mul_f32 v34, s76, v34\n"
"v_mul_f32 v35, s76, v35\n"
"v_mul_f32 v44, s76, v44\n"
"v_mul_f32 v45, s76, v45\n"
"v_mul_f32 v46, s76, v46\n"
"v_mul_f32 v47, s76, v47\n"
"v_cvt_pk_bf16_f32 v132, v32, v33\n"
"v_cvt_pk_bf16_f32 v133, v34, v35\n"
"v_cvt_pk_bf16_f32 v134, v44, v45\n"
"v_cvt_pk_bf16_f32 v135, v46, v47\n"
"v_mul_f32 v48, s76, v48\n"
"v_mul_f32 v49, s76, v49\n"
"v_mul_f32 v50, s76, v50\n"
"v_mul_f32 v51, s76, v51\n"
"v_mul_f32 v60, s76, v60\n"
"v_mul_f32 v61, s76, v61\n"
"v_mul_f32 v62, s76, v62\n"
"v_mul_f32 v63, s76, v63\n"
"v_cvt_pk_bf16_f32 v104, v48, v49\n"
"v_cvt_pk_bf16_f32 v105, v50, v51\n"
"v_cvt_pk_bf16_f32 v106, v60, v61\n"
"v_cvt_pk_bf16_f32 v107, v62, v63\n"
"v_mul_f32 v52, s76, v52\n"
"v_mul_f32 v53, s76, v53\n"
"v_mul_f32 v54, s76, v54\n"
"v_mul_f32 v55, s76, v55\n"
"v_mul_f32 v64, s76, v64\n"
"v_mul_f32 v65, s76, v65\n"
"v_mul_f32 v66, s76, v66\n"
"v_mul_f32 v67, s76, v67\n"
"v_cvt_pk_bf16_f32 v120, v52, v53\n"
"v_cvt_pk_bf16_f32 v121, v54, v55\n"
"v_cvt_pk_bf16_f32 v122, v64, v65\n"
"v_cvt_pk_bf16_f32 v123, v66, v67\n"
"v_mul_f32 v56, s76, v56\n"
"v_mul_f32 v57, s76, v57\n"
"v_mul_f32 v58, s76, v58\n"
"v_mul_f32 v59, s76, v59\n"
"v_mul_f32 v68, s76, v68\n"
"v_mul_f32 v69, s76, v69\n"
"v_mul_f32 v70, s76, v70\n"
"v_mul_f32 v71, s76, v71\n"
"v_cvt_pk_bf16_f32 v136, v56, v57\n"
"v_cvt_pk_bf16_f32 v137, v58, v59\n"
"v_cvt_pk_bf16_f32 v138, v68, v69\n"
"v_cvt_pk_bf16_f32 v139, v70, v71\n"
"v_mul_f32 v72, s76, v72\n"
"v_mul_f32 v73, s76, v73\n"
"v_mul_f32 v74, s76, v74\n"
"v_mul_f32 v75, s76, v75\n"
"v_mul_f32 v84, s76, v84\n"
"v_mul_f32 v85, s76, v85\n"
"v_mul_f32 v86, s76, v86\n"
"v_mul_f32 v87, s76, v87\n"
"v_cvt_pk_bf16_f32 v108, v72, v73\n"
"v_cvt_pk_bf16_f32 v109, v74, v75\n"
"v_cvt_pk_bf16_f32 v110, v84, v85\n"
"v_cvt_pk_bf16_f32 v111, v86, v87\n"
"v_mul_f32 v76, s76, v76\n"
"v_mul_f32 v77, s76, v77\n"
"v_mul_f32 v78, s76, v78\n"
"v_mul_f32 v79, s76, v79\n"
"v_mul_f32 v88, s76, v88\n"
"v_mul_f32 v89, s76, v89\n"
"v_mul_f32 v90, s76, v90\n"
"v_mul_f32 v91, s76, v91\n"
"v_cvt_pk_bf16_f32 v124, v76, v77\n"
"v_cvt_pk_bf16_f32 v125, v78, v79\n"
"v_cvt_pk_bf16_f32 v126, v88, v89\n"
"v_cvt_pk_bf16_f32 v127, v90, v91\n"
"v_mul_f32 v80, s76, v80\n"
"v_mul_f32 v81, s76, v81\n"
"v_mul_f32 v82, s76, v82\n"
"v_mul_f32 v83, s76, v83\n"
"v_mul_f32 v92, s76, v92\n"
"v_mul_f32 v93, s76, v93\n"
"v_mul_f32 v94, s76, v94\n"
"v_mul_f32 v95, s76, v95\n"
"v_cvt_pk_bf16_f32 v140, v80, v81\n"
"v_cvt_pk_bf16_f32 v141, v82, v83\n"
"v_cvt_pk_bf16_f32 v142, v92, v93\n"
"v_cvt_pk_bf16_f32 v143, v94, v95\n"
"s_waitcnt lgkmcnt(6)\n"
"v_mfma_f32_16x16x32_bf16 v[144:147], v[204:207], v[96:99], 0\n"
"v_mfma_f32_16x16x32_bf16 v[148:151], v[204:207], v[112:115], 0\n"
"v_mfma_f32_16x16x32_bf16 v[152:155], v[204:207], v[128:131], 0\n"
"ds_read_b128 v[204:207], v248 offset:40960\n"
"v_mfma_f32_16x16x32_bf16 v[156:159], v[208:211], v[96:99], 0\n"
"v_mfma_f32_16x16x32_bf16 v[160:163], v[208:211], v[112:115], 0\n"
"v_mfma_f32_16x16x32_bf16 v[164:167], v[208:211], v[128:131], 0\n"
"ds_read_b128 v[208:211], v248 offset:45056\n"
"s_waitcnt lgkmcnt(6)\n"
"v_mfma_f32_16x16x32_bf16 v[144:147], v[212:215], v[100:103], v[144:147]\n"
"v_mfma_f32_16x16x32_bf16 v[148:151], v[212:215], v[116:119], v[148:151]\n"
"v_mfma_f32_16x16x32_bf16 v[152:155], v[212:215], v[132:135], v[152:155]\n"
"ds_read_b128 v[212:215], v248 offset:41472\n"
"v_mfma_f32_16x16x32_bf16 v[156:159], v[216:219], v[100:103], v[156:159]\n"
"v_mfma_f32_16x16x32_bf16 v[160:163], v[216:219], v[116:119], v[160:163]\n"
"v_mfma_f32_16x16x32_bf16 v[164:167], v[216:219], v[132:135], v[164:167]\n"
"ds_read_b128 v[216:219], v248 offset:45568\n"
"s_waitcnt lgkmcnt(6)\n"
"v_mfma_f32_16x16x32_bf16 v[144:147], v[220:223], v[104:107], v[144:147]\n"
"v_mfma_f32_16x16x32_bf16 v[148:151], v[220:223], v[120:123], v[148:151]\n"
"v_mfma_f32_16x16x32_bf16 v[152:155], v[220:223], v[136:139], v[152:155]\n"
"ds_read_b128 v[220:223], v248 offset:41984\n"
"v_mfma_f32_16x16x32_bf16 v[156:159], v[224:227], v[104:107], v[156:159]\n"
"v_mfma_f32_16x16x32_bf16 v[160:163], v[224:227], v[120:123], v[160:163]\n"
"v_mfma_f32_16x16x32_bf16 v[164:167], v[224:227], v[136:139], v[164:167]\n"
"ds_read_b128 v[224:227], v248 offset:46080\n"
"s_waitcnt lgkmcnt(6)\n"
"v_mfma_f32_16x16x32_bf16 v[144:147], v[228:231], v[108:111], v[144:147]\n"
"v_mfma_f32_16x16x32_bf16 v[148:151], v[228:231], v[124:127], v[148:151]\n"
"v_mfma_f32_16x16x32_bf16 v[152:155], v[228:231], v[140:143], v[152:155]\n"
"ds_read_b128 v[228:231], v248 offset:42496\n"
"v_mfma_f32_16x16x32_bf16 v[156:159], v[232:235], v[108:111], v[156:159]\n"
"v_mfma_f32_16x16x32_bf16 v[160:163], v[232:235], v[124:127], v[160:163]\n"
"v_mfma_f32_16x16x32_bf16 v[164:167], v[232:235], v[140:143], v[164:167]\n"
"ds_read_b128 v[232:235], v248 offset:46592\n"
"v_mov_b32 v236, 0\n"
"v_mov_b32 v237, 0\n"
"v_mov_b32 v238, 0\n"
"v_max3_f32 v239, v144, v145, v146\n"
"s_nop 0\n"
"v_max3_f32 v242, v147, v156, v157\n"
"v_max3_f32 v239, v239, v158, v159\n"
"v_max3_f32 v240, v148, v149, v150\n"
"v_max3_f32 v243, v151, v160, v161\n"
"v_max3_f32 v240, v240, v162, v163\n"
"v_max3_f32 v241, v152, v153, v154\n"
"v_max3_f32 v244, v155, v164, v165\n"
"v_max3_f32 v241, v241, v166, v167\n"
"v_max_f32 v239, v239, v242\n"
"v_max_f32 v240, v240, v243\n"
"v_max_f32 v241, v241, v244\n"
"v_mov_b32 v242, v239\n"
"v_mov_b32 v243, v240\n"
"v_mov_b32 v244, v241\n"
"v_permlane32_swap_b32 v239, v242\n"
"v_permlane32_swap_b32 v240, v243\n"
"v_permlane32_swap_b32 v241, v244\n"
"v_max_f32 v239, v239, v242\n"
"v_max_f32 v240, v240, v243\n"
"v_max_f32 v241, v241, v244\n"
"v_mov_b32 v242, v239\n"
"v_mov_b32 v243, v240\n"
"v_mov_b32 v244, v241\n"
"v_permlane16_swap_b32 v239, v242\n"
"v_permlane16_swap_b32 v240, v243\n"
"v_permlane16_swap_b32 v241, v244\n"
"v_max_f32 v239, v239, v242\n"
"v_max_f32 v240, v240, v243\n"
"v_max_f32 v241, v241, v244\n"
"v_sub_f32 v144, v144, v239\n"
"v_sub_f32 v145, v145, v239\n"
"v_sub_f32 v146, v146, v239\n"
"v_sub_f32 v147, v147, v239\n"
"v_sub_f32 v156, v156, v239\n"
"v_sub_f32 v157, v157, v239\n"
"v_sub_f32 v158, v158, v239\n"
"v_sub_f32 v159, v159, v239\n"
"v_sub_f32 v192, 0, v239\n"
"v_sub_f32 v193, 0, v239\n"
"v_sub_f32 v194, 0, v239\n"
"v_sub_f32 v195, 0, v239\n"
"v_sub_f32 v148, v148, v240\n"
"v_sub_f32 v149, v149, v240\n"
"v_sub_f32 v150, v150, v240\n"
"v_sub_f32 v151, v151, v240\n"
"v_sub_f32 v160, v160, v240\n"
"v_sub_f32 v161, v161, v240\n"
"v_sub_f32 v162, v162, v240\n"
"v_sub_f32 v163, v163, v240\n"
"v_sub_f32 v196, 0, v240\n"
"v_sub_f32 v197, 0, v240\n"
"v_sub_f32 v198, 0, v240\n"
"v_sub_f32 v199, 0, v240\n"
"v_sub_f32 v152, v152, v241\n"
"v_sub_f32 v153, v153, v241\n"
"v_sub_f32 v154, v154, v241\n"
"v_sub_f32 v155, v155, v241\n"
"v_sub_f32 v164, v164, v241\n"
"v_sub_f32 v165, v165, v241\n"
"v_sub_f32 v166, v166, v241\n"
"v_sub_f32 v167, v167, v241\n"
"v_sub_f32 v200, 0, v241\n"
"v_sub_f32 v201, 0, v241\n"
"v_sub_f32 v202, 0, v241\n"
"v_sub_f32 v203, 0, v241\n"
"v_mov_b32 v244, 1065369472\n"
"v_mov_b32 v245, 1065369472\n"
"v_mov_b32 v246, 1065369472\n"
"v_mov_b32 v247, 1065369472\n"
"v_exp_f32 v144, v144\n"
"v_exp_f32 v145, v145\n"
"v_exp_f32 v146, v146\n"
"v_exp_f32 v147, v147\n"
"v_exp_f32 v156, v156\n"
"v_exp_f32 v157, v157\n"
"v_exp_f32 v158, v158\n"
"v_exp_f32 v159, v159\n"
"v_exp_f32 v148, v148\n"
"v_exp_f32 v149, v149\n"
"v_exp_f32 v150, v150\n"
"v_exp_f32 v151, v151\n"
"v_exp_f32 v160, v160\n"
"v_exp_f32 v161, v161\n"
"v_exp_f32 v162, v162\n"
"v_exp_f32 v163, v163\n"
"s_barrier\n"
"; step 0\n"
"s_waitcnt lgkmcnt(6)\n"
"v_mfma_f32_16x16x32_bf16 v[168:171], v[204:207], v[96:99], v[192:195]\n"
"v_cvt_pk_bf16_f32 v144, v144, v145\n"
"v_cvt_pk_bf16_f32 v145, v146, v147\n"
"v_mfma_f32_16x16x32_bf16 v[172:175], v[204:207], v[112:115], v[196:199]\n"
"v_cvt_pk_bf16_f32 v146, v156, v157\n"
"v_cvt_pk_bf16_f32 v147, v158, v159\n"
"v_mfma_f32_16x16x32_bf16 v[176:179], v[204:207], v[128:131], v[200:203]\n"
"ds_read_b64_tr_b16 v[204:205], v249 offset:0\n"
"ds_read_b64_tr_b16 v[206:207], v249 offset:4096\n"
"v_mfma_f32_16x16x32_bf16 v[180:183], v[208:211], v[96:99], v[192:195]\n"
"v_cvt_pk_bf16_f32 v148, v148, v149\n"
"v_cvt_pk_bf16_f32 v149, v150, v151\n"
"v_mfma_f32_16x16x32_bf16 v[184:187], v[208:211], v[112:115], v[196:199]\n"
"v_cvt_pk_bf16_f32 v150, v160, v161\n"
"v_cvt_pk_bf16_f32 v151, v162, v163\n"
"v_mfma_f32_16x16x32_bf16 v[188:191], v[208:211], v[128:131], v[200:203]\n"
"ds_read_b64_tr_b16 v[208:209], v250 offset:0\n"
"ds_read_b64_tr_b16 v[210:211], v250 offset:4096\n"
"s_waitcnt lgkmcnt(8)\n"
"v_mfma_f32_16x16x32_bf16 v[168:171], v[212:215], v[100:103], v[168:171]\n"
"v_exp_f32 v152, v152\n"
"v_mfma_f32_16x16x32_bf16 v[172:175], v[212:215], v[116:119], v[172:175]\n"
"v_exp_f32 v153, v153\n"
"v_mfma_f32_16x16x32_bf16 v[176:179], v[212:215], v[132:135], v[176:179]\n"
"ds_read_b64_tr_b16 v[212:213], v249 offset:512\n"
"ds_read_b64_tr_b16 v[214:215], v249 offset:4608\n"
"v_mfma_f32_16x16x32_bf16 v[180:183], v[216:219], v[100:103], v[180:183]\n"
"v_exp_f32 v154, v154\n"
"v_mfma_f32_16x16x32_bf16 v[184:187], v[216:219], v[116:119], v[184:187]\n"
"v_exp_f32 v155, v155\n"
"v_mfma_f32_16x16x32_bf16 v[188:191], v[216:219], v[132:135], v[188:191]\n"
"ds_read_b64_tr_b16 v[216:217], v250 offset:512\n"
"ds_read_b64_tr_b16 v[218:219], v250 offset:4608\n"
"s_waitcnt lgkmcnt(10)\n"
"v_mfma_f32_16x16x32_bf16 v[168:171], v[220:223], v[104:107], v[168:171]\n"
"v_exp_f32 v164, v164\n"
"v_mfma_f32_16x16x32_bf16 v[172:175], v[220:223], v[120:123], v[172:175]\n"
"v_exp_f32 v165, v165\n"
"v_mfma_f32_16x16x32_bf16 v[176:179], v[220:223], v[136:139], v[176:179]\n"
"ds_read_b64_tr_b16 v[220:221], v249 offset:1024\n"
"ds_read_b64_tr_b16 v[222:223], v249 offset:5120\n"
"v_mfma_f32_16x16x32_bf16 v[180:183], v[224:227], v[104:107], v[180:183]\n"
"v_exp_f32 v166, v166\n"
"v_mfma_f32_16x16x32_bf16 v[184:187], v[224:227], v[120:123], v[184:187]\n"
"v_exp_f32 v167, v167\n"
"v_mfma_f32_16x16x32_bf16 v[188:191], v[224:227], v[136:139], v[188:191]\n"
"ds_read_b64_tr_b16 v[224:225], v250 offset:1024\n"
"ds_read_b64_tr_b16 v[226:227], v250 offset:5120\n"
"v_cvt_pk_bf16_f32 v152, v152, v153\n"
"v_cvt_pk_bf16_f32 v153, v154, v155\n"
"v_cvt_pk_bf16_f32 v154, v164, v165\n"
"v_cvt_pk_bf16_f32 v155, v166, v167\n"
"v_mfma_f32_16x16x32_bf16 v[156:159], v[244:247], v[144:147], 0\n"
"v_mfma_f32_16x16x32_bf16 v[160:163], v[244:247], v[148:151], 0\n"
"v_mfma_f32_16x16x32_bf16 v[164:167], v[244:247], v[152:155], 0\n"
"s_waitcnt lgkmcnt(12)\n"
"v_mfma_f32_16x16x32_bf16 v[168:171], v[228:231], v[108:111], v[168:171]\n"
"v_mfma_f32_16x16x32_bf16 v[172:175], v[228:231], v[124:127], v[172:175]\n"
"v_mfma_f32_16x16x32_bf16 v[176:179], v[228:231], v[140:143], v[176:179]\n"
"ds_read_b64_tr_b16 v[228:229], v249 offset:1536\n"
"ds_read_b64_tr_b16 v[230:231], v249 offset:5632\n"
"v_mfma_f32_16x16x32_bf16 v[180:183], v[232:235], v[108:111], v[180:183]\n"
"v_mfma_f32_16x16x32_bf16 v[184:187], v[232:235], v[124:127], v[184:187]\n"
"v_mfma_f32_16x16x32_bf16 v[188:191], v[232:235], v[140:143], v[188:191]\n"
"ds_read_b64_tr_b16 v[232:233], v250 offset:1536\n"
"ds_read_b64_tr_b16 v[234:235], v250 offset:5632\n"
"s_waitcnt lgkmcnt(12)\n"
"v_mfma_f32_16x16x32_bf16 v[0:3], v[204:207], v[144:147], 0\n"
"v_exp_f32 v168, v168\n"
"v_mfma_f32_16x16x32_bf16 v[4:7], v[204:207], v[148:151], 0\n"
"v_exp_f32 v169, v169\n"
"v_mfma_f32_16x16x32_bf16 v[8:11], v[204:207], v[152:155], 0\n"
"ds_read_b128 v[204:207], v248 offset:0\n"
"v_add_f32 v236, v236, v156\n"
"v_mfma_f32_16x16x32_bf16 v[12:15], v[208:211], v[144:147], 0\n"
"v_exp_f32 v170, v170\n"
"v_mfma_f32_16x16x32_bf16 v[16:19], v[208:211], v[148:151], 0\n"
"v_exp_f32 v171, v171\n"
"v_mfma_f32_16x16x32_bf16 v[20:23], v[208:211], v[152:155], 0\n"
"ds_read_b128 v[208:211], v248 offset:4096\n"
"v_add_f32 v237, v237, v160\n"
"s_waitcnt lgkmcnt(10)\n"
"v_mfma_f32_16x16x32_bf16 v[24:27], v[212:215], v[144:147], 0\n"
"v_exp_f32 v180, v180\n"
"v_mfma_f32_16x16x32_bf16 v[28:31], v[212:215], v[148:151], 0\n"
"v_exp_f32 v181, v181\n"
"v_mfma_f32_16x16x32_bf16 v[32:35], v[212:215], v[152:155], 0\n"
"ds_read_b128 v[212:215], v248 offset:512\n"
"v_add_f32 v238, v238, v164\n"
"v_mfma_f32_16x16x32_bf16 v[36:39], v[216:219], v[144:147], 0\n"
"v_exp_f32 v182, v182\n"
"v_mfma_f32_16x16x32_bf16 v[40:43], v[216:219], v[148:151], 0\n"
"v_exp_f32 v183, v183\n"
"v_mfma_f32_16x16x32_bf16 v[44:47], v[216:219], v[152:155], 0\n"
"ds_read_b128 v[216:219], v248 offset:4608\n"
"v_cvt_pk_bf16_f32 v168, v168, v169\n"
"s_waitcnt lgkmcnt(8)\n"
"v_mfma_f32_16x16x32_bf16 v[48:51], v[220:223], v[144:147], 0\n"
"v_exp_f32 v172, v172\n"
"v_mfma_f32_16x16x32_bf16 v[52:55], v[220:223], v[148:151], 0\n"
"v_exp_f32 v173, v173\n"
"v_mfma_f32_16x16x32_bf16 v[56:59], v[220:223], v[152:155], 0\n"
"ds_read_b128 v[220:223], v248 offset:1024\n"
"v_cvt_pk_bf16_f32 v169, v170, v171\n"
"v_mfma_f32_16x16x32_bf16 v[60:63], v[224:227], v[144:147], 0\n"
"v_exp_f32 v174, v174\n"
"v_mfma_f32_16x16x32_bf16 v[64:67], v[224:227], v[148:151], 0\n"
"v_exp_f32 v175, v175\n"
"v_mfma_f32_16x16x32_bf16 v[68:71], v[224:227], v[152:155], 0\n"
"ds_read_b128 v[224:227], v248 offset:5120\n"
"v_cvt_pk_bf16_f32 v170, v180, v181\n"
"s_waitcnt lgkmcnt(6)\n"
"v_mfma_f32_16x16x32_bf16 v[72:75], v[228:231], v[144:147], 0\n"
"s_add_u32 s69, s69, 1\n"
"s_sub_u32 s68, s68, 1\n"
"s_cselect_b32 s70, -40960, s75\n"
"s_cselect_b32 s68, 5, s68\n"
"v_exp_f32 v184, v184\n"
"v_mfma_f32_16x16x32_bf16 v[76:79], v[228:231], v[148:151], 0\n"
"v_exp_f32 v185, v185\n"
"v_mfma_f32_16x16x32_bf16 v[80:83], v[228:231], v[152:155], 0\n"
"ds_read_b128 v[228:231], v248 offset:1536\n"
"v_cvt_pk_bf16_f32 v171, v182, v183\n"
"v_mfma_f32_16x16x32_bf16 v[84:87], v[232:235], v[144:147], 0\n"
"v_exp_f32 v186, v186\n"
"v_mfma_f32_16x16x32_bf16 v[88:91], v[232:235], v[148:151], 0\n"
"v_exp_f32 v187, v187\n"
"v_mfma_f32_16x16x32_bf16 v[92:95], v[232:235], v[152:155], 0\n"
"ds_read_b128 v[232:235], v248 offset:5632\n"
"v_add_u32 v249, s70, v249\n"
"v_add_u32 v250, s70, v250\n"
"v_add_u32 v248, s70, v248\n"
"s_barrier\n"
"s_mov_b32 s72, 6\n"
".Lxloop20:\n"
"; step 1\n"
"s_sleep 3\n"
"s_waitcnt lgkmcnt(6)\n"
"v_mfma_f32_16x16x32_bf16 v[144:147], v[204:207], v[96:99], v[192:195]\n"
"v_cvt_pk_bf16_f32 v172, v172, v173\n"
"v_cvt_pk_bf16_f32 v173, v174, v175\n"
"v_mfma_f32_16x16x32_bf16 v[148:151], v[204:207], v[112:115], v[196:199]\n"
"v_cvt_pk_bf16_f32 v174, v184, v185\n"
"v_cvt_pk_bf16_f32 v175, v186, v187\n"
"v_mfma_f32_16x16x32_bf16 v[152:155], v[204:207], v[128:131], v[200:203]\n"
"ds_read_b64_tr_b16 v[204:205], v249 offset:0\n"
"ds_read_b64_tr_b16 v[206:207], v249 offset:4096\n"
"v_mfma_f32_16x16x32_bf16 v[156:159], v[208:211], v[96:99], v[192:195]\n"
"v_exp_f32 v176, v176\n"
"v_mfma_f32_16x16x32_bf16 v[160:163], v[208:211], v[112:115], v[196:199]\n"
"v_exp_f32 v177, v177\n"
"v_mfma_f32_16x16x32_bf16 v[164:167], v[208:211], v[128:131], v[200:203]\n"
"ds_read_b64_tr_b16 v[208:209], v250 offset:0\n"
"ds_read_b64_tr_b16 v[210:211], v250 offset:4096\n"
"s_waitcnt lgkmcnt(8)\n"
"v_mfma_f32_16x16x32_bf16 v[144:147], v[212:215], v[100:103], v[144:147]\n"
"v_exp_f32 v178, v178\n"
"v_mfma_f32_16x16x32_bf16 v[148:151], v[212:215], v[116:119], v[148:151]\n"
"v_exp_f32 v179, v179\n"
"v_mfma_f32_16x16x32_bf16 v[152:155], v[212:215], v[132:135], v[152:155]\n"
"ds_read_b64_tr_b16 v[212:213], v249 offset:512\n"
"ds_read_b64_tr_b16 v[214:215], v249 offset:4608\n"
"v_mfma_f32_16x16x32_bf16 v[156:159], v[216:219], v[100:103], v[156:159]\n"
"v_exp_f32 v188, v188\n"
"v_mfma_f32_16x16x32_bf16 v[160:163], v[216:219], v[116:119], v[160:163]\n"
"v_exp_f32 v189, v189\n"
"v_mfma_f32_16x16x32_bf16 v[164:167], v[216:219], v[132:135], v[164:167]\n"
"ds_read_b64_tr_b16 v[216:217], v250 offset:512\n"
"ds_read_b64_tr_b16 v[218:219], v250 offset:4608\n"
"s_waitcnt lgkmcnt(10)\n"
"v_mfma_f32_16x16x32_bf16 v[144:147], v[220:223], v[104:107], v[144:147]\n"
"v_exp_f32 v190, v190\n"
"v_mfma_f32_16x16x32_bf16 v[148:151], v[220:223], v[120:123], v[148:151]\n"
"v_exp_f32 v191, v191\n"
"v_mfma_f32_16x16x32_bf16 v[152:155], v[220:223], v[136:139], v[152:155]\n"
"ds_read_b64_tr_b16 v[220:221], v249 offset:1024\n"
"ds_read_b64_tr_b16 v[222:223], v249 offset:5120\n"
"v_mfma_f32_16x16x32_bf16 v[156:159], v[224:227], v[104:107], v[156:159]\n"
"v_cvt_pk_bf16_f32 v176, v176, v177\n"
"v_cvt_pk_bf16_f32 v177, v178, v179\n"
"v_mfma_f32_16x16x32_bf16 v[160:163], v[224:227], v[120:123], v[160:163]\n"
"v_cvt_pk_bf16_f32 v178, v188, v189\n"
"v_cvt_pk_bf16_f32 v179, v190, v191\n"
"v_mfma_f32_16x16x32_bf16 v[164:167], v[224:227], v[136:139], v[164:167]\n"
"ds_read_b64_tr_b16 v[224:225], v250 offset:1024\n"
"ds_read_b64_tr_b16 v[226:227], v250 offset:5120\n"
"v_mfma_f32_16x16x32_bf16 v[180:183], v[244:247], v[168:171], 0\n"
"v_mfma_f32_16x16x32_bf16 v[184:187], v[244:247], v[172:175], 0\n"
"v_mfma_f32_16x16x32_bf16 v[188:191], v[244:247], v[176:179], 0\n"
"s_waitcnt lgkmcnt(12)\n"
"v_mfma_f32_16x16x32_bf16 v[144:147], v[228:231], v[108:111], v[144:147]\n"
"v_mfma_f32_16x16x32_bf16 v[148:151], v[228:231], v[124:127], v[148:151]\n"
"v_mfma_f32_16x16x32_bf16 v[152:155], v[228:231], v[140:143], v[152:155]\n"
"ds_read_b64_tr_b16 v[228:229], v249 offset:1536\n"
"ds_read_b64_tr_b16 v[230:231], v249 offset:5632\n"
"v_mfma_f32_16x16x32_bf16 v[156:159], v[232:235], v[108:111], v[156:159]\n"
"s_nop 0\n"
"v_max3_f32 v242, v180, v184, v188\n"
"v_mfma_f32_16x16x32_bf16 v[160:163], v[232:235], v[124:127], v[160:163]\n"
"v_cmp_nge_f32 vcc, 0x46000000, v242\n"
"v_mfma_f32_16x16x32_bf16 v[164:167], v[232:235], v[140:143], v[164:167]\n"
"ds_read_b64_tr_b16 v[232:233], v250 offset:1536\n"
"ds_read_b64_tr_b16 v[234:235], v250 offset:5632\n"
"s_cbranch_vccnz .LrAx20\n"
".LrAbx20:\n"
"s_waitcnt lgkmcnt(12)\n"
"v_mfma_f32_16x16x32_bf16 v[0:3], v[204:207], v[168:171], v[0:3]\n"
"v_exp_f32 v144, v144\n"
"v_mfma_f32_16x16x32_bf16 v[4:7], v[204:207], v[172:175], v[4:7]\n"
"v_exp_f32 v145, v145\n"
"v_mfma_f32_16x16x32_bf16 v[8:11], v[204:207], v[176:179], v[8:11]\n"
"ds_read_b128 v[204:207], v248 offset:0\n"
"v_add_f32 v236, v236, v180\n"
"v_mfma_f32_16x16x32_bf16 v[12:15], v[208:211], v[168:171], v[12:15]\n"
"v_exp_f32 v146, v146\n"
"v_mfma_f32_16x16x32_bf16 v[16:19], v[208:211], v[172:175], v[16:19]\n"
"v_exp_f32 v147, v147\n"
"v_mfma_f32_16x16x32_bf16 v[20:23], v[208:211], v[176:179], v[20:23]\n"
"ds_read_b128 v[208:211], v248 offset:4096\n"
"v_add_f32 v237, v237, v184\n"
"s_waitcnt lgkmcnt(10)\n"
"v_mfma_f32_16x16x32_bf16 v[24:27], v[212:215], v[168:171], v[24:27]\n"
"v_exp_f32 v156, v156\n"
"v_mfma_f32_16x16x32_bf16 v[28:31], v[212:215], v[172:175], v[28:31]\n"
"v_exp_f32 v157, v157\n"
"v_mfma_f32_16x16x32_bf16 v[32:35], v[212:215], v[176:179], v[32:35]\n"
"ds_read_b128 v[212:215], v248 offset:512\n"
"v_add_f32 v238, v238, v188\n"
"v_mfma_f32_16x16x32_bf16 v[36:39], v[216:219], v[168:171], v[36:39]\n"
"v_exp_f32 v158, v158\n"
"v_mfma_f32_16x16x32_bf16 v[40:43], v[216:219], v[172:175], v[40:43]\n"
"v_exp_f32 v159, v159\n"
"v_mfma_f32_16x16x32_bf16 v[44:47], v[216:219], v[176:179], v[44:47]\n"
"ds_read_b128 v[216:219], v248 offset:4608\n"
"v_cvt_pk_bf16_f32 v144, v144, v145\n"
"s_waitcnt lgkmcnt(8)\n"
"v_mfma_f32_16x16x32_bf16 v[48:51], v[220:223], v[168:171], v[48:51]\n"
"v_exp_f32 v148, v148\n"
"v_mfma_f32_16x16x32_bf16 v[52:55], v[220:223], v[172:175], v[52:55]\n"
"v_exp_f32 v149, v149\n"
"v_mfma_f32_16x16x32_bf16 v[56:59], v[220:223], v[176:179], v[56:59]\n"
"ds_read_b128 v[220:223], v248 offset:1024\n"
"v_cvt_pk_bf16_f32 v145, v146, v147\n"
"v_mfma_f32_16x16x32_bf16 v[60:63], v[224:227], v[168:171], v[60:63]\n"
"v_exp_f32 v150, v150\n"
"v_mfma_f32_16x16x32_bf16 v[64:67], v[224:227], v[172:175], v[64:67]\n"
"v_exp_f32 v151, v151\n"
"v_mfma_f32_16x16x32_bf16 v[68:71], v[224:227], v[176:179], v[68:71]\n"
"ds_read_b128 v[224:227], v248 offset:5120\n"
"v_cvt_pk_bf16_f32 v146, v156, v157\n"
"s_waitcnt lgkmcnt(6)\n"
"v_mfma_f32_16x16x32_bf16 v[72:75], v[228:231], v[168:171], v[72:75]\n"
"s_add_u32 s69, s69, 1\n"
"s_sub_u32 s68, s68, 1\n"
"s_cselect_b32 s70, -40960, s75\n"
"s_cselect_b32 s68, 5, s68\n"
"v_exp_f32 v160, v160\n"
"v_mfma_f32_16x16x32_bf16 v[76:79], v[228:231], v[172:175], v[76:79]\n"
"v_exp_f32 v161, v161\n"
"v_mfma_f32_16x16x32_bf16 v[80:83], v[228:231], v[176:179], v[80:83]\n"
"ds_read_b128 v[228:231], v248 offset:1536\n"
"v_cvt_pk_bf16_f32 v147, v158, v159\n"
"v_mfma_f32_16x16x32_bf16 v[84:87], v[232:235], v[168:171], v[84:87]\n"
"v_exp_f32 v162, v162\n"
"v_mfma_f32_16x16x32_bf16 v[88:91], v[232:235], v[172:175], v[88:91]\n"
"v_exp_f32 v163, v163\n"
"v_mfma_f32_16x16x32_bf16 v[92:95], v[232:235], v[176:179], v[92:95]\n"
"ds_read_b128 v[232:235], v248 offset:5632\n"
"v_add_u32 v249, s70, v249\n"
"v_add_u32 v250, s70, v250\n"
"v_add_u32 v248, s70, v248\n"
"s_barrier\n"
"; step 2\n"
"s_sleep 3\n"
"s_waitcnt lgkmcnt(6)\n"
"v_mfma_f32_16x16x32_bf16 v[168:171], v[204:207], v[96:99], v[192:195]\n"
"v_cvt_pk_bf16_f32 v148, v148, v149\n"
"v_cvt_pk_bf16_f32 v149, v150, v151\n"
"v_mfma_f32_16x16x32_bf16 v[172:175], v[204:207], v[112:115], v[196:199]\n"
"v_cvt_pk_bf16_f32 v150, v160, v161\n"
"v_cvt_pk_bf16_f32 v151, v162, v163\n"
"v_mfma_f32_16x16x32_bf16 v[176:179], v[204:207], v[128:131], v[200:203]\n"
"ds_read_b64_tr_b16 v[204:205], v249 offset:0\n"
"ds_read_b64_tr_b16 v[206:207], v249 offset:4096\n"
"v_mfma_f32_16x16x32_bf16 v[180:183], v[208:211], v[96:99], v[192:195]\n"
"v_exp_f32 v152, v152\n"
"v_mfma_f32_16x16x32_bf16 v[184:187], v[208:211], v[112:115], v[196:199]\n"
"v_exp_f32 v153, v153\n"
"v_mfma_f32_16x16x32_bf16 v[188:191], v[208:211], v[128:131], v[200:203]\n"
"ds_read_b64_tr_b16 v[208:209], v250 offset:0\n"
"ds_read_b64_tr_b16 v[210:211], v250 offset:4096\n"
"s_waitcnt lgkmcnt(8)\n"
"v_mfma_f32_16x16x32_bf16 v[168:171], v[212:215], v[100:103], v[168:171]\n"
"v_exp_f32 v154, v154\n"
"v_mfma_f32_16x16x32_bf16 v[172:175], v[212:215], v[116:119], v[172:175]\n"
"v_exp_f32 v155, v155\n"
"v_mfma_f32_16x16x32_bf16 v[176:179], v[212:215], v[132:135], v[176:179]\n"
"ds_read_b64_tr_b16 v[212:213], v249 offset:512\n"
"ds_read_b64_tr_b16 v[214:215], v249 offset:4608\n"
"v_mfma_f32_16x16x32_bf16 v[180:183], v[216:219], v[100:103], v[180:183]\n"
"v_exp_f32 v164, v164\n"
"v_mfma_f32_16x16x32_bf16 v[184:187], v[216:219], v[116:119], v[184:187]\n"
"v_exp_f32 v165, v165\n"
"v_mfma_f32_16x16x32_bf16 v[188:191], v[216:219], v[132:135], v[188:191]\n"
"ds_read_b64_tr_b16 v[216:217], v250 offset:512\n"
"ds_read_b64_tr_b16 v[218:219], v250 offset:4608\n"
"s_waitcnt lgkmcnt(10)\n"
"v_mfma_f32_16x16x32_bf16 v[168:171], v[220:223], v[104:107], v[168:171]\n"
"v_exp_f32 v166, v166\n"
"v_mfma_f32_16x16x32_bf16 v[172:175], v[220:223], v[120:123], v[172:175]\n"
"v_exp_f32 v167, v167\n"
"v_mfma_f32_16x16x32_bf16 v[176:179], v[220:223], v[136:139], v[176:179]\n"
"ds_read_b64_tr_b16 v[220:221], v249 offset:1024\n"
"ds_read_b64_tr_b16 v[222:223], v249 offset:5120\n"
"v_mfma_f32_16x16x32_bf16 v[180:183], v[224:227], v[104:107], v[180:183]\n"
"v_cvt_pk_bf16_f32 v152, v152, v153\n"
"v_cvt_pk_bf16_f32 v153, v154, v155\n"
"v_mfma_f32_16x16x32_bf16 v[184:187], v[224:227], v[120:123], v[184:187]\n"
"v_cvt_pk_bf16_f32 v154, v164, v165\n"
"v_cvt_pk_bf16_f32 v155, v166, v167\n"
"v_mfma_f32_16x16x32_bf16 v[188:191], v[224:227], v[136:139], v[188:191]\n"
"ds_read_b64_tr_b16 v[224:225], v250 offset:1024\n"
"ds_read_b64_tr_b16 v[226:227], v250 offset:5120\n"
"v_mfma_f32_16x16x32_bf16 v[156:159], v[244:247], v[144:147], 0\n"
"v_mfma_f32_16x16x32_bf16 v[160:163], v[244:247], v[148:151], 0\n"
"v_mfma_f32_16x16x32_bf16 v[164:167], v[244:247], v[152:155], 0\n"
"s_waitcnt lgkmcnt(12)\n"
"v_mfma_f32_16x16x32_bf16 v[168:171], v[228:231], v[108:111], v[168:171]\n"
"v_mfma_f32_16x16x32_bf16 v[172:175], v[228:231], v[124:127], v[172:175]\n"
"v_mfma_f32_16x16x32_bf16 v[176:179], v[228:231], v[140:143], v[176:179]\n"
"ds_read_b64_tr_b16 v[228:229], v249 offset:1536\n"
"ds_read_b64_tr_b16 v[230:231], v249 offset:5632\n"
"v_mfma_f32_16x16x32_bf16 v[180:183], v[232:235], v[108:111], v[180:183]\n"
"s_nop 0\n"
"v_max3_f32 v242, v156, v160, v164\n"
"v_mfma_f32_16x16x32_bf16 v[184:187], v[232:235], v[124:127], v[184:187]\n"
"v_cmp_nge_f32 vcc, 0x46000000, v242\n"
"v_mfma_f32_16x16x32_bf16 v[188:191], v[232:235], v[140:143], v[188:191]\n"
"ds_read_b64_tr_b16 v[232:233], v250 offset:1536\n"
"ds_read_b64_tr_b16 v[234:235], v250 offset:5632\n"
"s_cbranch_vccnz .LrAx21\n"
".LrAbx21:\n"
"s_waitcnt lgkmcnt(12)\n"
"v_mfma_f32_16x16x32_bf16 v[0:3], v[204:207], v[144:147], v[0:3]\n"
"v_exp_f32 v168, v168\n"
"v_mfma_f32_16x16x32_bf16 v[4:7], v[204:207], v[148:151], v[4:7]\n"
"v_exp_f32 v169, v169\n"
"v_mfma_f32_16x16x32_bf16 v[8:11], v[204:207], v[152:155], v[8:11]\n"
"ds_read_b128 v[204:207], v248 offset:0\n"
"v_add_f32 v236, v236, v156\n"
"v_mfma_f32_16x16x32_bf16 v[12:15], v[208:211], v[144:147], v[12:15]\n"
"v_exp_f32 v170, v170\n"
"v_mfma_f32_16x16x32_bf16 v[16:19], v[208:211], v[148:151], v[16:19]\n"
"v_exp_f32 v171, v171\n"
"v_mfma_f32_16x16x32_bf16 v[20:23], v[208:211], v[152:155], v[20:23]\n"
"ds_read_b128 v[208:211], v248 offset:4096\n"
"v_add_f32 v237, v237, v160\n"
"s_waitcnt lgkmcnt(10)\n"
"v_mfma_f32_16x16x32_bf16 v[24:27], v[212:215], v[144:147], v[24:27]\n"
"v_exp_f32 v180, v180\n"
"v_mfma_f32_16x16x32_bf16 v[28:31], v[212:215], v[148:151], v[28:31]\n"
"v_exp_f32 v181, v181\n"
"v_mfma_f32_16x16x32_bf16 v[32:35], v[212:215], v[152:155], v[32:35]\n"
"ds_read_b128 v[212:215], v248 offset:512\n"
"v_add_f32 v238, v238, v164\n"
"v_mfma_f32_16x16x32_bf16 v[36:39], v[216:219], v[144:147], v[36:39]\n"
"v_exp_f32 v182, v182\n"
"v_mfma_f32_16x16x32_bf16 v[40:43], v[216:219], v[148:151], v[40:43]\n"
"v_exp_f32 v183, v183\n"
"v_mfma_f32_16x16x32_bf16 v[44:47], v[216:219], v[152:155], v[44:47]\n"
"ds_read_b128 v[216:219], v248 offset:4608\n"
"v_cvt_pk_bf16_f32 v168, v168, v169\n"
"s_waitcnt lgkmcnt(8)\n"
"v_mfma_f32_16x16x32_bf16 v[48:51], v[220:223], v[144:147], v[48:51]\n"
"v_exp_f32 v172, v172\n"
"v_mfma_f32_16x16x32_bf16 v[52:55], v[220:223], v[148:151], v[52:55]\n"
"v_exp_f32 v173, v173\n"
"v_mfma_f32_16x16x32_bf16 v[56:59], v[220:223], v[152:155], v[56:59]\n"
"ds_read_b128 v[220:223], v248 offset:1024\n"
"v_cvt_pk_bf16_f32 v169, v170, v171\n"
"v_mfma_f32_16x16x32_bf16 v[60:63], v[224:227], v[144:147], v[60:63]\n"
"v_exp_f32 v174, v174\n"
"v_mfma_f32_16x16x32_bf16 v[64:67], v[224:227], v[148:151], v[64:67]\n"
"v_exp_f32 v175, v175\n"
"v_mfma_f32_16x16x32_bf16 v[68:71], v[224:227], v[152:155], v[68:71]\n"
"ds_read_b128 v[224:227], v248 offset:5120\n"
"v_cvt_pk_bf16_f32 v170, v180, v181\n"
"s_waitcnt lgkmcnt(6)\n"
"v_mfma_f32_16x16x32_bf16 v[72:75], v[228:231], v[144:147], v[72:75]\n"
"s_add_u32 s69, s69, 1\n"
"s_sub_u32 s68, s68, 1\n"
"s_cselect_b32 s70, -40960, s75\n"
"s_cselect_b32 s68, 5, s68\n"
"v_exp_f32 v184, v184\n"
"v_mfma_f32_16x16x32_bf16 v[76:79], v[228:231], v[148:151], v[76:79]\n"
"v_exp_f32 v185, v185\n"
"v_mfma_f32_16x16x32_bf16 v[80:83], v[228:231], v[152:155], v[80:83]\n"
"ds_read_b128 v[228:231], v248 offset:1536\n"
"v_cvt_pk_bf16_f32 v171, v182, v183\n"
"v_mfma_f32_16x16x32_bf16 v[84:87], v[232:235], v[144:147], v[84:87]\n"
"v_exp_f32 v186, v186\n"
"v_mfma_f32_16x16x32_bf16 v[88:91], v[232:235], v[148:151], v[88:91]\n"
"v_exp_f32 v187, v187\n"
"v_mfma_f32_16x16x32_bf16 v[92:95], v[232:235], v[152:155], v[92:95]\n"
"ds_read_b128 v[232:235], v248 offset:5632\n"
"v_add_u32 v249, s70, v249\n"
"v_add_u32 v250, s70, v250\n"
"v_add_u32 v248, s70, v248\n"
"s_barrier\n"
"s_sub_u32 s72, s72, 1\n"
"s_cmp_lg_u32 s72, 0\n"
"s_cbranch_scc1 .Lxloop20\n"
"; step 13\n"
"s_sleep 3\n"
"s_waitcnt lgkmcnt(6)\n"
"v_mfma_f32_16x16x32_bf16 v[144:147], v[204:207], v[96:99], v[192:195]\n"
"v_cvt_pk_bf16_f32 v172, v172, v173\n"
"v_cvt_pk_bf16_f32 v173, v174, v175\n"
"v_mfma_f32_16x16x32_bf16 v[148:151], v[204:207], v[112:115], v[196:199]\n"
"v_cvt_pk_bf16_f32 v174, v184, v185\n"
"v_cvt_pk_bf16_f32 v175, v186, v187\n"
"v_mfma_f32_16x16x32_bf16 v[152:155], v[204:207], v[128:131], v[200:203]\n"
"ds_read_b64_tr_b16 v[204:205], v249 offset:0\n"
"ds_read_b64_tr_b16 v[206:207], v249 offset:4096\n"
"v_mfma_f32_16x16x32_bf16 v[156:159], v[208:211], v[96:99], v[192:195]\n"
"v_exp_f32 v176, v176\n"
"v_mfma_f32_16x16x32_bf16 v[160:163], v[208:211], v[112:115], v[196:199]\n"
"v_exp_f32 v177, v177\n"
"v_mfma_f32_16x16x32_bf16 v[164:167], v[208:211], v[128:131], v[200:203]\n"
"ds_read_b64_tr_b16 v[208:209], v250 offset:0\n"
"ds_read_b64_tr_b16 v[210:211], v250 offset:4096\n"
"s_waitcnt lgkmcnt(8)\n"
"v_mfma_f32_16x16x32_bf16 v[144:147], v[212:215], v[100:103], v[144:147]\n"
"v_exp_f32 v178, v178\n"
"v_mfma_f32_16x16x32_bf16 v[148:151], v[212:215], v[116:119], v[148:151]\n"
"v_exp_f32 v179, v179\n"
"v_mfma_f32_16x16x32_bf16 v[152:155], v[212:215], v[132:135], v[152:155]\n"
"ds_read_b64_tr_b16 v[212:213], v249 offset:512\n"
"ds_read_b64_tr_b16 v[214:215], v249 offset:4608\n"
"v_mfma_f32_16x16x32_bf16 v[156:159], v[216:219], v[100:103], v[156:159]\n"
"v_exp_f32 v188, v188\n"
"v_mfma_f32_16x16x32_bf16 v[160:163], v[216:219], v[116:119], v[160:163]\n"
"v_exp_f32 v189, v189\n"
"v_mfma_f32_16x16x32_bf16 v[164:167], v[216:219], v[132:135], v[164:167]\n"
"ds_read_b64_tr_b16 v[216:217], v250 offset:512\n"
"ds_read_b64_tr_b16 v[218:219], v250 offset:4608\n"
"s_waitcnt lgkmcnt(10)\n"
"v_mfma_f32_16x16x32_bf16 v[144:147], v[220:223], v[104:107], v[144:147]\n"
"v_exp_f32 v190, v190\n"
"v_mfma_f32_16x16x32_bf16 v[148:151], v[220:223], v[120:123], v[148:151]\n"
"v_exp_f32 v191, v191\n"
"v_mfma_f32_16x16x32_bf16 v[152:155], v[220:223], v[136:139], v[152:155]\n"
"ds_read_b64_tr_b16 v[220:221], v249 offset:1024\n"
"ds_read_b64_tr_b16 v[222:223], v249 offset:5120\n"
"v_mfma_f32_16x16x32_bf16 v[156:159], v[224:227], v[104:107], v[156:159]\n"
"v_cvt_pk_bf16_f32 v176, v176, v177\n"
"v_cvt_pk_bf16_f32 v177, v178, v179\n"
"v_mfma_f32_16x16x32_bf16 v[160:163], v[224:227], v[120:123], v[160:163]\n"
"v_cvt_pk_bf16_f32 v178, v188, v189\n"
"v_cvt_pk_bf16_f32 v179, v190, v191\n"
"v_mfma_f32_16x16x32_bf16 v[164:167], v[224:227], v[136:139], v[164:167]\n"
"ds_read_b64_tr_b16 v[224:225], v250 offset:1024\n"
"ds_read_b64_tr_b16 v[226:227], v250 offset:5120\n"
"v_mfma_f32_16x16x32_bf16 v[180:183], v[244:247], v[168:171], 0\n"
"v_mfma_f32_16x16x32_bf16 v[184:187], v[244:247], v[172:175], 0\n"
"v_mfma_f32_16x16x32_bf16 v[188:191], v[244:247], v[176:179], 0\n"
"s_waitcnt lgkmcnt(12)\n"
"v_mfma_f32_16x16x32_bf16 v[144:147], v[228:231], v[108:111], v[144:147]\n"
"v_mfma_f32_16x16x32_bf16 v[148:151], v[228:231], v[124:127], v[148:151]\n"
"v_mfma_f32_16x16x32_bf16 v[152:155], v[228:231], v[140:143], v[152:155]\n"
"ds_read_b64_tr_b16 v[228:229], v249 offset:1536\n"
"ds_read_b64_tr_b16 v[230:231], v249 offset:5632\n"
"v_mfma_f32_16x16x32_bf16 v[156:159], v[232:235], v[108:111], v[156:159]\n"
"s_nop 0\n"
"v_max3_f32 v242, v180, v184, v188\n"
"v_mfma_f32_16x16x32_bf16 v[160:163], v[232:235], v[124:127], v[160:163]\n"
"v_cmp_nge_f32 vcc, 0x46000000, v242\n"
"v_mfma_f32_16x16x32_bf16 v[164:167], v[232:235], v[140:143], v[164:167]\n"
"ds_read_b64_tr_b16 v[232:233], v250 offset:1536\n"
"ds_read_b64_tr_b16 v[234:235], v250 offset:5632\n"
"s_cbranch_vccnz .LrAx22\n"
".LrAbx22:\n"
"s_waitcnt lgkmcnt(12)\n"
"v_mfma_f32_16x16x32_bf16 v[0:3], v[204:207], v[168:171], v[0:3]\n"
"v_exp_f32 v144, v144\n"
"v_mfma_f32_16x16x32_bf16 v[4:7], v[204:207], v[172:175], v[4:7]\n"
"v_exp_f32 v145, v145\n"
"v_mfma_f32_16x16x32_bf16 v[8:11], v[204:207], v[176:179], v[8:11]\n"
"ds_read_b128 v[204:207], v248 offset:0\n"
"v_add_f32 v236, v236, v180\n"
"v_mfma_f32_16x16x32_bf16 v[12:15], v[208:211], v[168:171], v[12:15]\n"
"v_exp_f32 v146, v146\n"
"v_mfma_f32_16x16x32_bf16 v[16:19], v[208:211], v[172:175], v[16:19]\n"
"v_exp_f32 v147, v147\n"
"v_mfma_f32_16x16x32_bf16 v[20:23], v[208:211], v[176:179], v[20:23]\n"
"ds_read_b128 v[208:211], v248 offset:4096\n"
"v_add_f32 v237, v237, v184\n"
"s_waitcnt lgkmcnt(10)\n"
"v_mfma_f32_16x16x32_bf16 v[24:27], v[212:215], v[168:171], v[24:27]\n"
"v_exp_f32 v156, v156\n"
"v_mfma_f32_16x16x32_bf16 v[28:31], v[212:215], v[172:175], v[28:31]\n"
"v_exp_f32 v157, v157\n"
"v_mfma_f32_16x16x32_bf16 v[32:35], v[212:215], v[176:179], v[32:35]\n"
"ds_read_b128 v[212:215], v248 offset:512\n"
"v_add_f32 v238, v238, v188\n"
"v_mfma_f32_16x16x32_bf16 v[36:39], v[216:219], v[168:171], v[36:39]\n"
"v_exp_f32 v158, v158\n"
"v_mfma_f32_16x16x32_bf16 v[40:43], v[216:219], v[172:175], v[40:43]\n"
"v_exp_f32 v159, v159\n"
"v_mfma_f32_16x16x32_bf16 v[44:47], v[216:219], v[176:179], v[44:47]\n"
"ds_read_b128 v[216:219], v248 offset:4608\n"
"v_cvt_pk_bf16_f32 v144, v144, v145\n"
"s_waitcnt lgkmcnt(8)\n"
"v_mfma_f32_16x16x32_bf16 v[48:51], v[220:223], v[168:171], v[48:51]\n"
"v_exp_f32 v148, v148\n"
"v_mfma_f32_16x16x32_bf16 v[52:55], v[220:223], v[172:175], v[52:55]\n"
"v_exp_f32 v149, v149\n"
"v_mfma_f32_16x16x32_bf16 v[56:59], v[220:223], v[176:179], v[56:59]\n"
"ds_read_b128 v[220:223], v248 offset:1024\n"
"v_cvt_pk_bf16_f32 v145, v146, v147\n"
"v_mfma_f32_16x16x32_bf16 v[60:63], v[224:227], v[168:171], v[60:63]\n"
"v_exp_f32 v150, v150\n"
"v_mfma_f32_16x16x32_bf16 v[64:67], v[224:227], v[172:175], v[64:67]\n"
"v_exp_f32 v151, v151\n"
"v_mfma_f32_16x16x32_bf16 v[68:71], v[224:227], v[176:179], v[68:71]\n"
"ds_read_b128 v[224:227], v248 offset:5120\n"
"v_cvt_pk_bf16_f32 v146, v156, v157\n"
"s_waitcnt lgkmcnt(6)\n"
"v_mfma_f32_16x16x32_bf16 v[72:75], v[228:231], v[168:171], v[72:75]\n"
"s_add_u32 s69, s69, 1\n"
"s_sub_u32 s68, s68, 1\n"
"s_cselect_b32 s70, -40960, s75\n"
"s_cselect_b32 s68, 5, s68\n"
"v_exp_f32 v160, v160\n"
"v_mfma_f32_16x16x32_bf16 v[76:79], v[228:231], v[172:175], v[76:79]\n"
"v_exp_f32 v161, v161\n"
"v_mfma_f32_16x16x32_bf16 v[80:83], v[228:231], v[176:179], v[80:83]\n"
"ds_read_b128 v[228:231], v248 offset:1536\n"
"v_cvt_pk_bf16_f32 v147, v158, v159\n"
"v_mfma_f32_16x16x32_bf16 v[84:87], v[232:235], v[168:171], v[84:87]\n"
"v_exp_f32 v162, v162\n"
"v_mfma_f32_16x16x32_bf16 v[88:91], v[232:235], v[172:175], v[88:91]\n"
"v_exp_f32 v163, v163\n"
"v_mfma_f32_16x16x32_bf16 v[92:95], v[232:235], v[176:179], v[92:95]\n"
"ds_read_b128 v[232:235], v248 offset:5632\n"
"v_add_u32 v249, s70, v249\n"
"v_add_u32 v250, s70, v250\n"
"v_add_u32 v248, s70, v248\n"
"s_barrier\n"
"; step 14\n"
"s_sleep 3\n"
"s_waitcnt lgkmcnt(6)\n"
"v_mfma_f32_16x16x32_bf16 v[168:171], v[204:207], v[96:99], v[192:195]\n"
"v_cvt_pk_bf16_f32 v148, v148, v149\n"
"v_cvt_pk_bf16_f32 v149, v150, v151\n"
"v_mfma_f32_16x16x32_bf16 v[172:175], v[204:207], v[112:115], v[196:199]\n"
"v_cvt_pk_bf16_f32 v150, v160, v161\n"
"v_cvt_pk_bf16_f32 v151, v162, v163\n"
"v_mfma_f32_16x16x32_bf16 v[176:179], v[204:207], v[128:131], v[200:203]\n"
"ds_read_b64_tr_b16 v[204:205], v249 offset:0\n"
"ds_read_b64_tr_b16 v[206:207], v249 offset:4096\n"
"v_mfma_f32_16x16x32_bf16 v[180:183], v[208:211], v[96:99], v[192:195]\n"
"v_exp_f32 v152, v152\n"
"v_mfma_f32_16x16x32_bf16 v[184:187], v[208:211], v[112:115], v[196:199]\n"
"v_exp_f32 v153, v153\n"
"v_mfma_f32_16x16x32_bf16 v[188:191], v[208:211], v[128:131], v[200:203]\n"
"ds_read_b64_tr_b16 v[208:209], v250 offset:0\n"
"ds_read_b64_tr_b16 v[210:211], v250 offset:4096\n"
"s_waitcnt lgkmcnt(8)\n"
"v_mfma_f32_16x16x32_bf16 v[168:171], v[212:215], v[100:103], v[168:171]\n"
"v_exp_f32 v154, v154\n"
"v_mfma_f32_16x16x32_bf16 v[172:175], v[212:215], v[116:119], v[172:175]\n"
"v_exp_f32 v155, v155\n"
"v_mfma_f32_16x16x32_bf16 v[176:179], v[212:215], v[132:135], v[176:179]\n"
"ds_read_b64_tr_b16 v[212:213], v249 offset:512\n"
"ds_read_b64_tr_b16 v[214:215], v249 offset:4608\n"
"v_mfma_f32_16x16x32_bf16 v[180:183], v[216:219], v[100:103], v[180:183]\n"
"v_exp_f32 v164, v164\n"
"v_mfma_f32_16x16x32_bf16 v[184:187], v[216:219], v[116:119], v[184:187]\n"
"v_exp_f32 v165, v165\n"
"v_mfma_f32_16x16x32_bf16 v[188:191], v[216:219], v[132:135], v[188:191]\n"
"ds_read_b64_tr_b16 v[216:217], v250 offset:512\n"
"ds_read_b64_tr_b16 v[218:219], v250 offset:4608\n"
"s_waitcnt lgkmcnt(10)\n"
"v_mfma_f32_16x16x32_bf16 v[168:171], v[220:223], v[104:107], v[168:171]\n"
"v_exp_f32 v166, v166\n"
"v_mfma_f32_16x16x32_bf16 v[172:175], v[220:223], v[120:123], v[172:175]\n"
"v_exp_f32 v167, v167\n"
"v_mfma_f32_16x16x32_bf16 v[176:179], v[220:223], v[136:139], v[176:179]\n"
"ds_read_b64_tr_b16 v[220:221], v249 offset:1024\n"
"ds_read_b64_tr_b16 v[222:223], v249 offset:5120\n"
"v_mfma_f32_16x16x32_bf16 v[180:183], v[224:227], v[104:107], v[180:183]\n"
"v_cvt_pk_bf16_f32 v152, v152, v153\n"
"v_cvt_pk_bf16_f32 v153, v154, v155\n"
"v_mfma_f32_16x16x32_bf16 v[184:187], v[224:227], v[120:123], v[184:187]\n"
"v_cvt_pk_bf16_f32 v154, v164, v165\n"
"v_cvt_pk_bf16_f32 v155, v166, v167\n"
"v_mfma_f32_16x16x32_bf16 v[188:191], v[224:227], v[136:139], v[188:191]\n"
"ds_read_b64_tr_b16 v[224:225], v250 offset:1024\n"
"ds_read_b64_tr_b16 v[226:227], v250 offset:5120\n"
"v_mfma_f32_16x16x32_bf16 v[156:159], v[244:247], v[144:147], 0\n"
"v_mfma_f32_16x16x32_bf16 v[160:163], v[244:247], v[148:151], 0\n"
"v_mfma_f32_16x16x32_bf16 v[164:167], v[244:247], v[152:155], 0\n"
"s_waitcnt lgkmcnt(12)\n"
"v_mfma_f32_16x16x32_bf16 v[168:171], v[228:231], v[108:111], v[168:171]\n"
"v_mfma_f32_16x16x32_bf16 v[172:175], v[228:231], v[124:127], v[172:175]\n"
"v_mfma_f32_16x16x32_bf16 v[176:179], v[228:231], v[140:143], v[176:179]\n"
"ds_read_b64_tr_b16 v[228:229], v249 offset:1536\n"
"ds_read_b64_tr_b16 v[230:231], v249 offset:5632\n"
"v_mfma_f32_16x16x32_bf16 v[180:183], v[232:235], v[108:111], v[180:183]\n"
"s_nop 0\n"
"v_max3_f32 v242, v156, v160, v164\n"
"v_mfma_f32_16x16x32_bf16 v[184:187], v[232:235], v[124:127], v[184:187]\n"
"v_cmp_nge_f32 vcc, 0x46000000, v242\n"
"v_mfma_f32_16x16x32_bf16 v[188:191], v[232:235], v[140:143], v[188:191]\n"
"ds_read_b64_tr_b16 v[232:233], v250 offset:1536\n"
"ds_read_b64_tr_b16 v[234:235], v250 offset:5632\n"
"s_cbranch_vccnz .LrAx23\n"
".LrAbx23:\n"
"s_waitcnt lgkmcnt(12)\n"
"v_mfma_f32_16x16x32_bf16 v[0:3], v[204:207], v[144:147], v[0:3]\n"
"v_exp_f32 v168, v168\n"
"v_mfma_f32_16x16x32_bf16 v[4:7], v[204:207], v[148:151], v[4:7]\n"
"v_exp_f32 v169, v169\n"
"v_mfma_f32_16x16x32_bf16 v[8:11], v[204:207], v[152:155], v[8:11]\n"
"v_add_f32 v236, v236, v156\n"
"v_mfma_f32_16x16x32_bf16 v[12:15], v[208:211], v[144:147], v[12:15]\n"
"v_exp_f32 v170, v170\n"
"v_mfma_f32_16x16x32_bf16 v[16:19], v[208:211], v[148:151], v[16:19]\n"
"v_exp_f32 v171, v171\n"
"v_mfma_f32_16x16x32_bf16 v[20:23], v[208:211], v[152:155], v[20:23]\n"
"v_add_f32 v237, v237, v160\n"
"s_waitcnt lgkmcnt(8)\n"
"v_mfma_f32_16x16x32_bf16 v[24:27], v[212:215], v[144:147], v[24:27]\n"
"v_exp_f32 v180, v180\n"
"v_mfma_f32_16x16x32_bf16 v[28:31], v[212:215], v[148:151], v[28:31]\n"
"v_exp_f32 v181, v181\n"
"v_mfma_f32_16x16x32_bf16 v[32:35], v[212:215], v[152:155], v[32:35]\n"
"v_add_f32 v238, v238, v164\n"
"v_mfma_f32_16x16x32_bf16 v[36:39], v[216:219], v[144:147], v[36:39]\n"
"v_exp_f32 v182, v182\n"
"v_mfma_f32_16x16x32_bf16 v[40:43], v[216:219], v[148:151], v[40:43]\n"
"v_exp_f32 v183, v183\n"
"v_mfma_f32_16x16x32_bf16 v[44:47], v[216:219], v[152:155], v[44:47]\n"
"v_cvt_pk_bf16_f32 v168, v168, v169\n"
"s_waitcnt lgkmcnt(4)\n"
"v_mfma_f32_16x16x32_bf16 v[48:51], v[220:223], v[144:147], v[48:51]\n"
"v_exp_f32 v172, v172\n"
"v_mfma_f32_16x16x32_bf16 v[52:55], v[220:223], v[148:151], v[52:55]\n"
"v_exp_f32 v173, v173\n"
"v_mfma_f32_16x16x32_bf16 v[56:59], v[220:223], v[152:155], v[56:59]\n"
"v_cvt_pk_bf16_f32 v169, v170, v171\n"
"v_mfma_f32_16x16x32_bf16 v[60:63], v[224:227], v[144:147], v[60:63]\n"
"v_exp_f32 v174, v174\n"
"v_mfma_f32_16x16x32_bf16 v[64:67], v[224:227], v[148:151], v[64:67]\n"
"v_exp_f32 v175, v175\n"
"v_mfma_f32_16x16x32_bf16 v[68:71], v[224:227], v[152:155], v[68:71]\n"
"v_cvt_pk_bf16_f32 v170, v180, v181\n"
"s_waitcnt lgkmcnt(0)\n"
"v_mfma_f32_16x16x32_bf16 v[72:75], v[228:231], v[144:147], v[72:75]\n"
"s_add_u32 s69, s69, 1\n"
"s_sub_u32 s68, s68, 1\n"
"s_cselect_b32 s70, -40960, s75\n"
"s_cselect_b32 s68, 5, s68\n"
"v_exp_f32 v184, v184\n"
"v_mfma_f32_16x16x32_bf16 v[76:79], v[228:231], v[148:151], v[76:79]\n"
"v_exp_f32 v185, v185\n"
"v_mfma_f32_16x16x32_bf16 v[80:83], v[228:231], v[152:155], v[80:83]\n"
"v_cvt_pk_bf16_f32 v171, v182, v183\n"
"v_mfma_f32_16x16x32_bf16 v[84:87], v[232:235], v[144:147], v[84:87]\n"
"v_exp_f32 v186, v186\n"
"v_mfma_f32_16x16x32_bf16 v[88:91], v[232:235], v[148:151], v[88:91]\n"
"v_exp_f32 v187, v187\n"
"v_mfma_f32_16x16x32_bf16 v[92:95], v[232:235], v[152:155], v[92:95]\n"
"v_add_u32 v249, s70, v249\n"
"v_add_u32 v250, s70, v250\n"
"v_add_u32 v248, s70, v248\n"
"s_barrier\n"
"; step 15\n"
"ds_read_b64_tr_b16 v[204:205], v249 offset:0\n"
"ds_read_b64_tr_b16 v[206:207], v249 offset:4096\n"
"ds_read_b64_tr_b16 v[208:209], v250 offset:0\n"
"ds_read_b64_tr_b16 v[210:211], v250 offset:4096\n"
"ds_read_b64_tr_b16 v[212:213], v249 offset:512\n"
"ds_read_b64_tr_b16 v[214:215], v249 offset:4608\n"
"ds_read_b64_tr_b16 v[216:217], v250 offset:512\n"
"ds_read_b64_tr_b16 v[218:219], v250 offset:4608\n"
"ds_read_b64_tr_b16 v[220:221], v249 offset:1024\n"
"ds_read_b64_tr_b16 v[222:223], v249 offset:5120\n"
"ds_read_b64_tr_b16 v[224:225], v250 offset:1024\n"
"ds_read_b64_tr_b16 v[226:227], v250 offset:5120\n"
"ds_read_b64_tr_b16 v[228:229], v249 offset:1536\n"
"ds_read_b64_tr_b16 v[230:231], v249 offset:5632\n"
"ds_read_b64_tr_b16 v[232:233], v250 offset:1536\n"
"ds_read_b64_tr_b16 v[234:235], v250 offset:5632\n"
"v_exp_f32 v176, v176\n"
"v_cvt_pk_bf16_f32 v172, v172, v173\n"
"v_exp_f32 v177, v177\n"
"v_cvt_pk_bf16_f32 v173, v174, v175\n"
"v_exp_f32 v178, v178\n"
"v_cvt_pk_bf16_f32 v174, v184, v185\n"
"v_exp_f32 v179, v179\n"
"v_cvt_pk_bf16_f32 v175, v186, v187\n"
"v_exp_f32 v188, v188\n"
"v_exp_f32 v189, v189\n"
"v_exp_f32 v190, v190\n"
"v_exp_f32 v191, v191\n"
"v_cvt_pk_bf16_f32 v176, v176, v177\n"
"v_cvt_pk_bf16_f32 v177, v178, v179\n"
"v_cvt_pk_bf16_f32 v178, v188, v189\n"
"v_cvt_pk_bf16_f32 v179, v190, v191\n"
"v_mfma_f32_16x16x32_bf16 v[180:183], v[244:247], v[168:171], 0\n"
"v_mfma_f32_16x16x32_bf16 v[184:187], v[244:247], v[172:175], 0\n"
"v_mfma_f32_16x16x32_bf16 v[188:191], v[244:247], v[176:179], 0\n"
"s_nop 7\n"
"v_max3_f32 v242, v180, v184, v188\n"
"v_cmp_nge_f32 vcc, 0x46000000, v242\n"
"s_cbranch_vccnz .LrAx24\n"
".LrAbx24:\n"
"v_add_f32 v236, v236, v180\n"
"v_add_f32 v237, v237, v184\n"
"v_add_f32 v238, v238, v188\n"
"s_waitcnt lgkmcnt(14)\n"
"v_mfma_f32_16x16x32_bf16 v[0:3], v[204:207], v[168:171], v[0:3]\n"
"s_waitcnt lgkmcnt(12)\n"
"v_mfma_f32_16x16x32_bf16 v[12:15], v[208:211], v[168:171], v[12:15]\n"
"v_rcp_f32 v239, v236\n"
"s_waitcnt lgkmcnt(10)\n"
"v_mfma_f32_16x16x32_bf16 v[24:27], v[212:215], v[168:171], v[24:27]\n"
"s_waitcnt lgkmcnt(8)\n"
"v_mfma_f32_16x16x32_bf16 v[36:39], v[216:219], v[168:171], v[36:39]\n"
"v_rcp_f32 v240, v237\n"
"s_waitcnt lgkmcnt(6)\n"
"v_mfma_f32_16x16x32_bf16 v[48:51], v[220:223], v[168:171], v[48:51]\n"
"s_waitcnt lgkmcnt(4)\n"
"v_mfma_f32_16x16x32_bf16 v[60:63], v[224:227], v[168:171], v[60:63]\n"
"s_waitcnt lgkmcnt(2)\n"
"v_mfma_f32_16x16x32_bf16 v[72:75], v[228:231], v[168:171], v[72:75]\n"
"v_rcp_f32 v241, v238\n"
"s_waitcnt lgkmcnt(0)\n"
"v_mfma_f32_16x16x32_bf16 v[84:87], v[232:235], v[168:171], v[84:87]\n"
"v_mfma_f32_16x16x32_bf16 v[4:7], v[204:207], v[172:175], v[4:7]\n"
"v_mfma_f32_16x16x32_bf16 v[16:19], v[208:211], v[172:175], v[16:19]\n"
"v_mul_f32 v0, v239, v0\n"
"v_mul_f32 v1, v239, v1\n"
"v_mul_f32 v2, v239, v2\n"
"v_mfma_f32_16x16x32_bf16 v[28:31], v[212:215], v[172:175], v[28:31]\n"
"v_mul_f32 v3, v239, v3\n"
"v_mul_f32 v12, v239, v12\n"
"v_mfma_f32_16x16x32_bf16 v[40:43], v[216:219], v[172:175], v[40:43]\n"
"v_mul_f32 v13, v239, v13\n"
"v_mul_f32 v14, v239, v14\n"
"v_mfma_f32_16x16x32_bf16 v[52:55], v[220:223], v[172:175], v[52:55]\n"
"v_mul_f32 v15, v239, v15\n"
"v_cvt_pk_bf16_f32 v96, v0, v1\n"
"v_mfma_f32_16x16x32_bf16 v[64:67], v[224:227], v[172:175], v[64:67]\n"
"v_cvt_pk_bf16_f32 v97, v2, v3\n"
"v_cvt_pk_bf16_f32 v98, v12, v13\n"
"v_cvt_pk_bf16_f32 v99, v14, v15\n"
"v_mfma_f32_16x16x32_bf16 v[76:79], v[228:231], v[172:175], v[76:79]\n"
"v_mul_f32 v24, v239, v24\n"
"v_mul_f32 v25, v239, v25\n"
"v_mfma_f32_16x16x32_bf16 v[88:91], v[232:235], v[172:175], v[88:91]\n"
"v_mul_f32 v26, v239, v26\n"
"v_mul_f32 v27, v239, v27\n"
"v_mfma_f32_16x16x32_bf16 v[8:11], v[204:207], v[176:179], v[8:11]\n"
"v_mul_f32 v36, v239, v36\n"
"v_mul_f32 v37, v239, v37\n"
"v_mfma_f32_16x16x32_bf16 v[20:23], v[208:211], v[176:179], v[20:23]\n"
"v_mul_f32 v38, v239, v38\n"
"v_mul_f32 v39, v239, v39\n"
"v_mfma_f32_16x16x32_bf16 v[32:35], v[212:215], v[176:179], v[32:35]\n"
"v_cvt_pk_bf16_f32 v100, v24, v25\n"
"v_cvt_pk_bf16_f32 v101, v26, v27\n"
"v_mfma_f32_16x16x32_bf16 v[44:47], v[216:219], v[176:179], v[44:47]\n"
"v_cvt_pk_bf16_f32 v102, v36, v37\n"
"v_cvt_pk_bf16_f32 v103, v38, v39\n"
"v_mfma_f32_16x16x32_bf16 v[56:59], v[220:223], v[176:179], v[56:59]\n"
"v_mul_f32 v48, v239, v48\n"
"v_mul_f32 v49, v239, v49\n"
"v_mfma_f32_16x16x32_bf16 v[68:71], v[224:227], v[176:179], v[68:71]\n"
"v_mul_f32 v50, v239, v50\n"
"v_mul_f32 v51, v239, v51\n"
"v_mfma_f32_16x16x32_bf16 v[80:83], v[228:231], v[176:179], v[80:83]\n"
"v_mul_f32 v60, v239, v60\n"
"v_mul_f32 v61, v239, v61\n"
"v_mfma_f32_16x16x32_bf16 v[92:95], v[232:235], v[176:179], v[92:95]\n"
"v_mul_f32 v62, v239, v62\n"
"v_mul_f32 v63, v239, v63\n"
"v_cvt_pk_bf16_f32 v104, v48, v49\n"
"v_cvt_pk_bf16_f32 v105, v50, v51\n"
"v_cvt_pk_bf16_f32 v106, v60, v61\n"
"v_cvt_pk_bf16_f32 v107, v62, v63\n"
"v_mul_f32 v72, v239, v72\n"
"v_mul_f32 v73, v239, v73\n"
"v_mul_f32 v74, v239, v74\n"
"v_mul_f32 v75, v239, v75\n"
"v_mul_f32 v84, v239, v84\n"
"v_mul_f32 v85, v239, v85\n"
"v_mul_f32 v86, v239, v86\n"
"v_mul_f32 v87, v239, v87\n"
"v_cvt_pk_bf16_f32 v108, v72, v73\n"
"v_cvt_pk_bf16_f32 v109, v74, v75\n"
"v_cvt_pk_bf16_f32 v110, v84, v85\n"
"v_cvt_pk_bf16_f32 v111, v86, v87\n"
"v_mul_f32 v4, v240, v4\n"
"v_mul_f32 v5, v240, v5\n"
"v_mul_f32 v6, v240, v6\n"
"v_mul_f32 v7, v240, v7\n"
"v_mul_f32 v16, v240, v16\n"
"v_mul_f32 v17, v240, v17\n"
"v_mul_f32 v18, v240, v18\n"
"v_mul_f32 v19, v240, v19\n"
"v_cvt_pk_bf16_f32 v112, v4, v5\n"
"v_cvt_pk_bf16_f32 v113, v6, v7\n"
"v_cvt_pk_bf16_f32 v114, v16, v17\n"
"v_cvt_pk_bf16_f32 v115, v18, v19\n"
"v_mul_f32 v28, v240, v28\n"
"v_mul_f32 v29, v240, v29\n"
"v_mul_f32 v30, v240, v30\n"
"v_mul_f32 v31, v240, v31\n"
"v_mul_f32 v40, v240, v40\n"
"v_mul_f32 v41, v240, v41\n"
"v_mul_f32 v42, v240, v42\n"
"v_mul_f32 v43, v240, v43\n"
"v_cvt_pk_bf16_f32 v116, v28, v29\n"
"v_cvt_pk_bf16_f32 v117, v30, v31\n"
"v_cvt_pk_bf16_f32 v118, v40, v41\n"
"v_cvt_pk_bf16_f32 v119, v42, v43\n"
"v_mul_f32 v52, v240, v52\n"
"v_mul_f32 v53, v240, v53\n"
"v_mul_f32 v54, v240, v54\n"
"v_mul_f32 v55, v240, v55\n"
"v_mul_f32 v64, v240, v64\n"
"v_mul_f32 v65, v240, v65\n"
"v_mul_f32 v66, v240, v66\n"
"v_mul_f32 v67, v240, v67\n"
"v_cvt_pk_bf16_f32 v120, v52, v53\n"
"v_cvt_pk_bf16_f32 v121, v54, v55\n"
"v_cvt_pk_bf16_f32 v122, v64, v65\n"
"v_cvt_pk_bf16_f32 v123, v66, v67\n"
"v_mul_f32 v76, v240, v76\n"
"v_mul_f32 v77, v240, v77\n"
"v_mul_f32 v78, v240, v78\n"
"v_mul_f32 v79, v240, v79\n"
"s_barrier\n"
"; step 16\n"
"s_setprio 0\n"
"v_mul_f32 v88, v240, v88\n"
"v_mul_f32 v89, v240, v89\n"
"v_mul_f32 v90, v240, v90\n"
"v_mul_f32 v91, v240, v91\n"
"v_cvt_pk_bf16_f32 v124, v76, v77\n"
"v_cvt_pk_bf16_f32 v125, v78, v79\n"
"v_cvt_pk_bf16_f32 v126, v88, v89\n"
"v_cvt_pk_bf16_f32 v127, v90, v91\n"
"v_mul_f32 v8, v241, v8\n"
"v_mul_f32 v9, v241, v9\n"
"v_mul_f32 v10, v241, v10\n"
"v_mul_f32 v11, v241, v11\n"
"v_mul_f32 v20, v241, v20\n"
"v_mul_f32 v21, v241, v21\n"
"v_mul_f32 v22, v241, v22\n"
"v_mul_f32 v23, v241, v23\n"
"v_cvt_pk_bf16_f32 v128, v8, v9\n"
"v_cvt_pk_bf16_f32 v129, v10, v11\n"
"s_add_u32 m0, s61, 73728\n"
"s_add_u32 s71, s77, 172032\n"
"buffer_load_dwordx4 v254, s[24:27], s71 offen nt lds\n"
"s_add_u32 m0, s61, 74624\n"
"s_nop 0\n"
"buffer_load_dwordx4 v254, s[24:27], s71 offen offset:128 nt lds\n"
"s_add_u32 m0, s61, 16384\n"
"s_add_u32 s71, s77, 180224\n"
"buffer_load_dwordx4 v254, s[20:23], s71 offen nt lds\n"
"s_add_u32 m0, s61, 17280\n"
"s_nop 0\n"
"buffer_load_dwordx4 v254, s[20:23], s71 offen offset:128 nt lds\n"
"s_barrier\n"
"; step 17\n"
"v_cvt_pk_bf16_f32 v130, v20, v21\n"
"v_cvt_pk_bf16_f32 v131, v22, v23\n"
"v_mul_f32 v32, v241, v32\n"
"v_mul_f32 v33, v241, v33\n"
"v_mul_f32 v34, v241, v34\n"
"v_mul_f32 v35, v241, v35\n"
"v_mul_f32 v44, v241, v44\n"
"v_mul_f32 v45, v241, v45\n"
"v_mul_f32 v46, v241, v46\n"
"v_mul_f32 v47, v241, v47\n"
"v_cvt_pk_bf16_f32 v132, v32, v33\n"
"v_cvt_pk_bf16_f32 v133, v34, v35\n"
"v_cvt_pk_bf16_f32 v134, v44, v45\n"
"v_cvt_pk_bf16_f32 v135, v46, v47\n"
"v_mul_f32 v56, v241, v56\n"
"v_mul_f32 v57, v241, v57\n"
"v_mul_f32 v58, v241, v58\n"
"v_mul_f32 v59, v241, v59\n"
"v_mul_f32 v68, v241, v68\n"
"s_add_u32 m0, s61, 81920\n"
"s_add_u32 s71, s77, 180224\n"
"buffer_load_dwordx4 v254, s[24:27], s71 offen nt lds\n"
"s_add_u32 m0, s61, 82816\n"
"s_nop 0\n"
"buffer_load_dwordx4 v254, s[24:27], s71 offen offset:128 nt lds\n"
"s_add_u32 m0, s61, 24576\n"
"s_add_u32 s71, s77, 188416\n"
"buffer_load_dwordx4 v254, s[20:23], s71 offen nt lds\n"
"s_add_u32 m0, s61, 25472\n"
"s_nop 0\n"
"buffer_load_dwordx4 v254, s[20:23], s71 offen offset:128 nt lds\n"
"s_add_u32 s74, s73, 0\n"
"buffer_store_dwordx4 v[96:99], v253, s[44:47], s74 offen sc1\n"
"s_add_u32 s74, s73, 0\n"
"buffer_store_dwordx4 v[100:103], v253, s[44:47], s74 offen offset:1024 sc1\n"
"s_barrier\n"
"; step 18\n"
"v_mul_f32 v69, v241, v69\n"
"v_mul_f32 v70, v241, v70\n"
"v_mul_f32 v71, v241, v71\n"
"v_cvt_pk_bf16_f32 v136, v56, v57\n"
"v_cvt_pk_bf16_f32 v137, v58, v59\n"
"v_cvt_pk_bf16_f32 v138, v68, v69\n"
"v_cvt_pk_bf16_f32 v139, v70, v71\n"
"v_mul_f32 v80, v241, v80\n"
"v_mul_f32 v81, v241, v81\n"
"v_mul_f32 v82, v241, v82\n"
"v_mul_f32 v83, v241, v83\n"
"v_mul_f32 v92, v241, v92\n"
"v_mul_f32 v93, v241, v93\n"
"v_mul_f32 v94, v241, v94\n"
"v_mul_f32 v95, v241, v95\n"
"v_cvt_pk_bf16_f32 v140, v80, v81\n"
"v_cvt_pk_bf16_f32 v141, v82, v83\n"
"v_cvt_pk_bf16_f32 v142, v92, v93\n"
"v_cvt_pk_bf16_f32 v143, v94, v95\n"
"s_add_u32 m0, s61, 90112\n"
"s_add_u32 s71, s77, 188416\n"
"buffer_load_dwordx4 v254, s[24:27], s71 offen nt lds\n"
"s_add_u32 m0, s61, 91008\n"
"s_nop 0\n"
"buffer_load_dwordx4 v254, s[24:27], s71 offen offset:128 nt lds\n"
"s_add_u32 m0, s61, 32768\n"
"s_add_u32 s71, s77, 196608\n"
"buffer_load_dwordx4 v254, s[20:23], s71 offen nt lds\n"
"s_add_u32 m0, s61, 33664\n"
"s_nop 0\n"
"buffer_load_dwordx4 v254, s[20:23], s71 offen offset:128 nt lds\n"
"s_add_u32 s74, s73, 0\n"
"buffer_store_dwordx4 v[104:107], v253, s[44:47], s74 offen offset:2048 sc1\n"
"s_add_u32 s74, s73, 0\n"
"buffer_store_dwordx4 v[108:111], v253, s[44:47], s74 offen offset:3072 sc1\n"
"s_barrier\n"
"; step 19\n"
"s_add_u32 m0, s61, 49152\n"
"s_add_u32 s71, s77, 196608\n"
"buffer_load_dwordx4 v254, s[24:27], s71 offen nt lds\n"
"s_add_u32 m0, s61, 50048\n"
"s_nop 0\n"
"buffer_load_dwordx4 v254, s[24:27], s71 offen offset:128 nt lds\n"
"s_add_u32 m0, s61, 40960\n"
"s_add_u32 s71, s77, 204800\n"
"buffer_load_dwordx4 v254, s[20:23], s71 offen nt lds\n"
"s_add_u32 m0, s61, 41856\n"
"s_nop 0\n"
"buffer_load_dwordx4 v254, s[20:23], s71 offen offset:128 nt lds\n"
"s_add_u32 s74, s73, 4096\n"
"buffer_store_dwordx4 v[112:115], v253, s[44:47], s74 offen sc1\n"
"s_add_u32 s74, s73, 4096\n"
"buffer_store_dwordx4 v[116:119], v253, s[44:47], s74 offen offset:1024 sc1\n"
"s_waitcnt vmcnt(18)\n"
"s_barrier\n"
"; step 20\n"
"s_add_u32 m0, s61, 57344\n"
"s_add_u32 s71, s77, 204800\n"
"buffer_load_dwordx4 v254, s[24:27], s71 offen nt lds\n"
"s_add_u32 m0, s61, 58240\n"
"s_nop 0\n"
"buffer_load_dwordx4 v254, s[24:27], s71 offen offset:128 nt lds\n"
"s_add_u32 m0, s61, 0\n"
"s_add_u32 s71, s77, 212992\n"
"buffer_load_dwordx4 v254, s[20:23], s71 offen nt lds\n"
"s_add_u32 m0, s61, 896\n"
"s_nop 0\n"
"buffer_load_dwordx4 v254, s[20:23], s71 offen offset:128 nt lds\n"
"s_add_u32 s74, s73, 4096\n"
"buffer_store_dwordx4 v[120:123], v253, s[44:47], s74 offen offset:2048 sc1\n"
"s_add_u32 s74, s73, 4096\n"
"buffer_store_dwordx4 v[124:127], v253, s[44:47], s74 offen offset:3072 sc1\n"
"s_waitcnt vmcnt(20)\n"
"s_barrier\n"
"; step 21\n"
"s_add_u32 m0, s61, 65536\n"
"s_add_u32 s71, s77, 212992\n"
"buffer_load_dwordx4 v254, s[24:27], s71 offen nt lds\n"
"s_add_u32 m0, s61, 66432\n"
"s_nop 0\n"
"buffer_load_dwordx4 v254, s[24:27], s71 offen offset:128 nt lds\n"
"s_add_u32 m0, s61, 8192\n"
"s_add_u32 s71, s77, 221184\n"
"buffer_load_dwordx4 v254, s[20:23], s71 offen nt lds\n"
"s_add_u32 m0, s61, 9088\n"
"s_nop 0\n"
"buffer_load_dwordx4 v254, s[20:23], s71 offen offset:128 nt lds\n"
"s_add_u32 s74, s73, 8192\n"
"buffer_store_dwordx4 v[128:131], v253, s[44:47], s74 offen sc1\n"
"s_add_u32 s74, s73, 8192\n"
"buffer_store_dwordx4 v[132:135], v253, s[44:47], s74 offen offset:1024 sc1\n"
"s_waitcnt vmcnt(20)\n"
"s_barrier\n"
"; step 22\n"
"s_add_u32 m0, s61, 73728\n"
"s_add_u32 s71, s77, 221184\n"
"buffer_load_dwordx4 v254, s[24:27], s71 offen nt lds\n"
"s_add_u32 m0, s61, 74624\n"
"s_nop 0\n"
"buffer_load_dwordx4 v254, s[24:27], s71 offen offset:128 nt lds\n"
"s_add_u32 m0, s61, 16384\n"
"s_add_u32 s71, s77, 229376\n"
"buffer_load_dwordx4 v254, s[20:23], s71 offen nt lds\n"
"s_add_u32 m0, s61, 17280\n"
"s_nop 0\n"
"buffer_load_dwordx4 v254, s[20:23], s71 offen offset:128 nt lds\n"
"s_add_u32 s74, s73, 8192\n"
"buffer_store_dwordx4 v[136:139], v253, s[44:47], s74 offen offset:2048 sc1\n"
"s_add_u32 s74, s73, 8192\n"
"buffer_store_dwordx4 v[140:143], v253, s[44:47], s74 offen offset:3072 sc1\n"
"s_waitcnt vmcnt(20)\n"
"s_barrier\n"
"; step 23\n"
"s_add_u32 m0, s61, 81920\n"
"s_add_u32 s71, s77, 229376\n"
"buffer_load_dwordx4 v254, s[24:27], s71 offen nt lds\n"
"s_add_u32 m0, s61, 82816\n"
"s_nop 0\n"
"buffer_load_dwordx4 v254, s[24:27], s71 offen offset:128 nt lds\n"
"s_add_u32 m0, s61, 24576\n"
"s_add_u32 s71, s77, 237568\n"
"buffer_load_dwordx4 v254, s[20:23], s71 offen nt lds\n"
"s_add_u32 m0, s61, 25472\n"
"s_nop 0\n"
"buffer_load_dwordx4 v254, s[20:23], s71 offen offset:128 nt lds\n"
"s_waitcnt vmcnt(18)\n"
"s_barrier\n"
"; step 24\n"
"s_add_u32 m0, s61, 90112\n"
"s_add_u32 s71, s77, 237568\n"
"buffer_load_dwordx4 v254, s[24:27], s71 offen nt lds\n"
"s_add_u32 m0, s61, 91008\n"
"s_nop 0\n"
"buffer_load_dwordx4 v254, s[24:27], s71 offen offset:128 nt lds\n"
"s_add_u32 m0, s61, 32768\n"
"s_add_u32 s71, s77, 245760\n"
"buffer_load_dwordx4 v254, s[20:23], s71 offen nt lds\n"
"s_add_u32 m0, s61, 33664\n"
"s_nop 0\n"
"buffer_load_dwordx4 v254, s[20:23], s71 offen offset:128 nt lds\n"
"s_waitcnt vmcnt(16)\n"
"s_barrier\n"
"; step 25\n"
"s_add_u32 m0, s61, 49152\n"
"s_add_u32 s71, s77, 245760\n"
"buffer_load_dwordx4 v254, s[24:27], s71 offen nt lds\n"
"s_add_u32 m0, s61, 50048\n"
"s_nop 0\n"
"buffer_load_dwordx4 v254, s[24:27], s71 offen offset:128 nt lds\n"
"s_add_u32 m0, s61, 40960\n"
"s_add_u32 s71, s77, 253952\n"
"buffer_load_dwordx4 v254, s[20:23], s71 offen nt lds\n"
"s_add_u32 m0, s61, 41856\n"
"s_nop 0\n"
"buffer_load_dwordx4 v254, s[20:23], s71 offen offset:128 nt lds\n"
"s_lshl_b32 s71, s58, 2\n"
"s_add_u32 s71, s71, s59\n"
"s_lshl_b32 s71, s71, 3\n"
"s_add_u32 s71, s71, s56\n"
"s_lshl_b32 s71, s71, 6\n"
"s_add_u32 s71, s71, 4096\n"
"s_lshl_b32 s70, s60, 3\n"
"s_add_u32 s71, s71, s70\n"
"v_mov_b32 v244, s80\n"
"v_mov_b32 v245, s81\n"
"v_cmp_eq_u32 vcc, 0, v253\n"
"v_mov_b32 v243, 2147483647\n"
"s_nop 0\n"
"v_cndmask_b32 v242, v243, v253, vcc\n"
"s_waitcnt vmcnt(12)\n"
"buffer_store_dwordx2 v[244:245], v242, s[48:51], s71 offen sc1\n"
"s_barrier\n"
"; step 26\n"
"s_add_u32 m0, s61, 57344\n"
"s_add_u32 s71, s77, 253952\n"
"buffer_load_dwordx4 v254, s[24:27], s71 offen nt lds\n"
"s_add_u32 m0, s61, 58240\n"
"s_nop 0\n"
"buffer_load_dwordx4 v254, s[24:27], s71 offen offset:128 nt lds\n"
"s_cmp_le_u32 s60, 1\n"
"s_cselect_b32 s71, 262144, 0\n"
"s_add_u32 s71, s71, 262144\n"
"s_add_u32 m0, s61, 0\n"
"s_add_u32 s71, s71, s33\n"
"buffer_load_dwordx4 v255, s[40:43], s71 offen lds\n"
"s_add_u32 m0, s61, 1024\n"
"s_xor_b32 s71, s71, 128\n"
"buffer_load_dwordx4 v255, s[40:43], s71 offen lds\n"
"s_waitcnt vmcnt(13)\n"
"s_barrier\n"
"; step 27\n"
"s_cmp_le_u32 s60, 1\n"
"s_cselect_b32 s71, 262144, 0\n"
"s_add_u32 s71, s71, 294912\n"
"s_add_u32 m0, s61, 8192\n"
"s_add_u32 s71, s71, s33\n"
"buffer_load_dwordx4 v255, s[40:43], s71 offen lds\n"
"s_add_u32 m0, s61, 9216\n"
"s_xor_b32 s71, s71, 128\n"
"buffer_load_dwordx4 v255, s[40:43], s71 offen lds\n"
"s_cmp_le_u32 s60, 1\n"
"s_cselect_b32 s71, 262144, 0\n"
"s_add_u32 s71, s71, 327680\n"
"s_add_u32 m0, s61, 65536\n"
"s_add_u32 s71, s71, s33\n"
"buffer_load_dwordx4 v255, s[40:43], s71 offen lds\n"
"s_add_u32 m0, s61, 66560\n"
"s_xor_b32 s71, s71, 128\n"
"buffer_load_dwordx4 v255, s[40:43], s71 offen lds\n"
"ds_read_b64_tr_b16 v[204:205], v249 offset:24576\n"
"ds_read_b64_tr_b16 v[206:207], v249 offset:28672\n"
"ds_read_b64_tr_b16 v[208:209], v250 offset:24576\n"
"ds_read_b64_tr_b16 v[210:211], v250 offset:28672\n"
"ds_read_b64_tr_b16 v[212:213], v249 offset:25088\n"
"ds_read_b64_tr_b16 v[214:215], v249 offset:29184\n"
"ds_read_b64_tr_b16 v[216:217], v250 offset:25088\n"
"ds_read_b64_tr_b16 v[218:219], v250 offset:29184\n"
"ds_read_b64_tr_b16 v[220:221], v249 offset:25600\n"
"ds_read_b64_tr_b16 v[222:223], v249 offset:29696\n"
"ds_read_b64_tr_b16 v[224:225], v250 offset:25600\n"
"ds_read_b64_tr_b16 v[226:227], v250 offset:29696\n"
"ds_read_b64_tr_b16 v[228:229], v249 offset:26112\n"
"ds_read_b64_tr_b16 v[230:231], v249 offset:30208\n"
"ds_read_b64_tr_b16 v[232:233], v250 offset:26112\n"
"ds_read_b64_tr_b16 v[234:235], v250 offset:30208\n"
"s_waitcnt vmcnt(13)\n"
"s_barrier\n"
"; step 28\n"
"s_lshl_b32 s62, s58, 2\n"
"s_add_u32 s62, s62, s59\n"
"s_lshl_b32 s62, s62, 3\n"
"s_add_u32 s62, s62, s56\n"
"s_lshl_b32 s62, s62, 6\n"
"s_add_u32 s62, s62, 4096\n"
"v_lshrrev_b32 v242, 1, v253\n"
"v_and_b32 v242, 24, v242\n"
"buffer_load_dwordx2 v[244:245], v242, s[48:51], s62 offen sc1\n"
"s_waitcnt lgkmcnt(12)\n"
"v_mfma_f32_16x16x32_bf16 v[0:3], v[204:207], v[96:99], 0\n"
"v_mfma_f32_16x16x32_bf16 v[4:7], v[204:207], v[112:115], 0\n"
"v_mfma_f32_16x16x32_bf16 v[8:11], v[204:207], v[128:131], 0\n"
"ds_read_b64_tr_b16 v[204:205], v249 offset:32768\n"
"ds_read_b64_tr_b16 v[206:207], v249 offset:36864\n"
"v_mfma_f32_16x16x32_bf16 v[12:15], v[208:211], v[96:99], 0\n"
"v_mfma_f32_16x16x32_bf16 v[16:19], v[208:211], v[112:115], 0\n"
"s_cmp_le_u32 s60, 1\n"
"s_cselect_b32 s71, 262144, 0\n"
"s_add_u32 s71, s71, 360448\n"
"s_add_u32 m0, s61, 16384\n"
"s_add_u32 s71, s71, s33\n"
"buffer_load_dwordx4 v255, s[40:43], s71 offen lds\n"
"s_add_u32 m0, s61, 17408\n"
"s_xor_b32 s71, s71, 128\n"
"buffer_load_dwordx4 v255, s[40:43], s71 offen lds\n"
"v_mfma_f32_16x16x32_bf16 v[20:23], v[208:211], v[128:131], 0\n"
"ds_read_b64_tr_b16 v[208:209], v250 offset:32768\n"
"ds_read_b64_tr_b16 v[210:211], v250 offset:36864\n"
"s_waitcnt lgkmcnt(12)\n"
"v_mfma_f32_16x16x32_bf16 v[24:27], v[212:215], v[96:99], 0\n"
"v_mfma_f32_16x16x32_bf16 v[28:31], v[212:215], v[112:115], 0\n"
"v_mfma_f32_16x16x32_bf16 v[32:35], v[212:215], v[128:131], 0\n"
"ds_read_b64_tr_b16 v[212:213], v249 offset:33280\n"
"ds_read_b64_tr_b16 v[214:215], v249 offset:37376\n"
"v_mfma_f32_16x16x32_bf16 v[36:39], v[216:219], v[96:99], 0\n"
"v_mfma_f32_16x16x32_bf16 v[40:43], v[216:219], v[112:115], 0\n"
"v_mfma_f32_16x16x32_bf16 v[44:47], v[216:219], v[128:131], 0\n"
"ds_read_b64_tr_b16 v[216:217], v250 offset:33280\n"
"ds_read_b64_tr_b16 v[218:219], v250 offset:37376\n"
"s_waitcnt lgkmcnt(12)\n"
"v_mfma_f32_16x16x32_bf16 v[48:51], v[220:223], v[96:99], 0\n"
"v_mfma_f32_16x16x32_bf16 v[52:55], v[220:223], v[112:115], 0\n"
"v_mfma_f32_16x16x32_bf16 v[56:59], v[220:223], v[128:131], 0\n"
"ds_read_b64_tr_b16 v[220:221], v249 offset:33792\n"
"ds_read_b64_tr_b16 v[222:223], v249 offset:37888\n"
"v_mfma_f32_16x16x32_bf16 v[60:63], v[224:227], v[96:99], 0\n"
"v_mfma_f32_16x16x32_bf16 v[64:67], v[224:227], v[112:115], 0\n"
"s_cmp_le_u32 s60, 2\n"
"s_cselect_b32 s71, 262144, 0\n"
"s_add_u32 s71, s71, 524288\n"
"s_add_u32 m0, s61, 73728\n"
"s_add_u32 s71, s71, s33\n"
"buffer_load_dwordx4 v255, s[40:43], s71 offen lds\n"
"s_add_u32 m0, s61, 74752\n"
"s_xor_b32 s71, s71, 128\n"
"buffer_load_dwordx4 v255, s[40:43], s71 offen lds\n"
"v_mfma_f32_16x16x32_bf16 v[68:71], v[224:227], v[128:131], 0\n"
"ds_read_b64_tr_b16 v[224:225], v250 offset:33792\n"
"ds_read_b64_tr_b16 v[226:227], v250 offset:37888\n"
"s_waitcnt lgkmcnt(12)\n"
"v_mfma_f32_16x16x32_bf16 v[72:75], v[228:231], v[96:99], 0\n"
"v_mfma_f32_16x16x32_bf16 v[76:79], v[228:231], v[112:115], 0\n"
"v_mfma_f32_16x16x32_bf16 v[80:83], v[228:231], v[128:131], 0\n"
"ds_read_b64_tr_b16 v[228:229], v249 offset:34304\n"
"ds_read_b64_tr_b16 v[230:231], v249 offset:38400\n"
"v_mfma_f32_16x16x32_bf16 v[84:87], v[232:235], v[96:99], 0\n"
"v_mfma_f32_16x16x32_bf16 v[88:91], v[232:235], v[112:115], 0\n"
"v_mfma_f32_16x16x32_bf16 v[92:95], v[232:235], v[128:131], 0\n"
"ds_read_b64_tr_b16 v[232:233], v250 offset:34304\n"
"ds_read_b64_tr_b16 v[234:235], v250 offset:38400\n"
"s_waitcnt vmcnt(14)\n"
"s_barrier\n"
"; step 29\n"
"s_mov_b32 s74, 262144\n"
"s_waitcnt vmcnt(4)\n"
"v_cmp_ne_u64 vcc, s[80:81], v[244:245]\n"
"s_cbranch_vccz .Lpolld0\n"
".Lpoll0:\n"
"buffer_load_dwordx2 v[244:245], v242, s[48:51], s62 offen sc1\n"
"s_waitcnt vmcnt(0)\n"
"v_cmp_ne_u64 vcc, s[80:81], v[244:245]\n"
"s_cbranch_vccz .Lpolld0\n"
"s_sleep 2\n"
"s_sub_u32 s74, s74, 1\n"
"s_cbranch_scc0 .Lpoll0\n"
".Lpolld0:\n"
"s_waitcnt lgkmcnt(12)\n"
"v_mfma_f32_16x16x32_bf16 v[0:3], v[204:207], v[100:103], v[0:3]\n"
"s_cmp_le_u32 s60, 0\n"
"s_cselect_b32 s82, 1, 0\n"
"s_sub_u32 s82, s82, s60\n"
"s_mul_i32 s82, s82, 98304\n"
"s_add_u32 s82, s82, s73\n"
"buffer_load_dwordx4 v[144:147], v253, s[44:47], s82 offen sc1\n"
"v_mfma_f32_16x16x32_bf16 v[4:7], v[204:207], v[116:119], v[4:7]\n"
"s_add_u32 s71, s82, 4096\n"
"buffer_load_dwordx4 v[160:163], v253, s[44:47], s71 offen sc1\n"
"v_mfma_f32_16x16x32_bf16 v[8:11], v[204:207], v[132:135], v[8:11]\n"
"ds_read_b64_tr_b16 v[204:205], v249 offset:40960\n"
"ds_read_b64_tr_b16 v[206:207], v249 offset:45056\n"
"v_mfma_f32_16x16x32_bf16 v[12:15], v[208:211], v[100:103], v[12:15]\n"
"s_add_u32 s71, s82, 8192\n"
"buffer_load_dwordx4 v[176:179], v253, s[44:47], s71 offen sc1\n"
"v_mfma_f32_16x16x32_bf16 v[16:19], v[208:211], v[116:119], v[16:19]\n"
"v_mfma_f32_16x16x32_bf16 v[20:23], v[208:211], v[132:135], v[20:23]\n"
"ds_read_b64_tr_b16 v[208:209], v250 offset:40960\n"
"ds_read_b64_tr_b16 v[210:211], v250 offset:45056\n"
"s_cmp_le_u32 s60, 0\n"
"s_cselect_b32 s82, 1, 0\n"
"s_sub_u32 s82, s82, s60\n"
"s_mul_i32 s82, s82, 98304\n"
"s_add_u32 s82, s82, s73\n"
"buffer_load_dwordx4 v[148:151], v253, s[44:47], s82 offen offset:1024 sc1\n"
"s_waitcnt lgkmcnt(12)\n"
"v_mfma_f32_16x16x32_bf16 v[24:27], v[212:215], v[100:103], v[24:27]\n"
"v_mfma_f32_16x16x32_bf16 v[28:31], v[212:215], v[116:119], v[28:31]\n"
"s_add_u32 s71, s82, 4096\n"
"buffer_load_dwordx4 v[164:167], v253, s[44:47], s71 offen offset:1024 sc1\n"
"v_mfma_f32_16x16x32_bf16 v[32:35], v[212:215], v[132:135], v[32:35]\n"
"ds_read_b64_tr_b16 v[212:213], v249 offset:41472\n"
"ds_read_b64_tr_b16 v[214:215], v249 offset:45568\n"
"v_mfma_f32_16x16x32_bf16 v[36:39], v[216:219], v[100:103], v[36:39]\n"
"s_add_u32 s71, s82, 8192\n"
"buffer_load_dwordx4 v[180:183], v253, s[44:47], s71 offen offset:1024 sc1\n"
"v_mfma_f32_16x16x32_bf16 v[40:43], v[216:219], v[116:119], v[40:43]\n"
"s_cmp_le_u32 s60, 0\n"
"s_cselect_b32 s82, 1, 0\n"
"s_sub_u32 s82, s82, s60\n"
"s_mul_i32 s82, s82, 98304\n"
"s_add_u32 s82, s82, s73\n"
"buffer_load_dwordx4 v[152:155], v253, s[44:47], s82 offen offset:2048 sc1\n"
"v_mfma_f32_16x16x32_bf16 v[44:47], v[216:219], v[132:135], v[44:47]\n"
"ds_read_b64_tr_b16 v[216:217], v250 offset:41472\n"
"ds_read_b64_tr_b16 v[218:219], v250 offset:45568\n"
"s_waitcnt lgkmcnt(12)\n"
"v_mfma_f32_16x16x32_bf16 v[48:51], v[220:223], v[100:103], v[48:51]\n"
"s_add_u32 s71, s82, 4096\n"
"buffer_load_dwordx4 v[168:171], v253, s[44:47], s71 offen offset:2048 sc1\n"
"v_mfma_f32_16x16x32_bf16 v[52:55], v[220:223], v[116:119], v[52:55]\n"
"s_add_u32 s71, s82, 8192\n"
"buffer_load_dwordx4 v[184:187], v253, s[44:47], s71 offen offset:2048 sc1\n"
"v_mfma_f32_16x16x32_bf16 v[56:59], v[220:223], v[132:135], v[56:59]\n"
"ds_read_b64_tr_b16 v[220:221], v249 offset:41984\n"
"ds_read_b64_tr_b16 v[222:223], v249 offset:46080\n"
"v_mfma_f32_16x16x32_bf16 v[60:63], v[224:227], v[100:103], v[60:63]\n"
"v_mfma_f32_16x16x32_bf16 v[64:67], v[224:227], v[116:119], v[64:67]\n"
"s_cmp_le_u32 s60, 2\n"
"s_cselect_b32 s71, 262144, 0\n"
"s_add_u32 s71, s71, 557056\n"
"s_add_u32 m0, s61, 24576\n"
"s_add_u32 s71, s71, s33\n"
"buffer_load_dwordx4 v255, s[40:43], s71 offen lds\n"
"s_add_u32 m0, s61, 25600\n"
"s_xor_b32 s71, s71, 128\n"
"buffer_load_dwordx4 v255, s[40:43], s71 offen lds\n"
"v_mfma_f32_16x16x32_bf16 v[68:71], v[224:227], v[132:135], v[68:71]\n"
"ds_read_b64_tr_b16 v[224:225], v250 offset:41984\n"
"ds_read_b64_tr_b16 v[226:227], v250 offset:46080\n"
"s_waitcnt lgkmcnt(12)\n"
"v_mfma_f32_16x16x32_bf16 v[72:75], v[228:231], v[100:103], v[72:75]\n"
"v_mfma_f32_16x16x32_bf16 v[76:79], v[228:231], v[116:119], v[76:79]\n"
"v_mfma_f32_16x16x32_bf16 v[80:83], v[228:231], v[132:135], v[80:83]\n"
"ds_read_b64_tr_b16 v[228:229], v249 offset:42496\n"
"ds_read_b64_tr_b16 v[230:231], v249 offset:46592\n"
"v_mfma_f32_16x16x32_bf16 v[84:87], v[232:235], v[100:103], v[84:87]\n"
"s_cmp_le_u32 s60, 2\n"
"s_cselect_b32 s71, 262144, 0\n"
"s_add_u32 s71, s71, 589824\n"
"s_add_u32 m0, s61, 81920\n"
"s_add_u32 s71, s71, s33\n"
"buffer_load_dwordx4 v255, s[40:43], s71 offen lds\n"
"s_add_u32 m0, s61, 82944\n"
"s_xor_b32 s71, s71, 128\n"
"buffer_load_dwordx4 v255, s[40:43], s71 offen lds\n"
"v_mfma_f32_16x16x32_bf16 v[88:91], v[232:235], v[116:119], v[88:91]\n"
"v_mfma_f32_16x16x32_bf16 v[92:95], v[232:235], v[132:135], v[92:95]\n"
"ds_read_b64_tr_b16 v[232:233], v250 offset:42496\n"
"ds_read_b64_tr_b16 v[234:235], v250 offset:46592\n"
"s_barrier\n"
"; step 30\n"
"s_waitcnt lgkmcnt(12)\n"
"v_mfma_f32_16x16x32_bf16 v[0:3], v[204:207], v[104:107], v[0:3]\n"
"s_cmp_le_u32 s60, 0\n"
"s_cselect_b32 s82, 1, 0\n"
"s_sub_u32 s82, s82, s60\n"
"s_mul_i32 s82, s82, 98304\n"
"s_add_u32 s82, s82, s73\n"
"buffer_load_dwordx4 v[156:159], v253, s[44:47], s82 offen offset:3072 sc1\n"
"v_mfma_f32_16x16x32_bf16 v[4:7], v[204:207], v[120:123], v[4:7]\n"
"s_add_u32 s71, s82, 4096\n"
"buffer_load_dwordx4 v[172:175], v253, s[44:47], s71 offen offset:3072 sc1\n"
"v_mfma_f32_16x16x32_bf16 v[8:11], v[204:207], v[136:139], v[8:11]\n"
"ds_read_b64_tr_b16 v[204:205], v249 offset:49152\n"
"ds_read_b64_tr_b16 v[206:207], v249 offset:53248\n"
"v_mfma_f32_16x16x32_bf16 v[12:15], v[208:211], v[104:107], v[12:15]\n"
"s_add_u32 s71, s82, 8192\n"
"buffer_load_dwordx4 v[188:191], v253, s[44:47], s71 offen offset:3072 sc1\n"
"v_mfma_f32_16x16x32_bf16 v[16:19], v[208:211], v[120:123], v[16:19]\n"
"v_mfma_f32_16x16x32_bf16 v[20:23], v[208:211], v[136:139], v[20:23]\n"
"ds_read_b64_tr_b16 v[208:209], v250 offset:49152\n"
"ds_read_b64_tr_b16 v[210:211], v250 offset:53248\n"
"s_cmp_le_u32 s60, 1\n"
"s_cselect_b32 s82, 2, 1\n"
"s_sub_u32 s82, s82, s60\n"
"s_mul_i32 s82, s82, 98304\n"
"s_add_u32 s82, s82, s73\n"
"buffer_load_dwordx4 v[96:99], v253, s[44:47], s82 offen sc1\n"
"s_waitcnt lgkmcnt(12)\n"
"v_mfma_f32_16x16x32_bf16 v[24:27], v[212:215], v[104:107], v[24:27]\n"
"v_mfma_f32_16x16x32_bf16 v[28:31], v[212:215], v[120:123], v[28:31]\n"
"s_add_u32 s71, s82, 4096\n"
"buffer_load_dwordx4 v[112:115], v253, s[44:47], s71 offen sc1\n"
"v_mfma_f32_16x16x32_bf16 v[32:35], v[212:215], v[136:139], v[32:35]\n"
"ds_read_b64_tr_b16 v[212:213], v249 offset:49664\n"
"ds_read_b64_tr_b16 v[214:215], v249 offset:53760\n"
"v_mfma_f32_16x16x32_bf16 v[36:39], v[216:219], v[104:107], v[36:39]\n"
"s_add_u32 s71, s82, 8192\n"
"buffer_load_dwordx4 v[128:131], v253, s[44:47], s71 offen sc1\n"
"v_mfma_f32_16x16x32_bf16 v[40:43], v[216:219], v[120:123], v[40:43]\n"
"s_cmp_le_u32 s60, 1\n"
"s_cselect_b32 s82, 2, 1\n"
"s_sub_u32 s82, s82, s60\n"
"s_mul_i32 s82, s82, 98304\n"
"s_add_u32 s82, s82, s73\n"
"buffer_load_dwordx4 v[100:103], v253, s[44:47], s82 offen offset:1024 sc1\n"
"v_mfma_f32_16x16x32_bf16 v[44:47], v[216:219], v[136:139], v[44:47]\n"
"ds_read_b64_tr_b16 v[216:217], v250 offset:49664\n"
"ds_read_b64_tr_b16 v[218:219], v250 offset:53760\n"
"s_waitcnt lgkmcnt(12)\n"
"v_mfma_f32_16x16x32_bf16 v[48:51], v[220:223], v[104:107], v[48:51]\n"
"s_add_u32 s71, s82, 4096\n"
"buffer_load_dwordx4 v[116:119], v253, s[44:47], s71 offen offset:1024 sc1\n"
"v_mfma_f32_16x16x32_bf16 v[52:55], v[220:223], v[120:123], v[52:55]\n"
"s_add_u32 s71, s82, 8192\n"
"buffer_load_dwordx4 v[132:135], v253, s[44:47], s71 offen offset:1024 sc1\n"
"v_mfma_f32_16x16x32_bf16 v[56:59], v[220:223], v[136:139], v[56:59]\n"
"ds_read_b64_tr_b16 v[220:221], v249 offset:50176\n"
"ds_read_b64_tr_b16 v[222:223], v249 offset:54272\n"
"v_mfma_f32_16x16x32_bf16 v[60:63], v[224:227], v[104:107], v[60:63]\n"
"v_mfma_f32_16x16x32_bf16 v[64:67], v[224:227], v[120:123], v[64:67]\n"
"s_cmp_le_u32 s60, 2\n"
"s_cselect_b32 s71, 262144, 0\n"
"s_add_u32 s71, s71, 622592\n"
"s_add_u32 m0, s61, 98304\n"
"s_add_u32 s71, s71, s33\n"
"buffer_load_dwordx4 v255, s[40:43], s71 offen lds\n"
"s_add_u32 m0, s61, 99328\n"
"s_xor_b32 s71, s71, 128\n"
"buffer_load_dwordx4 v255, s[40:43], s71 offen lds\n"
"v_mfma_f32_16x16x32_bf16 v[68:71], v[224:227], v[136:139], v[68:71]\n"
"ds_read_b64_tr_b16 v[224:225], v250 offset:50176\n"
"ds_read_b64_tr_b16 v[226:227], v250 offset:54272\n"
"s_waitcnt lgkmcnt(12)\n"
"v_mfma_f32_16x16x32_bf16 v[72:75], v[228:231], v[104:107], v[72:75]\n"
"v_mfma_f32_16x16x32_bf16 v[76:79], v[228:231], v[120:123], v[76:79]\n"
"v_mfma_f32_16x16x32_bf16 v[80:83], v[228:231], v[136:139], v[80:83]\n"
"ds_read_b64_tr_b16 v[228:229], v249 offset:50688\n"
"ds_read_b64_tr_b16 v[230:231], v249 offset:54784\n"
"v_mfma_f32_16x16x32_bf16 v[84:87], v[232:235], v[104:107], v[84:87]\n"
"s_add_u32 s71, s67, 131072\n"
"s_add_u32 m0, s61, 32768\n"
"s_and_b32 s71, s71, 1048575\n"
"s_add_u32 s71, s71, s33\n"
"buffer_load_dwordx4 v255, s[40:43], s71 offen lds\n"
"s_add_u32 m0, s61, 33792\n"
"s_xor_b32 s71, s71, 128\n"
"buffer_load_dwordx4 v255, s[40:43], s71 offen lds\n"
"v_mfma_f32_16x16x32_bf16 v[88:91], v[232:235], v[120:123], v[88:91]\n"
"v_mfma_f32_16x16x32_bf16 v[92:95], v[232:235], v[136:139], v[92:95]\n"
"ds_read_b64_tr_b16 v[232:233], v250 offset:50688\n"
"ds_read_b64_tr_b16 v[234:235], v250 offset:54784\n"
"s_barrier\n"
"; step 31\n"
"s_waitcnt lgkmcnt(12)\n"
"v_mfma_f32_16x16x32_bf16 v[0:3], v[204:207], v[108:111], v[0:3]\n"
"s_cmp_le_u32 s60, 1\n"
"s_cselect_b32 s82, 2, 1\n"
"s_sub_u32 s82, s82, s60\n"
"s_mul_i32 s82, s82, 98304\n"
"s_add_u32 s82, s82, s73\n"
"buffer_load_dwordx4 v[104:107], v253, s[44:47], s82 offen offset:2048 sc1\n"
"v_mfma_f32_16x16x32_bf16 v[4:7], v[204:207], v[124:127], v[4:7]\n"
"v_mfma_f32_16x16x32_bf16 v[8:11], v[204:207], v[140:143], v[8:11]\n"
"ds_read_b64_tr_b16 v[204:205], v249 offset:57344\n"
"ds_read_b64_tr_b16 v[206:207], v249 offset:61440\n"
"v_mfma_f32_16x16x32_bf16 v[12:15], v[208:211], v[108:111], v[12:15]\n"
"s_add_u32 s71, s82, 4096\n"
"buffer_load_dwordx4 v[120:123], v253, s[44:47], s71 offen offset:2048 sc1\n"
"v_mfma_f32_16x16x32_bf16 v[16:19], v[208:211], v[124:127], v[16:19]\n"
"v_mfma_f32_16x16x32_bf16 v[20:23], v[208:211], v[140:143], v[20:23]\n"
"ds_read_b64_tr_b16 v[208:209], v250 offset:57344\n"
"ds_read_b64_tr_b16 v[210:211], v250 offset:61440\n"
"s_waitcnt lgkmcnt(12)\n"
"v_mfma_f32_16x16x32_bf16 v[24:27], v[212:215], v[108:111], v[24:27]\n"
"s_add_u32 s71, s82, 8192\n"
"buffer_load_dwordx4 v[136:139], v253, s[44:47], s71 offen offset:2048 sc1\n"
"v_mfma_f32_16x16x32_bf16 v[28:31], v[212:215], v[124:127], v[28:31]\n"
"v_mfma_f32_16x16x32_bf16 v[32:35], v[212:215], v[140:143], v[32:35]\n"
"ds_read_b64_tr_b16 v[212:213], v249 offset:57856\n"
"ds_read_b64_tr_b16 v[214:215], v249 offset:61952\n"
"v_mfma_f32_16x16x32_bf16 v[36:39], v[216:219], v[108:111], v[36:39]\n"
"v_mfma_f32_16x16x32_bf16 v[40:43], v[216:219], v[124:127], v[40:43]\n"
"v_mfma_f32_16x16x32_bf16 v[44:47], v[216:219], v[140:143], v[44:47]\n"
"ds_read_b64_tr_b16 v[216:217], v250 offset:57856\n"
"ds_read_b64_tr_b16 v[218:219], v250 offset:61952\n"
"s_waitcnt lgkmcnt(12)\n"
"v_mfma_f32_16x16x32_bf16 v[48:51], v[220:223], v[108:111], v[48:51]\n"
"s_add_u32 s71, s67, 163840\n"
"s_add_u32 m0, s61, 90112\n"
"s_and_b32 s71, s71, 1048575\n"
"s_add_u32 s71, s71, s33\n"
"buffer_load_dwordx4 v255, s[40:43], s71 offen lds\n"
"s_add_u32 m0, s61, 91136\n"
"s_xor_b32 s71, s71, 128\n"
"buffer_load_dwordx4 v255, s[40:43], s71 offen lds\n"
"v_mfma_f32_16x16x32_bf16 v[52:55], v[220:223], v[124:127], v[52:55]\n"
"v_mfma_f32_16x16x32_bf16 v[56:59], v[220:223], v[140:143], v[56:59]\n"
"ds_read_b64_tr_b16 v[220:221], v249 offset:58368\n"
"ds_read_b64_tr_b16 v[222:223], v249 offset:62464\n"
"v_mfma_f32_16x16x32_bf16 v[60:63], v[224:227], v[108:111], v[60:63]\n"
"v_mfma_f32_16x16x32_bf16 v[64:67], v[224:227], v[124:127], v[64:67]\n"
"v_mfma_f32_16x16x32_bf16 v[68:71], v[224:227], v[140:143], v[68:71]\n"
"ds_read_b64_tr_b16 v[224:225], v250 offset:58368\n"
"ds_read_b64_tr_b16 v[226:227], v250 offset:62464\n"
"s_waitcnt lgkmcnt(12)\n"
"v_mfma_f32_16x16x32_bf16 v[72:75], v[228:231], v[108:111], v[72:75]\n"
"v_mfma_f32_16x16x32_bf16 v[76:79], v[228:231], v[124:127], v[76:79]\n"
"s_add_u32 s71, s67, 196608\n"
"s_add_u32 m0, s61, 106496\n"
"s_and_b32 s71, s71, 1048575\n"
"s_add_u32 s71, s71, s33\n"
"buffer_load_dwordx4 v255, s[40:43], s71 offen lds\n"
"s_add_u32 m0, s61, 107520\n"
"s_xor_b32 s71, s71, 128\n"
"buffer_load_dwordx4 v255, s[40:43], s71 offen lds\n"
"v_mfma_f32_16x16x32_bf16 v[80:83], v[228:231], v[140:143], v[80:83]\n"
"ds_read_b64_tr_b16 v[228:229], v249 offset:58880\n"
"ds_read_b64_tr_b16 v[230:231], v249 offset:62976\n"
"v_mfma_f32_16x16x32_bf16 v[84:87], v[232:235], v[108:111], v[84:87]\n"
"v_mfma_f32_16x16x32_bf16 v[88:91], v[232:235], v[124:127], v[88:91]\n"
"v_mfma_f32_16x16x32_bf16 v[92:95], v[232:235], v[140:143], v[92:95]\n"
"ds_read_b64_tr_b16 v[232:233], v250 offset:58880\n"
"ds_read_b64_tr_b16 v[234:235], v250 offset:62976\n"
"s_waitcnt vmcnt(33)\n"
"s_barrier\n"
"; step 32\n"
"s_waitcnt lgkmcnt(12)\n"
"s_waitcnt vmcnt(32)\n"
"v_mfma_f32_16x16x32_bf16 v[0:3], v[204:207], v[144:147], v[0:3]\n"
"s_waitcnt vmcnt(31)\n"
"v_mfma_f32_16x16x32_bf16 v[4:7], v[204:207], v[160:163], v[4:7]\n"
"s_waitcnt vmcnt(30)\n"
"v_mfma_f32_16x16x32_bf16 v[8:11], v[204:207], v[176:179], v[8:11]\n"
"v_add_u32 v249, 24576, v249\n"
"v_add_u32 v250, 24576, v250\n"
"ds_read_b64_tr_b16 v[204:205], v249 offset:40960\n"
"ds_read_b64_tr_b16 v[206:207], v249 offset:45056\n"
"v_mfma_f32_16x16x32_bf16 v[12:15], v[208:211], v[144:147], v[12:15]\n"
"v_mfma_f32_16x16x32_bf16 v[16:19], v[208:211], v[160:163], v[16:19]\n"
"v_mfma_f32_16x16x32_bf16 v[20:23], v[208:211], v[176:179], v[20:23]\n"
"ds_read_b64_tr_b16 v[208:209], v250 offset:40960\n"
"ds_read_b64_tr_b16 v[210:211], v250 offset:45056\n"
"s_waitcnt lgkmcnt(12)\n"
"v_mfma_f32_16x16x32_bf16 v[24:27], v[212:215], v[144:147], v[24:27]\n"
"v_mfma_f32_16x16x32_bf16 v[28:31], v[212:215], v[160:163], v[28:31]\n"
"v_mfma_f32_16x16x32_bf16 v[32:35], v[212:215], v[176:179], v[32:35]\n"
"ds_read_b64_tr_b16 v[212:213], v249 offset:41472\n"
"ds_read_b64_tr_b16 v[214:215], v249 offset:45568\n"
"v_mfma_f32_16x16x32_bf16 v[36:39], v[216:219], v[144:147], v[36:39]\n"
"v_mfma_f32_16x16x32_bf16 v[40:43], v[216:219], v[160:163], v[40:43]\n"
"v_mfma_f32_16x16x32_bf16 v[44:47], v[216:219], v[176:179], v[44:47]\n"
"ds_read_b64_tr_b16 v[216:217], v250 offset:41472\n"
"ds_read_b64_tr_b16 v[218:219], v250 offset:45568\n"
"s_waitcnt lgkmcnt(12)\n"
"v_mfma_f32_16x16x32_bf16 v[48:51], v[220:223], v[144:147], v[48:51]\n"
"v_mfma_f32_16x16x32_bf16 v[52:55], v[220:223], v[160:163], v[52:55]\n"
"v_mfma_f32_16x16x32_bf16 v[56:59], v[220:223], v[176:179], v[56:59]\n"
"ds_read_b64_tr_b16 v[220:221], v249 offset:41984\n"
"ds_read_b64_tr_b16 v[222:223], v249 offset:46080\n"
"v_mfma_f32_16x16x32_bf16 v[60:63], v[224:227], v[144:147], v[60:63]\n"
"v_mfma_f32_16x16x32_bf16 v[64:67], v[224:227], v[160:163], v[64:67]\n"
"v_mfma_f32_16x16x32_bf16 v[68:71], v[224:227], v[176:179], v[68:71]\n"
"ds_read_b64_tr_b16 v[224:225], v250 offset:41984\n"
"ds_read_b64_tr_b16 v[226:227], v250 offset:46080\n"
"s_waitcnt lgkmcnt(12)\n"
"v_mfma_f32_16x16x32_bf16 v[72:75], v[228:231], v[144:147], v[72:75]\n"
"v_mfma_f32_16x16x32_bf16 v[76:79], v[228:231], v[160:163], v[76:79]\n"
"v_mfma_f32_16x16x32_bf16 v[80:83], v[228:231], v[176:179], v[80:83]\n"
"ds_read_b64_tr_b16 v[228:229], v249 offset:42496\n"
"ds_read_b64_tr_b16 v[230:231], v249 offset:46592\n"
"v_mfma_f32_16x16x32_bf16 v[84:87], v[232:235], v[144:147], v[84:87]\n"
"v_mfma_f32_16x16x32_bf16 v[88:91], v[232:235], v[160:163], v[88:91]\n"
"v_mfma_f32_16x16x32_bf16 v[92:95], v[232:235], v[176:179], v[92:95]\n"
"ds_read_b64_tr_b16 v[232:233], v250 offset:42496\n"
"ds_read_b64_tr_b16 v[234:235], v250 offset:46592\n"
"s_waitcnt lgkmcnt(12)\n"
"s_waitcnt vmcnt(29)\n"
"v_mfma_f32_16x16x32_bf16 v[0:3], v[204:207], v[148:151], v[0:3]\n"
"s_waitcnt vmcnt(28)\n"
"v_mfma_f32_16x16x32_bf16 v[4:7], v[204:207], v[164:167], v[4:7]\n"
"s_waitcnt vmcnt(27)\n"
"v_mfma_f32_16x16x32_bf16 v[8:11], v[204:207], v[180:183], v[8:11]\n"
"ds_read_b64_tr_b16 v[204:205], v249 offset:49152\n"
"ds_read_b64_tr_b16 v[206:207], v249 offset:53248\n"
"v_mfma_f32_16x16x32_bf16 v[12:15], v[208:211], v[148:151], v[12:15]\n"
"v_mfma_f32_16x16x32_bf16 v[16:19], v[208:211], v[164:167], v[16:19]\n"
"v_mfma_f32_16x16x32_bf16 v[20:23], v[208:211], v[180:183], v[20:23]\n"
"ds_read_b64_tr_b16 v[208:209], v250 offset:49152\n"
"ds_read_b64_tr_b16 v[210:211], v250 offset:53248\n"
"s_waitcnt lgkmcnt(12)\n"
"v_mfma_f32_16x16x32_bf16 v[24:27], v[212:215], v[148:151], v[24:27]\n"
"v_mfma_f32_16x16x32_bf16 v[28:31], v[212:215], v[164:167], v[28:31]\n"
"v_mfma_f32_16x16x32_bf16 v[32:35], v[212:215], v[180:183], v[32:35]\n"
"ds_read_b64_tr_b16 v[212:213], v249 offset:49664\n"
"ds_read_b64_tr_b16 v[214:215], v249 offset:53760\n"
"v_mfma_f32_16x16x32_bf16 v[36:39], v[216:219], v[148:151], v[36:39]\n"
"v_mfma_f32_16x16x32_bf16 v[40:43], v[216:219], v[164:167], v[40:43]\n"
"v_mfma_f32_16x16x32_bf16 v[44:47], v[216:219], v[180:183], v[44:47]\n"
"ds_read_b64_tr_b16 v[216:217], v250 offset:49664\n"
"ds_read_b64_tr_b16 v[218:219], v250 offset:53760\n"
"s_waitcnt lgkmcnt(12)\n"
"v_mfma_f32_16x16x32_bf16 v[48:51], v[220:223], v[148:151], v[48:51]\n"
"v_mfma_f32_16x16x32_bf16 v[52:55], v[220:223], v[164:167], v[52:55]\n"
"v_mfma_f32_16x16x32_bf16 v[56:59], v[220:223], v[180:183], v[56:59]\n"
"ds_read_b64_tr_b16 v[220:221], v249 offset:50176\n"
"ds_read_b64_tr_b16 v[222:223], v249 offset:54272\n"
"v_mfma_f32_16x16x32_bf16 v[60:63], v[224:227], v[148:151], v[60:63]\n"
"v_mfma_f32_16x16x32_bf16 v[64:67], v[224:227], v[164:167], v[64:67]\n"
"v_mfma_f32_16x16x32_bf16 v[68:71], v[224:227], v[180:183], v[68:71]\n"
"ds_read_b64_tr_b16 v[224:225], v250 offset:50176\n"
"ds_read_b64_tr_b16 v[226:227], v250 offset:54272\n"
"s_waitcnt lgkmcnt(12)\n"
"v_mfma_f32_16x16x32_bf16 v[72:75], v[228:231], v[148:151], v[72:75]\n"
"v_mfma_f32_16x16x32_bf16 v[76:79], v[228:231], v[164:167], v[76:79]\n"
"v_mfma_f32_16x16x32_bf16 v[80:83], v[228:231], v[180:183], v[80:83]\n"
"ds_read_b64_tr_b16 v[228:229], v249 offset:50688\n"
"ds_read_b64_tr_b16 v[230:231], v249 offset:54784\n"
"v_mfma_f32_16x16x32_bf16 v[84:87], v[232:235], v[148:151], v[84:87]\n"
"v_mfma_f32_16x16x32_bf16 v[88:91], v[232:235], v[164:167], v[88:91]\n"
"v_mfma_f32_16x16x32_bf16 v[92:95], v[232:235], v[180:183], v[92:95]\n"
"ds_read_b64_tr_b16 v[232:233], v250 offset:50688\n"
"ds_read_b64_tr_b16 v[234:235], v250 offset:54784\n"
"s_waitcnt lgkmcnt(12)\n"
"s_waitcnt vmcnt(26)\n"
"v_mfma_f32_16x16x32_bf16 v[0:3], v[204:207], v[152:155], v[0:3]\n"
"s_waitcnt vmcnt(25)\n"
"v_mfma_f32_16x16x32_bf16 v[4:7], v[204:207], v[168:171], v[4:7]\n"
"s_cmp_le_u32 s60, 1\n"
"s_cselect_b32 s82, 2, 1\n"
"s_sub_u32 s82, s82, s60\n"
"s_mul_i32 s82, s82, 98304\n"
"s_add_u32 s82, s82, s73\n"
"buffer_load_dwordx4 v[108:111], v253, s[44:47], s82 offen offset:3072 sc1\n"
"s_waitcnt vmcnt(25)\n"
"v_mfma_f32_16x16x32_bf16 v[8:11], v[204:207], v[184:187], v[8:11]\n"
"ds_read_b64_tr_b16 v[204:205], v249 offset:57344\n"
"ds_read_b64_tr_b16 v[206:207], v249 offset:61440\n"
"v_mfma_f32_16x16x32_bf16 v[12:15], v[208:211], v[152:155], v[12:15]\n"
"v_mfma_f32_16x16x32_bf16 v[16:19], v[208:211], v[168:171], v[16:19]\n"
"v_mfma_f32_16x16x32_bf16 v[20:23], v[208:211], v[184:187], v[20:23]\n"
"ds_read_b64_tr_b16 v[208:209], v250 offset:57344\n"
"ds_read_b64_tr_b16 v[210:211], v250 offset:61440\n"
"s_waitcnt lgkmcnt(12)\n"
"v_mfma_f32_16x16x32_bf16 v[24:27], v[212:215], v[152:155], v[24:27]\n"
"v_mfma_f32_16x16x32_bf16 v[28:31], v[212:215], v[168:171], v[28:31]\n"
"v_mfma_f32_16x16x32_bf16 v[32:35], v[212:215], v[184:187], v[32:35]\n"
"ds_read_b64_tr_b16 v[212:213], v249 offset:57856\n"
"ds_read_b64_tr_b16 v[214:215], v249 offset:61952\n"
"v_mfma_f32_16x16x32_bf16 v[36:39], v[216:219], v[152:155], v[36:39]\n"
"v_mfma_f32_16x16x32_bf16 v[40:43], v[216:219], v[168:171], v[40:43]\n"
"s_add_u32 s71, s82, 4096\n"
"buffer_load_dwordx4 v[124:127], v253, s[44:47], s71 offen offset:3072 sc1\n"
"v_mfma_f32_16x16x32_bf16 v[44:47], v[216:219], v[184:187], v[44:47]\n"
"ds_read_b64_tr_b16 v[216:217], v250 offset:57856\n"
"ds_read_b64_tr_b16 v[218:219], v250 offset:61952\n"
"s_waitcnt lgkmcnt(12)\n"
"v_mfma_f32_16x16x32_bf16 v[48:51], v[220:223], v[152:155], v[48:51]\n"
"v_mfma_f32_16x16x32_bf16 v[52:55], v[220:223], v[168:171], v[52:55]\n"
"v_mfma_f32_16x16x32_bf16 v[56:59], v[220:223], v[184:187], v[56:59]\n"
"ds_read_b64_tr_b16 v[220:221], v249 offset:58368\n"
"ds_read_b64_tr_b16 v[222:223], v249 offset:62464\n"
"v_mfma_f32_16x16x32_bf16 v[60:63], v[224:227], v[152:155], v[60:63]\n"
"v_mfma_f32_16x16x32_bf16 v[64:67], v[224:227], v[168:171], v[64:67]\n"
"s_add_u32 s71, s82, 8192\n"
"buffer_load_dwordx4 v[140:143], v253, s[44:47], s71 offen offset:3072 sc1\n"
"v_mfma_f32_16x16x32_bf16 v[68:71], v[224:227], v[184:187], v[68:71]\n"
"ds_read_b64_tr_b16 v[224:225], v250 offset:58368\n"
"ds_read_b64_tr_b16 v[226:227], v250 offset:62464\n"
"s_waitcnt lgkmcnt(12)\n"
"v_mfma_f32_16x16x32_bf16 v[72:75], v[228:231], v[152:155], v[72:75]\n"
"v_mfma_f32_16x16x32_bf16 v[76:79], v[228:231], v[168:171], v[76:79]\n"
"v_mfma_f32_16x16x32_bf16 v[80:83], v[228:231], v[184:187], v[80:83]\n"
"ds_read_b64_tr_b16 v[228:229], v249 offset:58880\n"
"ds_read_b64_tr_b16 v[230:231], v249 offset:62976\n"
"v_mfma_f32_16x16x32_bf16 v[84:87], v[232:235], v[152:155], v[84:87]\n"
"v_mfma_f32_16x16x32_bf16 v[88:91], v[232:235], v[168:171], v[88:91]\n"
"v_mfma_f32_16x16x32_bf16 v[92:95], v[232:235], v[184:187], v[92:95]\n"
"ds_read_b64_tr_b16 v[232:233], v250 offset:58880\n"
"ds_read_b64_tr_b16 v[234:235], v250 offset:62976\n"
"s_waitcnt lgkmcnt(12)\n"
"s_waitcnt vmcnt(22)\n"
"v_mfma_f32_16x16x32_bf16 v[0:3], v[204:207], v[156:159], v[0:3]\n"
"s_waitcnt vmcnt(21)\n"
"v_mfma_f32_16x16x32_bf16 v[4:7], v[204:207], v[172:175], v[4:7]\n"
"s_cmp_le_u32 s60, 2\n"
"s_cselect_b32 s82, 3, 2\n"
"s_sub_u32 s82, s82, s60\n"
"s_mul_i32 s82, s82, 98304\n"
"s_add_u32 s82, s82, s73\n"
"buffer_load_dwordx4 v[144:147], v253, s[44:47], s82 offen sc1\n"
"s_waitcnt vmcnt(21)\n"
"v_mfma_f32_16x16x32_bf16 v[8:11], v[204:207], v[188:191], v[8:11]\n"
"v_add_u32 v249, -98304, v249\n"
"v_add_u32 v250, -98304, v250\n"
"ds_read_b64_tr_b16 v[204:205], v249 offset:0\n"
"ds_read_b64_tr_b16 v[206:207], v249 offset:4096\n"
"v_mfma_f32_16x16x32_bf16 v[12:15], v[208:211], v[156:159], v[12:15]\n"
"v_mfma_f32_16x16x32_bf16 v[16:19], v[208:211], v[172:175], v[16:19]\n"
"v_mfma_f32_16x16x32_bf16 v[20:23], v[208:211], v[188:191], v[20:23]\n"
"ds_read_b64_tr_b16 v[208:209], v250 offset:0\n"
"ds_read_b64_tr_b16 v[210:211], v250 offset:4096\n"
"s_waitcnt lgkmcnt(12)\n"
"v_mfma_f32_16x16x32_bf16 v[24:27], v[212:215], v[156:159], v[24:27]\n"
"v_mfma_f32_16x16x32_bf16 v[28:31], v[212:215], v[172:175], v[28:31]\n"
"v_mfma_f32_16x16x32_bf16 v[32:35], v[212:215], v[188:191], v[32:35]\n"
"ds_read_b64_tr_b16 v[212:213], v249 offset:512\n"
"ds_read_b64_tr_b16 v[214:215], v249 offset:4608\n"
"v_mfma_f32_16x16x32_bf16 v[36:39], v[216:219], v[156:159], v[36:39]\n"
"v_mfma_f32_16x16x32_bf16 v[40:43], v[216:219], v[172:175], v[40:43]\n"
"s_add_u32 s71, s82, 4096\n"
"buffer_load_dwordx4 v[160:163], v253, s[44:47], s71 offen sc1\n"
"v_mfma_f32_16x16x32_bf16 v[44:47], v[216:219], v[188:191], v[44:47]\n"
"ds_read_b64_tr_b16 v[216:217], v250 offset:512\n"
"ds_read_b64_tr_b16 v[218:219], v250 offset:4608\n"
"s_waitcnt lgkmcnt(12)\n"
"v_mfma_f32_16x16x32_bf16 v[48:51], v[220:223], v[156:159], v[48:51]\n"
"v_mfma_f32_16x16x32_bf16 v[52:55], v[220:223], v[172:175], v[52:55]\n"
"v_mfma_f32_16x16x32_bf16 v[56:59], v[220:223], v[188:191], v[56:59]\n"
"ds_read_b64_tr_b16 v[220:221], v249 offset:1024\n"
"ds_read_b64_tr_b16 v[222:223], v249 offset:5120\n"
"v_mfma_f32_16x16x32_bf16 v[60:63], v[224:227], v[156:159], v[60:63]\n"
"v_mfma_f32_16x16x32_bf16 v[64:67], v[224:227], v[172:175], v[64:67]\n"
"s_add_u32 s71, s82, 8192\n"
"buffer_load_dwordx4 v[176:179], v253, s[44:47], s71 offen sc1\n"
"v_mfma_f32_16x16x32_bf16 v[68:71], v[224:227], v[188:191], v[68:71]\n"
"ds_read_b64_tr_b16 v[224:225], v250 offset:1024\n"
"ds_read_b64_tr_b16 v[226:227], v250 offset:5120\n"
"s_waitcnt lgkmcnt(12)\n"
"v_mfma_f32_16x16x32_bf16 v[72:75], v[228:231], v[156:159], v[72:75]\n"
"v_mfma_f32_16x16x32_bf16 v[76:79], v[228:231], v[172:175], v[76:79]\n"
"v_mfma_f32_16x16x32_bf16 v[80:83], v[228:231], v[188:191], v[80:83]\n"
"ds_read_b64_tr_b16 v[228:229], v249 offset:1536\n"
"ds_read_b64_tr_b16 v[230:231], v249 offset:5632\n"
"v_mfma_f32_16x16x32_bf16 v[84:87], v[232:235], v[156:159], v[84:87]\n"
"v_mfma_f32_16x16x32_bf16 v[88:91], v[232:235], v[172:175], v[88:91]\n"
"v_mfma_f32_16x16x32_bf16 v[92:95], v[232:235], v[188:191], v[92:95]\n"
"ds_read_b64_tr_b16 v[232:233], v250 offset:1536\n"
"ds_read_b64_tr_b16 v[234:235], v250 offset:5632\n"
"s_waitcnt lgkmcnt(12)\n"
"s_waitcnt vmcnt(22)\n"
"v_mfma_f32_16x16x32_bf16 v[0:3], v[204:207], v[96:99], v[0:3]\n"
"s_waitcnt vmcnt(21)\n"
"v_mfma_f32_16x16x32_bf16 v[4:7], v[204:207], v[112:115], v[4:7]\n"
"s_cmp_le_u32 s60, 2\n"
"s_cselect_b32 s82, 3, 2\n"
"s_sub_u32 s82, s82, s60\n"
"s_mul_i32 s82, s82, 98304\n"
"s_add_u32 s82, s82, s73\n"
"buffer_load_dwordx4 v[148:151], v253, s[44:47], s82 offen offset:1024 sc1\n"
"s_waitcnt vmcnt(21)\n"
"v_mfma_f32_16x16x32_bf16 v[8:11], v[204:207], v[128:131], v[8:11]\n"
"ds_read_b64_tr_b16 v[204:205], v249 offset:8192\n"
"ds_read_b64_tr_b16 v[206:207], v249 offset:12288\n"
"v_mfma_f32_16x16x32_bf16 v[12:15], v[208:211], v[96:99], v[12:15]\n"
"v_mfma_f32_16x16x32_bf16 v[16:19], v[208:211], v[112:115], v[16:19]\n"
"v_mfma_f32_16x16x32_bf16 v[20:23], v[208:211], v[128:131], v[20:23]\n"
"ds_read_b64_tr_b16 v[208:209], v250 offset:8192\n"
"ds_read_b64_tr_b16 v[210:211], v250 offset:12288\n"
"s_waitcnt lgkmcnt(12)\n"
"v_mfma_f32_16x16x32_bf16 v[24:27], v[212:215], v[96:99], v[24:27]\n"
"v_mfma_f32_16x16x32_bf16 v[28:31], v[212:215], v[112:115], v[28:31]\n"
"v_mfma_f32_16x16x32_bf16 v[32:35], v[212:215], v[128:131], v[32:35]\n"
"ds_read_b64_tr_b16 v[212:213], v249 offset:8704\n"
"ds_read_b64_tr_b16 v[214:215], v249 offset:12800\n"
"v_mfma_f32_16x16x32_bf16 v[36:39], v[216:219], v[96:99], v[36:39]\n"
"v_mfma_f32_16x16x32_bf16 v[40:43], v[216:219], v[112:115], v[40:43]\n"
"s_add_u32 s71, s82, 4096\n"
"buffer_load_dwordx4 v[164:167], v253, s[44:47], s71 offen offset:1024 sc1\n"
"v_mfma_f32_16x16x32_bf16 v[44:47], v[216:219], v[128:131], v[44:47]\n"
"ds_read_b64_tr_b16 v[216:217], v250 offset:8704\n"
"ds_read_b64_tr_b16 v[218:219], v250 offset:12800\n"
"s_waitcnt lgkmcnt(12)\n"
"v_mfma_f32_16x16x32_bf16 v[48:51], v[220:223], v[96:99], v[48:51]\n"
"v_mfma_f32_16x16x32_bf16 v[52:55], v[220:223], v[112:115], v[52:55]\n"
"v_mfma_f32_16x16x32_bf16 v[56:59], v[220:223], v[128:131], v[56:59]\n"
"ds_read_b64_tr_b16 v[220:221], v249 offset:9216\n"
"ds_read_b64_tr_b16 v[222:223], v249 offset:13312\n"
"v_mfma_f32_16x16x32_bf16 v[60:63], v[224:227], v[96:99], v[60:63]\n"
"v_mfma_f32_16x16x32_bf16 v[64:67], v[224:227], v[112:115], v[64:67]\n"
"s_add_u32 s71, s82, 8192\n"
"buffer_load_dwordx4 v[180:183], v253, s[44:47], s71 offen offset:1024 sc1\n"
"v_mfma_f32_16x16x32_bf16 v[68:71], v[224:227], v[128:131], v[68:71]\n"
"ds_read_b64_tr_b16 v[224:225], v250 offset:9216\n"
"ds_read_b64_tr_b16 v[226:227], v250 offset:13312\n"
"s_waitcnt lgkmcnt(12)\n"
"v_mfma_f32_16x16x32_bf16 v[72:75], v[228:231], v[96:99], v[72:75]\n"
"v_mfma_f32_16x16x32_bf16 v[76:79], v[228:231], v[112:115], v[76:79]\n"
"v_mfma_f32_16x16x32_bf16 v[80:83], v[228:231], v[128:131], v[80:83]\n"
"ds_read_b64_tr_b16 v[228:229], v249 offset:9728\n"
"ds_read_b64_tr_b16 v[230:231], v249 offset:13824\n"
"v_mfma_f32_16x16x32_bf16 v[84:87], v[232:235], v[96:99], v[84:87]\n"
"v_mfma_f32_16x16x32_bf16 v[88:91], v[232:235], v[112:115], v[88:91]\n"
"v_mfma_f32_16x16x32_bf16 v[92:95], v[232:235], v[128:131], v[92:95]\n"
"ds_read_b64_tr_b16 v[232:233], v250 offset:9728\n"
"ds_read_b64_tr_b16 v[234:235], v250 offset:13824\n"
"s_waitcnt lgkmcnt(12)\n"
"s_waitcnt vmcnt(22)\n"
"v_mfma_f32_16x16x32_bf16 v[0:3], v[204:207], v[100:103], v[0:3]\n"
"s_waitcnt vmcnt(21)\n"
"v_mfma_f32_16x16x32_bf16 v[4:7], v[204:207], v[116:119], v[4:7]\n"
"s_cmp_le_u32 s60, 2\n"
"s_cselect_b32 s82, 3, 2\n"
"s_sub_u32 s82, s82, s60\n"
"s_mul_i32 s82, s82, 98304\n"
"s_add_u32 s82, s82, s73\n"
"buffer_load_dwordx4 v[152:155], v253, s[44:47], s82 offen offset:2048 sc1\n"
"s_waitcnt vmcnt(21)\n"
"v_mfma_f32_16x16x32_bf16 v[8:11], v[204:207], v[132:135], v[8:11]\n"
"v_add_u32 v249, 65536, v249\n"
"v_add_u32 v250, 65536, v250\n"
"ds_read_b64_tr_b16 v[204:205], v249 offset:0\n"
"ds_read_b64_tr_b16 v[206:207], v249 offset:4096\n"
"v_mfma_f32_16x16x32_bf16 v[12:15], v[208:211], v[100:103], v[12:15]\n"
"v_mfma_f32_16x16x32_bf16 v[16:19], v[208:211], v[116:119], v[16:19]\n"
"v_mfma_f32_16x16x32_bf16 v[20:23], v[208:211], v[132:135], v[20:23]\n"
"ds_read_b64_tr_b16 v[208:209], v250 offset:0\n"
"ds_read_b64_tr_b16 v[210:211], v250 offset:4096\n"
"s_waitcnt lgkmcnt(12)\n"
"v_mfma_f32_16x16x32_bf16 v[24:27], v[212:215], v[100:103], v[24:27]\n"
"v_mfma_f32_16x16x32_bf16 v[28:31], v[212:215], v[116:119], v[28:31]\n"
"v_mfma_f32_16x16x32_bf16 v[32:35], v[212:215], v[132:135], v[32:35]\n"
"ds_read_b64_tr_b16 v[212:213], v249 offset:512\n"
"ds_read_b64_tr_b16 v[214:215], v249 offset:4608\n"
"v_mfma_f32_16x16x32_bf16 v[36:39], v[216:219], v[100:103], v[36:39]\n"
"v_mfma_f32_16x16x32_bf16 v[40:43], v[216:219], v[116:119], v[40:43]\n"
"s_add_u32 s71, s82, 4096\n"
"buffer_load_dwordx4 v[168:171], v253, s[44:47], s71 offen offset:2048 sc1\n"
"v_mfma_f32_16x16x32_bf16 v[44:47], v[216:219], v[132:135], v[44:47]\n"
"ds_read_b64_tr_b16 v[216:217], v250 offset:512\n"
"ds_read_b64_tr_b16 v[218:219], v250 offset:4608\n"
"s_waitcnt lgkmcnt(12)\n"
"v_mfma_f32_16x16x32_bf16 v[48:51], v[220:223], v[100:103], v[48:51]\n"
"v_mfma_f32_16x16x32_bf16 v[52:55], v[220:223], v[116:119], v[52:55]\n"
"v_mfma_f32_16x16x32_bf16 v[56:59], v[220:223], v[132:135], v[56:59]\n"
"ds_read_b64_tr_b16 v[220:221], v249 offset:1024\n"
"ds_read_b64_tr_b16 v[222:223], v249 offset:5120\n"
"v_mfma_f32_16x16x32_bf16 v[60:63], v[224:227], v[100:103], v[60:63]\n"
"v_mfma_f32_16x16x32_bf16 v[64:67], v[224:227], v[116:119], v[64:67]\n"
"s_add_u32 s71, s82, 8192\n"
"buffer_load_dwordx4 v[184:187], v253, s[44:47], s71 offen offset:2048 sc1\n"
"v_mfma_f32_16x16x32_bf16 v[68:71], v[224:227], v[132:135], v[68:71]\n"
"ds_read_b64_tr_b16 v[224:225], v250 offset:1024\n"
"ds_read_b64_tr_b16 v[226:227], v250 offset:5120\n"
"s_waitcnt lgkmcnt(12)\n"
"v_mfma_f32_16x16x32_bf16 v[72:75], v[228:231], v[100:103], v[72:75]\n"
"v_mfma_f32_16x16x32_bf16 v[76:79], v[228:231], v[116:119], v[76:79]\n"
"v_mfma_f32_16x16x32_bf16 v[80:83], v[228:231], v[132:135], v[80:83]\n"
"ds_read_b64_tr_b16 v[228:229], v249 offset:1536\n"
"ds_read_b64_tr_b16 v[230:231], v249 offset:5632\n"
"v_mfma_f32_16x16x32_bf16 v[84:87], v[232:235], v[100:103], v[84:87]\n"
"v_mfma_f32_16x16x32_bf16 v[88:91], v[232:235], v[116:119], v[88:91]\n"
"v_mfma_f32_16x16x32_bf16 v[92:95], v[232:235], v[132:135], v[92:95]\n"
"ds_read_b64_tr_b16 v[232:233], v250 offset:1536\n"
"ds_read_b64_tr_b16 v[234:235], v250 offset:5632\n"
"s_waitcnt lgkmcnt(12)\n"
"s_waitcnt vmcnt(18)\n"
"v_mfma_f32_16x16x32_bf16 v[0:3], v[204:207], v[104:107], v[0:3]\n"
"s_waitcnt vmcnt(17)\n"
"v_mfma_f32_16x16x32_bf16 v[4:7], v[204:207], v[120:123], v[4:7]\n"
"s_cmp_le_u32 s60, 2\n"
"s_cselect_b32 s82, 3, 2\n"
"s_sub_u32 s82, s82, s60\n"
"s_mul_i32 s82, s82, 98304\n"
"s_add_u32 s82, s82, s73\n"
"buffer_load_dwordx4 v[156:159], v253, s[44:47], s82 offen offset:3072 sc1\n"
"s_waitcnt vmcnt(17)\n"
"v_mfma_f32_16x16x32_bf16 v[8:11], v[204:207], v[136:139], v[8:11]\n"
"v_add_u32 v249, -65536, v249\n"
"v_add_u32 v250, -65536, v250\n"
"ds_read_b64_tr_b16 v[204:205], v249 offset:16384\n"
"ds_read_b64_tr_b16 v[206:207], v249 offset:20480\n"
"v_mfma_f32_16x16x32_bf16 v[12:15], v[208:211], v[104:107], v[12:15]\n"
"v_mfma_f32_16x16x32_bf16 v[16:19], v[208:211], v[120:123], v[16:19]\n"
"v_mfma_f32_16x16x32_bf16 v[20:23], v[208:211], v[136:139], v[20:23]\n"
"ds_read_b64_tr_b16 v[208:209], v250 offset:16384\n"
"ds_read_b64_tr_b16 v[210:211], v250 offset:20480\n"
"s_waitcnt lgkmcnt(12)\n"
"v_mfma_f32_16x16x32_bf16 v[24:27], v[212:215], v[104:107], v[24:27]\n"
"v_mfma_f32_16x16x32_bf16 v[28:31], v[212:215], v[120:123], v[28:31]\n"
"v_mfma_f32_16x16x32_bf16 v[32:35], v[212:215], v[136:139], v[32:35]\n"
"ds_read_b64_tr_b16 v[212:213], v249 offset:16896\n"
"ds_read_b64_tr_b16 v[214:215], v249 offset:20992\n"
"v_mfma_f32_16x16x32_bf16 v[36:39], v[216:219], v[104:107], v[36:39]\n"
"v_mfma_f32_16x16x32_bf16 v[40:43], v[216:219], v[120:123], v[40:43]\n"
"s_add_u32 s71, s82, 4096\n"
"buffer_load_dwordx4 v[172:175], v253, s[44:47], s71 offen offset:3072 sc1\n"
"v_mfma_f32_16x16x32_bf16 v[44:47], v[216:219], v[136:139], v[44:47]\n"
"ds_read_b64_tr_b16 v[216:217], v250 offset:16896\n"
"ds_read_b64_tr_b16 v[218:219], v250 offset:20992\n"
"s_waitcnt lgkmcnt(12)\n"
"v_mfma_f32_16x16x32_bf16 v[48:51], v[220:223], v[104:107], v[48:51]\n"
"v_mfma_f32_16x16x32_bf16 v[52:55], v[220:223], v[120:123], v[52:55]\n"
"v_mfma_f32_16x16x32_bf16 v[56:59], v[220:223], v[136:139], v[56:59]\n"
"ds_read_b64_tr_b16 v[220:221], v249 offset:17408\n"
"ds_read_b64_tr_b16 v[222:223], v249 offset:21504\n"
"v_mfma_f32_16x16x32_bf16 v[60:63], v[224:227], v[104:107], v[60:63]\n"
"v_mfma_f32_16x16x32_bf16 v[64:67], v[224:227], v[120:123], v[64:67]\n"
"s_add_u32 s71, s82, 8192\n"
"buffer_load_dwordx4 v[188:191], v253, s[44:47], s71 offen offset:3072 sc1\n"
"v_mfma_f32_16x16x32_bf16 v[68:71], v[224:227], v[136:139], v[68:71]\n"
"ds_read_b64_tr_b16 v[224:225], v250 offset:17408\n"
"ds_read_b64_tr_b16 v[226:227], v250 offset:21504\n"
"s_waitcnt lgkmcnt(12)\n"
"v_mfma_f32_16x16x32_bf16 v[72:75], v[228:231], v[104:107], v[72:75]\n"
"v_mfma_f32_16x16x32_bf16 v[76:79], v[228:231], v[120:123], v[76:79]\n"
"v_mfma_f32_16x16x32_bf16 v[80:83], v[228:231], v[136:139], v[80:83]\n"
"ds_read_b64_tr_b16 v[228:229], v249 offset:17920\n"
"ds_read_b64_tr_b16 v[230:231], v249 offset:22016\n"
"v_mfma_f32_16x16x32_bf16 v[84:87], v[232:235], v[104:107], v[84:87]\n"
"v_mfma_f32_16x16x32_bf16 v[88:91], v[232:235], v[120:123], v[88:91]\n"
"v_mfma_f32_16x16x32_bf16 v[92:95], v[232:235], v[136:139], v[92:95]\n"
"ds_read_b64_tr_b16 v[232:233], v250 offset:17920\n"
"ds_read_b64_tr_b16 v[234:235], v250 offset:22016\n"
"s_waitcnt lgkmcnt(12)\n"
"s_waitcnt vmcnt(14)\n"
"v_mfma_f32_16x16x32_bf16 v[0:3], v[204:207], v[108:111], v[0:3]\n"
"s_waitcnt vmcnt(13)\n"
"v_mfma_f32_16x16x32_bf16 v[4:7], v[204:207], v[124:127], v[4:7]\n"
"s_waitcnt vmcnt(12)\n"
"v_mfma_f32_16x16x32_bf16 v[8:11], v[204:207], v[140:143], v[8:11]\n"
"v_add_u32 v249, 73728, v249\n"
"v_add_u32 v250, 73728, v250\n"
"ds_read_b64_tr_b16 v[204:205], v249 offset:0\n"
"ds_read_b64_tr_b16 v[206:207], v249 offset:4096\n"
"v_mfma_f32_16x16x32_bf16 v[12:15], v[208:211], v[108:111], v[12:15]\n"
"v_mfma_f32_16x16x32_bf16 v[16:19], v[208:211], v[124:127], v[16:19]\n"
"v_mfma_f32_16x16x32_bf16 v[20:23], v[208:211], v[140:143], v[20:23]\n"
"ds_read_b64_tr_b16 v[208:209], v250 offset:0\n"
"ds_read_b64_tr_b16 v[210:211], v250 offset:4096\n"
"s_waitcnt lgkmcnt(12)\n"
"v_mfma_f32_16x16x32_bf16 v[24:27], v[212:215], v[108:111], v[24:27]\n"
"v_mfma_f32_16x16x32_bf16 v[28:31], v[212:215], v[124:127], v[28:31]\n"
"v_mfma_f32_16x16x32_bf16 v[32:35], v[212:215], v[140:143], v[32:35]\n"
"ds_read_b64_tr_b16 v[212:213], v249 offset:512\n"
"ds_read_b64_tr_b16 v[214:215], v249 offset:4608\n"
"v_mfma_f32_16x16x32_bf16 v[36:39], v[216:219], v[108:111], v[36:39]\n"
"v_mfma_f32_16x16x32_bf16 v[40:43], v[216:219], v[124:127], v[40:43]\n"
"v_mfma_f32_16x16x32_bf16 v[44:47], v[216:219], v[140:143], v[44:47]\n"
"ds_read_b64_tr_b16 v[216:217], v250 offset:512\n"
"ds_read_b64_tr_b16 v[218:219], v250 offset:4608\n"
"s_waitcnt lgkmcnt(12)\n"
"v_mfma_f32_16x16x32_bf16 v[48:51], v[220:223], v[108:111], v[48:51]\n"
"v_mfma_f32_16x16x32_bf16 v[52:55], v[220:223], v[124:127], v[52:55]\n"
"v_mfma_f32_16x16x32_bf16 v[56:59], v[220:223], v[140:143], v[56:59]\n"
"ds_read_b64_tr_b16 v[220:221], v249 offset:1024\n"
"ds_read_b64_tr_b16 v[222:223], v249 offset:5120\n"
"v_mfma_f32_16x16x32_bf16 v[60:63], v[224:227], v[108:111], v[60:63]\n"
"v_mfma_f32_16x16x32_bf16 v[64:67], v[224:227], v[124:127], v[64:67]\n"
"v_mfma_f32_16x16x32_bf16 v[68:71], v[224:227], v[140:143], v[68:71]\n"
"ds_read_b64_tr_b16 v[224:225], v250 offset:1024\n"
"ds_read_b64_tr_b16 v[226:227], v250 offset:5120\n"
"s_waitcnt lgkmcnt(12)\n"
"v_mfma_f32_16x16x32_bf16 v[72:75], v[228:231], v[108:111], v[72:75]\n"
"v_mfma_f32_16x16x32_bf16 v[76:79], v[228:231], v[124:127], v[76:79]\n"
"v_mfma_f32_16x16x32_bf16 v[80:83], v[228:231], v[140:143], v[80:83]\n"
"ds_read_b64_tr_b16 v[228:229], v249 offset:1536\n"
"ds_read_b64_tr_b16 v[230:231], v249 offset:5632\n"
"v_mfma_f32_16x16x32_bf16 v[84:87], v[232:235], v[108:111], v[84:87]\n"
"v_mfma_f32_16x16x32_bf16 v[88:91], v[232:235], v[124:127], v[88:91]\n"
"v_mfma_f32_16x16x32_bf16 v[92:95], v[232:235], v[140:143], v[92:95]\n"
"ds_read_b64_tr_b16 v[232:233], v250 offset:1536\n"
"ds_read_b64_tr_b16 v[234:235], v250 offset:5632\n"
"s_barrier\n"
"; step 33\n"
"s_lshl_b32 s62, s58, 2\n"
"s_add_u32 s62, s62, s59\n"
"s_lshl_b32 s62, s62, 3\n"
"s_add_u32 s62, s62, s56\n"
"s_lshl_b32 s62, s62, 6\n"
"s_add_u32 s62, s62, 4352\n"
"v_lshrrev_b32 v242, 1, v253\n"
"v_and_b32 v242, 24, v242\n"
"buffer_load_dwordx2 v[244:245], v242, s[48:51], s62 offen sc1\n"
"s_waitcnt lgkmcnt(12)\n"
"s_waitcnt vmcnt(12)\n"
"v_mfma_f32_16x16x32_bf16 v[0:3], v[204:207], v[144:147], v[0:3]\n"
"s_waitcnt vmcnt(11)\n"
"v_mfma_f32_16x16x32_bf16 v[4:7], v[204:207], v[160:163], v[4:7]\n"
"s_waitcnt vmcnt(10)\n"
"v_mfma_f32_16x16x32_bf16 v[8:11], v[204:207], v[176:179], v[8:11]\n"
"v_add_u32 v249, -73728, v249\n"
"v_add_u32 v250, -73728, v250\n"
"ds_read_b64_tr_b16 v[204:205], v249 offset:24576\n"
"ds_read_b64_tr_b16 v[206:207], v249 offset:28672\n"
"v_mfma_f32_16x16x32_bf16 v[12:15], v[208:211], v[144:147], v[12:15]\n"
"v_mfma_f32_16x16x32_bf16 v[16:19], v[208:211], v[160:163], v[16:19]\n"
"v_mfma_f32_16x16x32_bf16 v[20:23], v[208:211], v[176:179], v[20:23]\n"
"ds_read_b64_tr_b16 v[208:209], v250 offset:24576\n"
"ds_read_b64_tr_b16 v[210:211], v250 offset:28672\n"
"s_waitcnt lgkmcnt(12)\n"
"v_mfma_f32_16x16x32_bf16 v[24:27], v[212:215], v[144:147], v[24:27]\n"
"v_mfma_f32_16x16x32_bf16 v[28:31], v[212:215], v[160:163], v[28:31]\n"
"v_mfma_f32_16x16x32_bf16 v[32:35], v[212:215], v[176:179], v[32:35]\n"
"ds_read_b64_tr_b16 v[212:213], v249 offset:25088\n"
"ds_read_b64_tr_b16 v[214:215], v249 offset:29184\n"
"v_mfma_f32_16x16x32_bf16 v[36:39], v[216:219], v[144:147], v[36:39]\n"
"v_mfma_f32_16x16x32_bf16 v[40:43], v[216:219], v[160:163], v[40:43]\n"
"v_mfma_f32_16x16x32_bf16 v[44:47], v[216:219], v[176:179], v[44:47]\n"
"ds_read_b64_tr_b16 v[216:217], v250 offset:25088\n"
"ds_read_b64_tr_b16 v[218:219], v250 offset:29184\n"
"s_waitcnt lgkmcnt(12)\n"
"v_mfma_f32_16x16x32_bf16 v[48:51], v[220:223], v[144:147], v[48:51]\n"
"v_mfma_f32_16x16x32_bf16 v[52:55], v[220:223], v[160:163], v[52:55]\n"
"v_mfma_f32_16x16x32_bf16 v[56:59], v[220:223], v[176:179], v[56:59]\n"
"ds_read_b64_tr_b16 v[220:221], v249 offset:25600\n"
"ds_read_b64_tr_b16 v[222:223], v249 offset:29696\n"
"v_mfma_f32_16x16x32_bf16 v[60:63], v[224:227], v[144:147], v[60:63]\n"
"v_mfma_f32_16x16x32_bf16 v[64:67], v[224:227], v[160:163], v[64:67]\n"
"v_mfma_f32_16x16x32_bf16 v[68:71], v[224:227], v[176:179], v[68:71]\n"
"ds_read_b64_tr_b16 v[224:225], v250 offset:25600\n"
"ds_read_b64_tr_b16 v[226:227], v250 offset:29696\n"
"s_waitcnt lgkmcnt(12)\n"
"v_mfma_f32_16x16x32_bf16 v[72:75], v[228:231], v[144:147], v[72:75]\n"
"v_mfma_f32_16x16x32_bf16 v[76:79], v[228:231], v[160:163], v[76:79]\n"
"v_mfma_f32_16x16x32_bf16 v[80:83], v[228:231], v[176:179], v[80:83]\n"
"ds_read_b64_tr_b16 v[228:229], v249 offset:26112\n"
"ds_read_b64_tr_b16 v[230:231], v249 offset:30208\n"
"v_mfma_f32_16x16x32_bf16 v[84:87], v[232:235], v[144:147], v[84:87]\n"
"v_mfma_f32_16x16x32_bf16 v[88:91], v[232:235], v[160:163], v[88:91]\n"
"v_mfma_f32_16x16x32_bf16 v[92:95], v[232:235], v[176:179], v[92:95]\n"
"ds_read_b64_tr_b16 v[232:233], v250 offset:26112\n"
"ds_read_b64_tr_b16 v[234:235], v250 offset:30208\n"
"s_waitcnt lgkmcnt(12)\n"
"s_waitcnt vmcnt(9)\n"
"v_mfma_f32_16x16x32_bf16 v[0:3], v[204:207], v[148:151], v[0:3]\n"
"s_waitcnt vmcnt(8)\n"
"v_mfma_f32_16x16x32_bf16 v[4:7], v[204:207], v[164:167], v[4:7]\n"
"s_waitcnt vmcnt(7)\n"
"v_mfma_f32_16x16x32_bf16 v[8:11], v[204:207], v[180:183], v[8:11]\n"
"v_add_u32 v249, 81920, v249\n"
"v_add_u32 v250, 81920, v250\n"
"ds_read_b64_tr_b16 v[204:205], v249 offset:0\n"
"ds_read_b64_tr_b16 v[206:207], v249 offset:4096\n"
"v_mfma_f32_16x16x32_bf16 v[12:15], v[208:211], v[148:151], v[12:15]\n"
"v_mfma_f32_16x16x32_bf16 v[16:19], v[208:211], v[164:167], v[16:19]\n"
"v_mfma_f32_16x16x32_bf16 v[20:23], v[208:211], v[180:183], v[20:23]\n"
"ds_read_b64_tr_b16 v[208:209], v250 offset:0\n"
"ds_read_b64_tr_b16 v[210:211], v250 offset:4096\n"
"s_waitcnt lgkmcnt(12)\n"
"v_mfma_f32_16x16x32_bf16 v[24:27], v[212:215], v[148:151], v[24:27]\n"
"v_mfma_f32_16x16x32_bf16 v[28:31], v[212:215], v[164:167], v[28:31]\n"
"v_mfma_f32_16x16x32_bf16 v[32:35], v[212:215], v[180:183], v[32:35]\n"
"ds_read_b64_tr_b16 v[212:213], v249 offset:512\n"
"ds_read_b64_tr_b16 v[214:215], v249 offset:4608\n"
"v_mfma_f32_16x16x32_bf16 v[36:39], v[216:219], v[148:151], v[36:39]\n"
"v_mfma_f32_16x16x32_bf16 v[40:43], v[216:219], v[164:167], v[40:43]\n"
"v_mfma_f32_16x16x32_bf16 v[44:47], v[216:219], v[180:183], v[44:47]\n"
"ds_read_b64_tr_b16 v[216:217], v250 offset:512\n"
"ds_read_b64_tr_b16 v[218:219], v250 offset:4608\n"
"s_waitcnt lgkmcnt(12)\n"
"v_mfma_f32_16x16x32_bf16 v[48:51], v[220:223], v[148:151], v[48:51]\n"
"v_mfma_f32_16x16x32_bf16 v[52:55], v[220:223], v[164:167], v[52:55]\n"
"v_mfma_f32_16x16x32_bf16 v[56:59], v[220:223], v[180:183], v[56:59]\n"
"ds_read_b64_tr_b16 v[220:221], v249 offset:1024\n"
"ds_read_b64_tr_b16 v[222:223], v249 offset:5120\n"
"v_mfma_f32_16x16x32_bf16 v[60:63], v[224:227], v[148:151], v[60:63]\n"
"v_mfma_f32_16x16x32_bf16 v[64:67], v[224:227], v[164:167], v[64:67]\n"
"v_mfma_f32_16x16x32_bf16 v[68:71], v[224:227], v[180:183], v[68:71]\n"
"ds_read_b64_tr_b16 v[224:225], v250 offset:1024\n"
"ds_read_b64_tr_b16 v[226:227], v250 offset:5120\n"
"s_waitcnt lgkmcnt(12)\n"
"v_mfma_f32_16x16x32_bf16 v[72:75], v[228:231], v[148:151], v[72:75]\n"
"v_mfma_f32_16x16x32_bf16 v[76:79], v[228:231], v[164:167], v[76:79]\n"
"v_mfma_f32_16x16x32_bf16 v[80:83], v[228:231], v[180:183], v[80:83]\n"
"ds_read_b64_tr_b16 v[228:229], v249 offset:1536\n"
"ds_read_b64_tr_b16 v[230:231], v249 offset:5632\n"
"v_mfma_f32_16x16x32_bf16 v[84:87], v[232:235], v[148:151], v[84:87]\n"
"v_mfma_f32_16x16x32_bf16 v[88:91], v[232:235], v[164:167], v[88:91]\n"
"v_mfma_f32_16x16x32_bf16 v[92:95], v[232:235], v[180:183], v[92:95]\n"
"ds_read_b64_tr_b16 v[232:233], v250 offset:1536\n"
"ds_read_b64_tr_b16 v[234:235], v250 offset:5632\n"
"s_mov_b32 s74, 262144\n"
"s_waitcnt vmcnt(0)\n"
"v_cmp_ne_u64 vcc, s[80:81], v[244:245]\n"
"s_cbranch_vccz .Lpolld1\n"
".Lpoll1:\n"
"buffer_load_dwordx2 v[244:245], v242, s[48:51], s62 offen sc1\n"
"s_waitcnt vmcnt(0)\n"
"v_cmp_ne_u64 vcc, s[80:81], v[244:245]\n"
"s_cbranch_vccz .Lpolld1\n"
"s_sleep 2\n"
"s_sub_u32 s74, s74, 1\n"
"s_cbranch_scc0 .Lpoll1\n"
".Lpolld1:\n"
"s_waitcnt lgkmcnt(12)\n"
"v_mfma_f32_16x16x32_bf16 v[0:3], v[204:207], v[152:155], v[0:3]\n"
"v_mfma_f32_16x16x32_bf16 v[4:7], v[204:207], v[168:171], v[4:7]\n"
"s_add_u32 s82, s60, 0\n"
"s_and_b32 s82, s82, 3\n"
"s_sub_u32 s82, s82, s60\n"
"s_mul_i32 s82, s82, 98304\n"
"s_add_u32 s82, s82, s73\n"
"s_add_u32 s82, s82, 49152\n"
"buffer_load_dwordx4 v[96:99], v253, s[44:47], s82 offen sc1\n"
"v_mfma_f32_16x16x32_bf16 v[8:11], v[204:207], v[184:187], v[8:11]\n"
"ds_read_b64_tr_b16 v[204:205], v249 offset:16384\n"
"ds_read_b64_tr_b16 v[206:207], v249 offset:20480\n"
"v_mfma_f32_16x16x32_bf16 v[12:15], v[208:211], v[152:155], v[12:15]\n"
"v_mfma_f32_16x16x32_bf16 v[16:19], v[208:211], v[168:171], v[16:19]\n"
"v_mfma_f32_16x16x32_bf16 v[20:23], v[208:211], v[184:187], v[20:23]\n"
"ds_read_b64_tr_b16 v[208:209], v250 offset:16384\n"
"ds_read_b64_tr_b16 v[210:211], v250 offset:20480\n"
"s_waitcnt lgkmcnt(12)\n"
"v_mfma_f32_16x16x32_bf16 v[24:27], v[212:215], v[152:155], v[24:27]\n"
"v_mfma_f32_16x16x32_bf16 v[28:31], v[212:215], v[168:171], v[28:31]\n"
"v_mfma_f32_16x16x32_bf16 v[32:35], v[212:215], v[184:187], v[32:35]\n"
"ds_read_b64_tr_b16 v[212:213], v249 offset:16896\n"
"ds_read_b64_tr_b16 v[214:215], v249 offset:20992\n"
"v_mfma_f32_16x16x32_bf16 v[36:39], v[216:219], v[152:155], v[36:39]\n"
"v_mfma_f32_16x16x32_bf16 v[40:43], v[216:219], v[168:171], v[40:43]\n"
"s_add_u32 s71, s82, 4096\n"
"buffer_load_dwordx4 v[112:115], v253, s[44:47], s71 offen sc1\n"
"v_mfma_f32_16x16x32_bf16 v[44:47], v[216:219], v[184:187], v[44:47]\n"
"ds_read_b64_tr_b16 v[216:217], v250 offset:16896\n"
"ds_read_b64_tr_b16 v[218:219], v250 offset:20992\n"
"s_waitcnt lgkmcnt(12)\n"
"v_mfma_f32_16x16x32_bf16 v[48:51], v[220:223], v[152:155], v[48:51]\n"
"v_mfma_f32_16x16x32_bf16 v[52:55], v[220:223], v[168:171], v[52:55]\n"
"v_mfma_f32_16x16x32_bf16 v[56:59], v[220:223], v[184:187], v[56:59]\n"
"ds_read_b64_tr_b16 v[220:221], v249 offset:17408\n"
"ds_read_b64_tr_b16 v[222:223], v249 offset:21504\n"
"v_mfma_f32_16x16x32_bf16 v[60:63], v[224:227], v[152:155], v[60:63]\n"
"v_mfma_f32_16x16x32_bf16 v[64:67], v[224:227], v[168:171], v[64:67]\n"
"s_add_u32 s71, s82, 8192\n"
"buffer_load_dwordx4 v[128:131], v253, s[44:47], s71 offen sc1\n"
"v_mfma_f32_16x16x32_bf16 v[68:71], v[224:227], v[184:187], v[68:71]\n"
"ds_read_b64_tr_b16 v[224:225], v250 offset:17408\n"
"ds_read_b64_tr_b16 v[226:227], v250 offset:21504\n"
"s_waitcnt lgkmcnt(12)\n"
"v_mfma_f32_16x16x32_bf16 v[72:75], v[228:231], v[152:155], v[72:75]\n"
"v_mfma_f32_16x16x32_bf16 v[76:79], v[228:231], v[168:171], v[76:79]\n"
"v_mfma_f32_16x16x32_bf16 v[80:83], v[228:231], v[184:187], v[80:83]\n"
"ds_read_b64_tr_b16 v[228:229], v249 offset:17920\n"
"ds_read_b64_tr_b16 v[230:231], v249 offset:22016\n"
"v_mfma_f32_16x16x32_bf16 v[84:87], v[232:235], v[152:155], v[84:87]\n"
"v_mfma_f32_16x16x32_bf16 v[88:91], v[232:235], v[168:171], v[88:91]\n"
"v_mfma_f32_16x16x32_bf16 v[92:95], v[232:235], v[184:187], v[92:95]\n"
"ds_read_b64_tr_b16 v[232:233], v250 offset:17920\n"
"ds_read_b64_tr_b16 v[234:235], v250 offset:22016\n"
"s_waitcnt lgkmcnt(12)\n"
"v_mfma_f32_16x16x32_bf16 v[0:3], v[204:207], v[156:159], v[0:3]\n"
"v_mfma_f32_16x16x32_bf16 v[4:7], v[204:207], v[172:175], v[4:7]\n"
"s_add_u32 s82, s60, 0\n"
"s_and_b32 s82, s82, 3\n"
"s_sub_u32 s82, s82, s60\n"
"s_mul_i32 s82, s82, 98304\n"
"s_add_u32 s82, s82, s73\n"
"s_add_u32 s82, s82, 49152\n"
"buffer_load_dwordx4 v[100:103], v253, s[44:47], s82 offen offset:1024 sc1\n"
"v_mfma_f32_16x16x32_bf16 v[8:11], v[204:207], v[188:191], v[8:11]\n"
"v_add_u32 v249, -81920, v249\n"
"v_add_u32 v250, -81920, v250\n"
"ds_read_b64_tr_b16 v[204:205], v249 offset:32768\n"
"ds_read_b64_tr_b16 v[206:207], v249 offset:36864\n"
"v_mfma_f32_16x16x32_bf16 v[12:15], v[208:211], v[156:159], v[12:15]\n"
"v_mfma_f32_16x16x32_bf16 v[16:19], v[208:211], v[172:175], v[16:19]\n"
"v_mfma_f32_16x16x32_bf16 v[20:23], v[208:211], v[188:191], v[20:23]\n"
"ds_read_b64_tr_b16 v[208:209], v250 offset:32768\n"
"ds_read_b64_tr_b16 v[210:211], v250 offset:36864\n"
"s_waitcnt lgkmcnt(12)\n"
"v_mfma_f32_16x16x32_bf16 v[24:27], v[212:215], v[156:159], v[24:27]\n"
"v_mfma_f32_16x16x32_bf16 v[28:31], v[212:215], v[172:175], v[28:31]\n"
"v_mfma_f32_16x16x32_bf16 v[32:35], v[212:215], v[188:191], v[32:35]\n"
"ds_read_b64_tr_b16 v[212:213], v249 offset:33280\n"
"ds_read_b64_tr_b16 v[214:215], v249 offset:37376\n"
"v_mfma_f32_16x16x32_bf16 v[36:39], v[216:219], v[156:159], v[36:39]\n"
"v_mfma_f32_16x16x32_bf16 v[40:43], v[216:219], v[172:175], v[40:43]\n"
"s_add_u32 s71, s82, 4096\n"
"buffer_load_dwordx4 v[116:119], v253, s[44:47], s71 offen offset:1024 sc1\n"
"v_mfma_f32_16x16x32_bf16 v[44:47], v[216:219], v[188:191], v[44:47]\n"
"ds_read_b64_tr_b16 v[216:217], v250 offset:33280\n"
"ds_read_b64_tr_b16 v[218:219], v250 offset:37376\n"
"s_waitcnt lgkmcnt(12)\n"
"v_mfma_f32_16x16x32_bf16 v[48:51], v[220:223], v[156:159], v[48:51]\n"
"v_mfma_f32_16x16x32_bf16 v[52:55], v[220:223], v[172:175], v[52:55]\n"
"v_mfma_f32_16x16x32_bf16 v[56:59], v[220:223], v[188:191], v[56:59]\n"
"ds_read_b64_tr_b16 v[220:221], v249 offset:33792\n"
"ds_read_b64_tr_b16 v[222:223], v249 offset:37888\n"
"v_mfma_f32_16x16x32_bf16 v[60:63], v[224:227], v[156:159], v[60:63]\n"
"v_mfma_f32_16x16x32_bf16 v[64:67], v[224:227], v[172:175], v[64:67]\n"
"s_add_u32 s71, s82, 8192\n"
"buffer_load_dwordx4 v[132:135], v253, s[44:47], s71 offen offset:1024 sc1\n"
"v_mfma_f32_16x16x32_bf16 v[68:71], v[224:227], v[188:191], v[68:71]\n"
"ds_read_b64_tr_b16 v[224:225], v250 offset:33792\n"
"ds_read_b64_tr_b16 v[226:227], v250 offset:37888\n"
"s_waitcnt lgkmcnt(12)\n"
"v_mfma_f32_16x16x32_bf16 v[72:75], v[228:231], v[156:159], v[72:75]\n"
"v_mfma_f32_16x16x32_bf16 v[76:79], v[228:231], v[172:175], v[76:79]\n"
"v_mfma_f32_16x16x32_bf16 v[80:83], v[228:231], v[188:191], v[80:83]\n"
"ds_read_b64_tr_b16 v[228:229], v249 offset:34304\n"
"ds_read_b64_tr_b16 v[230:231], v249 offset:38400\n"
"v_mfma_f32_16x16x32_bf16 v[84:87], v[232:235], v[156:159], v[84:87]\n"
"v_mfma_f32_16x16x32_bf16 v[88:91], v[232:235], v[172:175], v[88:91]\n"
"v_mfma_f32_16x16x32_bf16 v[92:95], v[232:235], v[188:191], v[92:95]\n"
"ds_read_b64_tr_b16 v[232:233], v250 offset:34304\n"
"ds_read_b64_tr_b16 v[234:235], v250 offset:38400\n"
"s_barrier\n"
"; step 34\n"
"s_waitcnt lgkmcnt(12)\n"
"s_waitcnt vmcnt(5)\n"
"v_mfma_f32_16x16x32_bf16 v[0:3], v[204:207], v[96:99], v[0:3]\n"
"s_waitcnt vmcnt(4)\n"
"v_mfma_f32_16x16x32_bf16 v[4:7], v[204:207], v[112:115], v[4:7]\n"
"s_add_u32 s82, s60, 0\n"
"s_and_b32 s82, s82, 3\n"
"s_sub_u32 s82, s82, s60\n"
"s_mul_i32 s82, s82, 98304\n"
"s_add_u32 s82, s82, s73\n"
"s_add_u32 s82, s82, 49152\n"
"buffer_load_dwordx4 v[104:107], v253, s[44:47], s82 offen offset:2048 sc1\n"
"s_waitcnt vmcnt(4)\n"
"v_mfma_f32_16x16x32_bf16 v[8:11], v[204:207], v[128:131], v[8:11]\n"
"v_add_u32 v249, 90112, v249\n"
"v_add_u32 v250, 90112, v250\n"
"ds_read_b64_tr_b16 v[204:205], v249 offset:0\n"
"ds_read_b64_tr_b16 v[206:207], v249 offset:4096\n"
"v_mfma_f32_16x16x32_bf16 v[12:15], v[208:211], v[96:99], v[12:15]\n"
"v_mfma_f32_16x16x32_bf16 v[16:19], v[208:211], v[112:115], v[16:19]\n"
"v_mfma_f32_16x16x32_bf16 v[20:23], v[208:211], v[128:131], v[20:23]\n"
"ds_read_b64_tr_b16 v[208:209], v250 offset:0\n"
"ds_read_b64_tr_b16 v[210:211], v250 offset:4096\n"
"s_waitcnt lgkmcnt(12)\n"
"v_mfma_f32_16x16x32_bf16 v[24:27], v[212:215], v[96:99], v[24:27]\n"
"v_mfma_f32_16x16x32_bf16 v[28:31], v[212:215], v[112:115], v[28:31]\n"
"v_mfma_f32_16x16x32_bf16 v[32:35], v[212:215], v[128:131], v[32:35]\n"
"ds_read_b64_tr_b16 v[212:213], v249 offset:512\n"
"ds_read_b64_tr_b16 v[214:215], v249 offset:4608\n"
"v_mfma_f32_16x16x32_bf16 v[36:39], v[216:219], v[96:99], v[36:39]\n"
"v_mfma_f32_16x16x32_bf16 v[40:43], v[216:219], v[112:115], v[40:43]\n"
"s_add_u32 s71, s82, 4096\n"
"buffer_load_dwordx4 v[120:123], v253, s[44:47], s71 offen offset:2048 sc1\n"
"v_mfma_f32_16x16x32_bf16 v[44:47], v[216:219], v[128:131], v[44:47]\n"
"ds_read_b64_tr_b16 v[216:217], v250 offset:512\n"
"ds_read_b64_tr_b16 v[218:219], v250 offset:4608\n"
"s_waitcnt lgkmcnt(12)\n"
"v_mfma_f32_16x16x32_bf16 v[48:51], v[220:223], v[96:99], v[48:51]\n"
"v_mfma_f32_16x16x32_bf16 v[52:55], v[220:223], v[112:115], v[52:55]\n"
"v_mfma_f32_16x16x32_bf16 v[56:59], v[220:223], v[128:131], v[56:59]\n"
"ds_read_b64_tr_b16 v[220:221], v249 offset:1024\n"
"ds_read_b64_tr_b16 v[222:223], v249 offset:5120\n"
"v_mfma_f32_16x16x32_bf16 v[60:63], v[224:227], v[96:99], v[60:63]\n"
"v_mfma_f32_16x16x32_bf16 v[64:67], v[224:227], v[112:115], v[64:67]\n"
"s_add_u32 s71, s82, 8192\n"
"buffer_load_dwordx4 v[136:139], v253, s[44:47], s71 offen offset:2048 sc1\n"
"v_mfma_f32_16x16x32_bf16 v[68:71], v[224:227], v[128:131], v[68:71]\n"
"ds_read_b64_tr_b16 v[224:225], v250 offset:1024\n"
"ds_read_b64_tr_b16 v[226:227], v250 offset:5120\n"
"s_waitcnt lgkmcnt(12)\n"
"v_mfma_f32_16x16x32_bf16 v[72:75], v[228:231], v[96:99], v[72:75]\n"
"v_mfma_f32_16x16x32_bf16 v[76:79], v[228:231], v[112:115], v[76:79]\n"
"v_mfma_f32_16x16x32_bf16 v[80:83], v[228:231], v[128:131], v[80:83]\n"
"ds_read_b64_tr_b16 v[228:229], v249 offset:1536\n"
"ds_read_b64_tr_b16 v[230:231], v249 offset:5632\n"
"v_mfma_f32_16x16x32_bf16 v[84:87], v[232:235], v[96:99], v[84:87]\n"
"v_mfma_f32_16x16x32_bf16 v[88:91], v[232:235], v[112:115], v[88:91]\n"
"v_mfma_f32_16x16x32_bf16 v[92:95], v[232:235], v[128:131], v[92:95]\n"
"ds_read_b64_tr_b16 v[232:233], v250 offset:1536\n"
"ds_read_b64_tr_b16 v[234:235], v250 offset:5632\n"
"s_waitcnt lgkmcnt(12)\n"
"s_waitcnt vmcnt(5)\n"
"v_mfma_f32_16x16x32_bf16 v[0:3], v[204:207], v[100:103], v[0:3]\n"
"s_waitcnt vmcnt(4)\n"
"v_mfma_f32_16x16x32_bf16 v[4:7], v[204:207], v[116:119], v[4:7]\n"
"s_add_u32 s82, s60, 0\n"
"s_and_b32 s82, s82, 3\n"
"s_sub_u32 s82, s82, s60\n"
"s_mul_i32 s82, s82, 98304\n"
"s_add_u32 s82, s82, s73\n"
"s_add_u32 s82, s82, 49152\n"
"buffer_load_dwordx4 v[108:111], v253, s[44:47], s82 offen offset:3072 sc1\n"
"s_waitcnt vmcnt(4)\n"
"v_mfma_f32_16x16x32_bf16 v[8:11], v[204:207], v[132:135], v[8:11]\n"
"ds_read_b64_tr_b16 v[204:205], v249 offset:16384\n"
"ds_read_b64_tr_b16 v[206:207], v249 offset:20480\n"
"v_mfma_f32_16x16x32_bf16 v[12:15], v[208:211], v[100:103], v[12:15]\n"
"v_mfma_f32_16x16x32_bf16 v[16:19], v[208:211], v[116:119], v[16:19]\n"
"v_mfma_f32_16x16x32_bf16 v[20:23], v[208:211], v[132:135], v[20:23]\n"
"ds_read_b64_tr_b16 v[208:209], v250 offset:16384\n"
"ds_read_b64_tr_b16 v[210:211], v250 offset:20480\n"
"s_waitcnt lgkmcnt(12)\n"
"v_mfma_f32_16x16x32_bf16 v[24:27], v[212:215], v[100:103], v[24:27]\n"
"v_mfma_f32_16x16x32_bf16 v[28:31], v[212:215], v[116:119], v[28:31]\n"
"v_mfma_f32_16x16x32_bf16 v[32:35], v[212:215], v[132:135], v[32:35]\n"
"ds_read_b64_tr_b16 v[212:213], v249 offset:16896\n"
"ds_read_b64_tr_b16 v[214:215], v249 offset:20992\n"
"v_mfma_f32_16x16x32_bf16 v[36:39], v[216:219], v[100:103], v[36:39]\n"
"v_mfma_f32_16x16x32_bf16 v[40:43], v[216:219], v[116:119], v[40:43]\n"
"s_add_u32 s71, s82, 4096\n"
"buffer_load_dwordx4 v[124:127], v253, s[44:47], s71 offen offset:3072 sc1\n"
"v_mfma_f32_16x16x32_bf16 v[44:47], v[216:219], v[132:135], v[44:47]\n"
"ds_read_b64_tr_b16 v[216:217], v250 offset:16896\n"
"ds_read_b64_tr_b16 v[218:219], v250 offset:20992\n"
"s_waitcnt lgkmcnt(12)\n"
"v_mfma_f32_16x16x32_bf16 v[48:51], v[220:223], v[100:103], v[48:51]\n"
"v_mfma_f32_16x16x32_bf16 v[52:55], v[220:223], v[116:119], v[52:55]\n"
"v_mfma_f32_16x16x32_bf16 v[56:59], v[220:223], v[132:135], v[56:59]\n"
"ds_read_b64_tr_b16 v[220:221], v249 offset:17408\n"
"ds_read_b64_tr_b16 v[222:223], v249 offset:21504\n"
"v_mfma_f32_16x16x32_bf16 v[60:63], v[224:227], v[100:103], v[60:63]\n"
"v_mfma_f32_16x16x32_bf16 v[64:67], v[224:227], v[116:119], v[64:67]\n"
"s_add_u32 s71, s82, 8192\n"
"buffer_load_dwordx4 v[140:143], v253, s[44:47], s71 offen offset:3072 sc1\n"
"v_mfma_f32_16x16x32_bf16 v[68:71], v[224:227], v[132:135], v[68:71]\n"
"ds_read_b64_tr_b16 v[224:225], v250 offset:17408\n"
"ds_read_b64_tr_b16 v[226:227], v250 offset:21504\n"
"s_waitcnt lgkmcnt(12)\n"
"v_mfma_f32_16x16x32_bf16 v[72:75], v[228:231], v[100:103], v[72:75]\n"
"v_mfma_f32_16x16x32_bf16 v[76:79], v[228:231], v[116:119], v[76:79]\n"
"v_mfma_f32_16x16x32_bf16 v[80:83], v[228:231], v[132:135], v[80:83]\n"
"ds_read_b64_tr_b16 v[228:229], v249 offset:17920\n"
"ds_read_b64_tr_b16 v[230:231], v249 offset:22016\n"
"v_mfma_f32_16x16x32_bf16 v[84:87], v[232:235], v[100:103], v[84:87]\n"
"v_mfma_f32_16x16x32_bf16 v[88:91], v[232:235], v[116:119], v[88:91]\n"
"v_mfma_f32_16x16x32_bf16 v[92:95], v[232:235], v[132:135], v[92:95]\n"
"ds_read_b64_tr_b16 v[232:233], v250 offset:17920\n"
"ds_read_b64_tr_b16 v[234:235], v250 offset:22016\n"
"s_waitcnt lgkmcnt(12)\n"
"s_waitcnt vmcnt(5)\n"
"v_mfma_f32_16x16x32_bf16 v[0:3], v[204:207], v[104:107], v[0:3]\n"
"s_waitcnt vmcnt(4)\n"
"v_mfma_f32_16x16x32_bf16 v[4:7], v[204:207], v[120:123], v[4:7]\n"
"s_cmp_le_u32 s60, 0\n"
"s_cselect_b32 s82, 1, 0\n"
"s_sub_u32 s82, s82, s60\n"
"s_mul_i32 s82, s82, 98304\n"
"s_add_u32 s82, s82, s73\n"
"s_add_u32 s82, s82, 49152\n"
"buffer_load_dwordx4 v[144:147], v253, s[44:47], s82 offen sc1\n"
"s_waitcnt vmcnt(4)\n"
"v_mfma_f32_16x16x32_bf16 v[8:11], v[204:207], v[136:139], v[8:11]\n"
"v_add_u32 v249, -90112, v249\n"
"v_add_u32 v250, -90112, v250\n"
"ds_read_b64_tr_b16 v[204:205], v249 offset:40960\n"
"ds_read_b64_tr_b16 v[206:207], v249 offset:45056\n"
"v_mfma_f32_16x16x32_bf16 v[12:15], v[208:211], v[104:107], v[12:15]\n"
"v_mfma_f32_16x16x32_bf16 v[16:19], v[208:211], v[120:123], v[16:19]\n"
"v_mfma_f32_16x16x32_bf16 v[20:23], v[208:211], v[136:139], v[20:23]\n"
"ds_read_b64_tr_b16 v[208:209], v250 offset:40960\n"
"ds_read_b64_tr_b16 v[210:211], v250 offset:45056\n"
"s_waitcnt lgkmcnt(12)\n"
"v_mfma_f32_16x16x32_bf16 v[24:27], v[212:215], v[104:107], v[24:27]\n"
"v_mfma_f32_16x16x32_bf16 v[28:31], v[212:215], v[120:123], v[28:31]\n"
"v_mfma_f32_16x16x32_bf16 v[32:35], v[212:215], v[136:139], v[32:35]\n"
"ds_read_b64_tr_b16 v[212:213], v249 offset:41472\n"
"ds_read_b64_tr_b16 v[214:215], v249 offset:45568\n"
"v_mfma_f32_16x16x32_bf16 v[36:39], v[216:219], v[104:107], v[36:39]\n"
"v_mfma_f32_16x16x32_bf16 v[40:43], v[216:219], v[120:123], v[40:43]\n"
"s_add_u32 s71, s82, 4096\n"
"buffer_load_dwordx4 v[160:163], v253, s[44:47], s71 offen sc1\n"
"v_mfma_f32_16x16x32_bf16 v[44:47], v[216:219], v[136:139], v[44:47]\n"
"ds_read_b64_tr_b16 v[216:217], v250 offset:41472\n"
"ds_read_b64_tr_b16 v[218:219], v250 offset:45568\n"
"s_waitcnt lgkmcnt(12)\n"
"v_mfma_f32_16x16x32_bf16 v[48:51], v[220:223], v[104:107], v[48:51]\n"
"v_mfma_f32_16x16x32_bf16 v[52:55], v[220:223], v[120:123], v[52:55]\n"
"v_mfma_f32_16x16x32_bf16 v[56:59], v[220:223], v[136:139], v[56:59]\n"
"ds_read_b64_tr_b16 v[220:221], v249 offset:41984\n"
"ds_read_b64_tr_b16 v[222:223], v249 offset:46080\n"
"v_mfma_f32_16x16x32_bf16 v[60:63], v[224:227], v[104:107], v[60:63]\n"
"v_mfma_f32_16x16x32_bf16 v[64:67], v[224:227], v[120:123], v[64:67]\n"
"s_add_u32 s71, s82, 8192\n"
"buffer_load_dwordx4 v[176:179], v253, s[44:47], s71 offen sc1\n"
"v_mfma_f32_16x16x32_bf16 v[68:71], v[224:227], v[136:139], v[68:71]\n"
"ds_read_b64_tr_b16 v[224:225], v250 offset:41984\n"
"ds_read_b64_tr_b16 v[226:227], v250 offset:46080\n"
"s_waitcnt lgkmcnt(12)\n"
"v_mfma_f32_16x16x32_bf16 v[72:75], v[228:231], v[104:107], v[72:75]\n"
"v_mfma_f32_16x16x32_bf16 v[76:79], v[228:231], v[120:123], v[76:79]\n"
"v_mfma_f32_16x16x32_bf16 v[80:83], v[228:231], v[136:139], v[80:83]\n"
"ds_read_b64_tr_b16 v[228:229], v249 offset:42496\n"
"ds_read_b64_tr_b16 v[230:231], v249 offset:46592\n"
"v_mfma_f32_16x16x32_bf16 v[84:87], v[232:235], v[104:107], v[84:87]\n"
"v_mfma_f32_16x16x32_bf16 v[88:91], v[232:235], v[120:123], v[88:91]\n"
"v_mfma_f32_16x16x32_bf16 v[92:95], v[232:235], v[136:139], v[92:95]\n"
"ds_read_b64_tr_b16 v[232:233], v250 offset:42496\n"
"ds_read_b64_tr_b16 v[234:235], v250 offset:46592\n"
"s_waitcnt lgkmcnt(12)\n"
"s_waitcnt vmcnt(5)\n"
"v_mfma_f32_16x16x32_bf16 v[0:3], v[204:207], v[108:111], v[0:3]\n"
"s_waitcnt vmcnt(4)\n"
"v_mfma_f32_16x16x32_bf16 v[4:7], v[204:207], v[124:127], v[4:7]\n"
"s_cmp_le_u32 s60, 0\n"
"s_cselect_b32 s82, 1, 0\n"
"s_sub_u32 s82, s82, s60\n"
"s_mul_i32 s82, s82, 98304\n"
"s_add_u32 s82, s82, s73\n"
"s_add_u32 s82, s82, 49152\n"
"buffer_load_dwordx4 v[148:151], v253, s[44:47], s82 offen offset:1024 sc1\n"
"s_waitcnt vmcnt(4)\n"
"v_mfma_f32_16x16x32_bf16 v[8:11], v[204:207], v[140:143], v[8:11]\n"
"ds_read_b64_tr_b16 v[204:205], v249 offset:49152\n"
"ds_read_b64_tr_b16 v[206:207], v249 offset:53248\n"
"v_mfma_f32_16x16x32_bf16 v[12:15], v[208:211], v[108:111], v[12:15]\n"
"v_mfma_f32_16x16x32_bf16 v[16:19], v[208:211], v[124:127], v[16:19]\n"
"v_mfma_f32_16x16x32_bf16 v[20:23], v[208:211], v[140:143], v[20:23]\n"
"ds_read_b64_tr_b16 v[208:209], v250 offset:49152\n"
"ds_read_b64_tr_b16 v[210:211], v250 offset:53248\n"
"s_waitcnt lgkmcnt(12)\n"
"v_mfma_f32_16x16x32_bf16 v[24:27], v[212:215], v[108:111], v[24:27]\n"
"v_mfma_f32_16x16x32_bf16 v[28:31], v[212:215], v[124:127], v[28:31]\n"
"v_mfma_f32_16x16x32_bf16 v[32:35], v[212:215], v[140:143], v[32:35]\n"
"ds_read_b64_tr_b16 v[212:213], v249 offset:49664\n"
"ds_read_b64_tr_b16 v[214:215], v249 offset:53760\n"
"v_mfma_f32_16x16x32_bf16 v[36:39], v[216:219], v[108:111], v[36:39]\n"
"v_mfma_f32_16x16x32_bf16 v[40:43], v[216:219], v[124:127], v[40:43]\n"
"s_add_u32 s71, s82, 4096\n"
"buffer_load_dwordx4 v[164:167], v253, s[44:47], s71 offen offset:1024 sc1\n"
"v_mfma_f32_16x16x32_bf16 v[44:47], v[216:219], v[140:143], v[44:47]\n"
"ds_read_b64_tr_b16 v[216:217], v250 offset:49664\n"
"ds_read_b64_tr_b16 v[218:219], v250 offset:53760\n"
"s_waitcnt lgkmcnt(12)\n"
"v_mfma_f32_16x16x32_bf16 v[48:51], v[220:223], v[108:111], v[48:51]\n"
"v_mfma_f32_16x16x32_bf16 v[52:55], v[220:223], v[124:127], v[52:55]\n"
"v_mfma_f32_16x16x32_bf16 v[56:59], v[220:223], v[140:143], v[56:59]\n"
"ds_read_b64_tr_b16 v[220:221], v249 offset:50176\n"
"ds_read_b64_tr_b16 v[222:223], v249 offset:54272\n"
"v_mfma_f32_16x16x32_bf16 v[60:63], v[224:227], v[108:111], v[60:63]\n"
"v_mfma_f32_16x16x32_bf16 v[64:67], v[224:227], v[124:127], v[64:67]\n"
"s_add_u32 s71, s82, 8192\n"
"buffer_load_dwordx4 v[180:183], v253, s[44:47], s71 offen offset:1024 sc1\n"
"v_mfma_f32_16x16x32_bf16 v[68:71], v[224:227], v[140:143], v[68:71]\n"
"ds_read_b64_tr_b16 v[224:225], v250 offset:50176\n"
"ds_read_b64_tr_b16 v[226:227], v250 offset:54272\n"
"s_waitcnt lgkmcnt(12)\n"
"v_mfma_f32_16x16x32_bf16 v[72:75], v[228:231], v[108:111], v[72:75]\n"
"v_mfma_f32_16x16x32_bf16 v[76:79], v[228:231], v[124:127], v[76:79]\n"
"v_mfma_f32_16x16x32_bf16 v[80:83], v[228:231], v[140:143], v[80:83]\n"
"ds_read_b64_tr_b16 v[228:229], v249 offset:50688\n"
"ds_read_b64_tr_b16 v[230:231], v249 offset:54784\n"
"v_mfma_f32_16x16x32_bf16 v[84:87], v[232:235], v[108:111], v[84:87]\n"
"v_mfma_f32_16x16x32_bf16 v[88:91], v[232:235], v[124:127], v[88:91]\n"
"v_mfma_f32_16x16x32_bf16 v[92:95], v[232:235], v[140:143], v[92:95]\n"
"ds_read_b64_tr_b16 v[232:233], v250 offset:50688\n"
"ds_read_b64_tr_b16 v[234:235], v250 offset:54784\n"
"s_barrier\n"
"; step 35\n"
"s_waitcnt lgkmcnt(12)\n"
"s_waitcnt vmcnt(5)\n"
"v_mfma_f32_16x16x32_bf16 v[0:3], v[204:207], v[144:147], v[0:3]\n"
"s_waitcnt vmcnt(4)\n"
"v_mfma_f32_16x16x32_bf16 v[4:7], v[204:207], v[160:163], v[4:7]\n"
"s_cmp_le_u32 s60, 0\n"
"s_cselect_b32 s82, 1, 0\n"
"s_sub_u32 s82, s82, s60\n"
"s_mul_i32 s82, s82, 98304\n"
"s_add_u32 s82, s82, s73\n"
"s_add_u32 s82, s82, 49152\n"
"buffer_load_dwordx4 v[152:155], v253, s[44:47], s82 offen offset:2048 sc1\n"
"s_waitcnt vmcnt(4)\n"
"v_mfma_f32_16x16x32_bf16 v[8:11], v[204:207], v[176:179], v[8:11]\n"
"v_add_u32 v249, 98304, v249\n"
"v_add_u32 v250, 98304, v250\n"
"ds_read_b64_tr_b16 v[204:205], v249 offset:16384\n"
"ds_read_b64_tr_b16 v[206:207], v249 offset:20480\n"
"v_mfma_f32_16x16x32_bf16 v[12:15], v[208:211], v[144:147], v[12:15]\n"
"v_mfma_f32_16x16x32_bf16 v[16:19], v[208:211], v[160:163], v[16:19]\n"
"v_mfma_f32_16x16x32_bf16 v[20:23], v[208:211], v[176:179], v[20:23]\n"
"ds_read_b64_tr_b16 v[208:209], v250 offset:16384\n"
"ds_read_b64_tr_b16 v[210:211], v250 offset:20480\n"
"s_waitcnt lgkmcnt(12)\n"
"v_mfma_f32_16x16x32_bf16 v[24:27], v[212:215], v[144:147], v[24:27]\n"
"v_mfma_f32_16x16x32_bf16 v[28:31], v[212:215], v[160:163], v[28:31]\n"
"v_mfma_f32_16x16x32_bf16 v[32:35], v[212:215], v[176:179], v[32:35]\n"
"ds_read_b64_tr_b16 v[212:213], v249 offset:16896\n"
"ds_read_b64_tr_b16 v[214:215], v249 offset:20992\n"
"v_mfma_f32_16x16x32_bf16 v[36:39], v[216:219], v[144:147], v[36:39]\n"
"v_mfma_f32_16x16x32_bf16 v[40:43], v[216:219], v[160:163], v[40:43]\n"
"s_add_u32 s71, s82, 4096\n"
"buffer_load_dwordx4 v[168:171], v253, s[44:47], s71 offen offset:2048 sc1\n"
"v_mfma_f32_16x16x32_bf16 v[44:47], v[216:219], v[176:179], v[44:47]\n"
"ds_read_b64_tr_b16 v[216:217], v250 offset:16896\n"
"ds_read_b64_tr_b16 v[218:219], v250 offset:20992\n"
"s_waitcnt lgkmcnt(12)\n"
"v_mfma_f32_16x16x32_bf16 v[48:51], v[220:223], v[144:147], v[48:51]\n"
"v_mfma_f32_16x16x32_bf16 v[52:55], v[220:223], v[160:163], v[52:55]\n"
"v_mfma_f32_16x16x32_bf16 v[56:59], v[220:223], v[176:179], v[56:59]\n"
"ds_read_b64_tr_b16 v[220:221], v249 offset:17408\n"
"ds_read_b64_tr_b16 v[222:223], v249 offset:21504\n"
"v_mfma_f32_16x16x32_bf16 v[60:63], v[224:227], v[144:147], v[60:63]\n"
"v_mfma_f32_16x16x32_bf16 v[64:67], v[224:227], v[160:163], v[64:67]\n"
"s_add_u32 s71, s82, 8192\n"
"buffer_load_dwordx4 v[184:187], v253, s[44:47], s71 offen offset:2048 sc1\n"
"v_mfma_f32_16x16x32_bf16 v[68:71], v[224:227], v[176:179], v[68:71]\n"
"ds_read_b64_tr_b16 v[224:225], v250 offset:17408\n"
"ds_read_b64_tr_b16 v[226:227], v250 offset:21504\n"
"s_waitcnt lgkmcnt(12)\n"
"v_mfma_f32_16x16x32_bf16 v[72:75], v[228:231], v[144:147], v[72:75]\n"
"v_mfma_f32_16x16x32_bf16 v[76:79], v[228:231], v[160:163], v[76:79]\n"
"v_mfma_f32_16x16x32_bf16 v[80:83], v[228:231], v[176:179], v[80:83]\n"
"ds_read_b64_tr_b16 v[228:229], v249 offset:17920\n"
"ds_read_b64_tr_b16 v[230:231], v249 offset:22016\n"
"v_mfma_f32_16x16x32_bf16 v[84:87], v[232:235], v[144:147], v[84:87]\n"
"v_mfma_f32_16x16x32_bf16 v[88:91], v[232:235], v[160:163], v[88:91]\n"
"v_mfma_f32_16x16x32_bf16 v[92:95], v[232:235], v[176:179], v[92:95]\n"
"ds_read_b64_tr_b16 v[232:233], v250 offset:17920\n"
"ds_read_b64_tr_b16 v[234:235], v250 offset:22016\n"
"s_waitcnt lgkmcnt(12)\n"
"s_waitcnt vmcnt(5)\n"
"v_mfma_f32_16x16x32_bf16 v[0:3], v[204:207], v[148:151], v[0:3]\n"
"s_waitcnt vmcnt(4)\n"
"v_mfma_f32_16x16x32_bf16 v[4:7], v[204:207], v[164:167], v[4:7]\n"
"s_cmp_le_u32 s60, 0\n"
"s_cselect_b32 s82, 1, 0\n"
"s_sub_u32 s82, s82, s60\n"
"s_mul_i32 s82, s82, 98304\n"
"s_add_u32 s82, s82, s73\n"
"s_add_u32 s82, s82, 49152\n"
"buffer_load_dwordx4 v[156:159], v253, s[44:47], s82 offen offset:3072 sc1\n"
"s_waitcnt vmcnt(4)\n"
"v_mfma_f32_16x16x32_bf16 v[8:11], v[204:207], v[180:183], v[8:11]\n"
"v_add_u32 v249, -98304, v249\n"
"v_add_u32 v250, -98304, v250\n"
"ds_read_b64_tr_b16 v[204:205], v249 offset:57344\n"
"ds_read_b64_tr_b16 v[206:207], v249 offset:61440\n"
"v_mfma_f32_16x16x32_bf16 v[12:15], v[208:211], v[148:151], v[12:15]\n"
"v_mfma_f32_16x16x32_bf16 v[16:19], v[208:211], v[164:167], v[16:19]\n"
"v_mfma_f32_16x16x32_bf16 v[20:23], v[208:211], v[180:183], v[20:23]\n"
"ds_read_b64_tr_b16 v[208:209], v250 offset:57344\n"
"ds_read_b64_tr_b16 v[210:211], v250 offset:61440\n"
"s_waitcnt lgkmcnt(12)\n"
"v_mfma_f32_16x16x32_bf16 v[24:27], v[212:215], v[148:151], v[24:27]\n"
"v_mfma_f32_16x16x32_bf16 v[28:31], v[212:215], v[164:167], v[28:31]\n"
"v_mfma_f32_16x16x32_bf16 v[32:35], v[212:215], v[180:183], v[32:35]\n"
"ds_read_b64_tr_b16 v[212:213], v249 offset:57856\n"
"ds_read_b64_tr_b16 v[214:215], v249 offset:61952\n"
"v_mfma_f32_16x16x32_bf16 v[36:39], v[216:219], v[148:151], v[36:39]\n"
"v_mfma_f32_16x16x32_bf16 v[40:43], v[216:219], v[164:167], v[40:43]\n"
"s_add_u32 s71, s82, 4096\n"
"buffer_load_dwordx4 v[172:175], v253, s[44:47], s71 offen offset:3072 sc1\n"
"v_mfma_f32_16x16x32_bf16 v[44:47], v[216:219], v[180:183], v[44:47]\n"
"ds_read_b64_tr_b16 v[216:217], v250 offset:57856\n"
"ds_read_b64_tr_b16 v[218:219], v250 offset:61952\n"
"s_waitcnt lgkmcnt(12)\n"
"v_mfma_f32_16x16x32_bf16 v[48:51], v[220:223], v[148:151], v[48:51]\n"
"v_mfma_f32_16x16x32_bf16 v[52:55], v[220:223], v[164:167], v[52:55]\n"
"v_mfma_f32_16x16x32_bf16 v[56:59], v[220:223], v[180:183], v[56:59]\n"
"ds_read_b64_tr_b16 v[220:221], v249 offset:58368\n"
"ds_read_b64_tr_b16 v[222:223], v249 offset:62464\n"
"v_mfma_f32_16x16x32_bf16 v[60:63], v[224:227], v[148:151], v[60:63]\n"
"v_mfma_f32_16x16x32_bf16 v[64:67], v[224:227], v[164:167], v[64:67]\n"
"s_add_u32 s71, s82, 8192\n"
"buffer_load_dwordx4 v[188:191], v253, s[44:47], s71 offen offset:3072 sc1\n"
"v_mfma_f32_16x16x32_bf16 v[68:71], v[224:227], v[180:183], v[68:71]\n"
"ds_read_b64_tr_b16 v[224:225], v250 offset:58368\n"
"ds_read_b64_tr_b16 v[226:227], v250 offset:62464\n"
"s_waitcnt lgkmcnt(12)\n"
"v_mfma_f32_16x16x32_bf16 v[72:75], v[228:231], v[148:151], v[72:75]\n"
"v_mfma_f32_16x16x32_bf16 v[76:79], v[228:231], v[164:167], v[76:79]\n"
"v_mfma_f32_16x16x32_bf16 v[80:83], v[228:231], v[180:183], v[80:83]\n"
"ds_read_b64_tr_b16 v[228:229], v249 offset:58880\n"
"ds_read_b64_tr_b16 v[230:231], v249 offset:62976\n"
"v_mfma_f32_16x16x32_bf16 v[84:87], v[232:235], v[148:151], v[84:87]\n"
"v_mfma_f32_16x16x32_bf16 v[88:91], v[232:235], v[164:167], v[88:91]\n"
"v_mfma_f32_16x16x32_bf16 v[92:95], v[232:235], v[180:183], v[92:95]\n"
"ds_read_b64_tr_b16 v[232:233], v250 offset:58880\n"
"ds_read_b64_tr_b16 v[234:235], v250 offset:62976\n"
"s_waitcnt lgkmcnt(12)\n"
"s_waitcnt vmcnt(5)\n"
"v_mfma_f32_16x16x32_bf16 v[0:3], v[204:207], v[152:155], v[0:3]\n"
"s_waitcnt vmcnt(4)\n"
"v_mfma_f32_16x16x32_bf16 v[4:7], v[204:207], v[168:171], v[4:7]\n"
"s_cmp_le_u32 s60, 1\n"
"s_cselect_b32 s82, 2, 1\n"
"s_sub_u32 s82, s82, s60\n"
"s_mul_i32 s82, s82, 98304\n"
"s_add_u32 s82, s82, s73\n"
"s_add_u32 s82, s82, 49152\n"
"buffer_load_dwordx4 v[96:99], v253, s[44:47], s82 offen sc1\n"
"s_waitcnt vmcnt(4)\n"
"v_mfma_f32_16x16x32_bf16 v[8:11], v[204:207], v[184:187], v[8:11]\n"
"v_add_u32 v249, 98304, v249\n"
"v_add_u32 v250, 98304, v250\n"
"ds_read_b64_tr_b16 v[204:205], v249 offset:24576\n"
"ds_read_b64_tr_b16 v[206:207], v249 offset:28672\n"
"v_mfma_f32_16x16x32_bf16 v[12:15], v[208:211], v[152:155], v[12:15]\n"
"v_mfma_f32_16x16x32_bf16 v[16:19], v[208:211], v[168:171], v[16:19]\n"
"v_mfma_f32_16x16x32_bf16 v[20:23], v[208:211], v[184:187], v[20:23]\n"
"ds_read_b64_tr_b16 v[208:209], v250 offset:24576\n"
"ds_read_b64_tr_b16 v[210:211], v250 offset:28672\n"
"s_waitcnt lgkmcnt(12)\n"
"v_mfma_f32_16x16x32_bf16 v[24:27], v[212:215], v[152:155], v[24:27]\n"
"v_mfma_f32_16x16x32_bf16 v[28:31], v[212:215], v[168:171], v[28:31]\n"
"v_mfma_f32_16x16x32_bf16 v[32:35], v[212:215], v[184:187], v[32:35]\n"
"ds_read_b64_tr_b16 v[212:213], v249 offset:25088\n"
"ds_read_b64_tr_b16 v[214:215], v249 offset:29184\n"
"v_mfma_f32_16x16x32_bf16 v[36:39], v[216:219], v[152:155], v[36:39]\n"
"v_mfma_f32_16x16x32_bf16 v[40:43], v[216:219], v[168:171], v[40:43]\n"
"s_add_u32 s71, s82, 4096\n"
"buffer_load_dwordx4 v[112:115], v253, s[44:47], s71 offen sc1\n"
"v_mfma_f32_16x16x32_bf16 v[44:47], v[216:219], v[184:187], v[44:47]\n"
"ds_read_b64_tr_b16 v[216:217], v250 offset:25088\n"
"ds_read_b64_tr_b16 v[218:219], v250 offset:29184\n"
"s_waitcnt lgkmcnt(12)\n"
"v_mfma_f32_16x16x32_bf16 v[48:51], v[220:223], v[152:155], v[48:51]\n"
"v_mfma_f32_16x16x32_bf16 v[52:55], v[220:223], v[168:171], v[52:55]\n"
"v_mfma_f32_16x16x32_bf16 v[56:59], v[220:223], v[184:187], v[56:59]\n"
"ds_read_b64_tr_b16 v[220:221], v249 offset:25600\n"
"ds_read_b64_tr_b16 v[222:223], v249 offset:29696\n"
"v_mfma_f32_16x16x32_bf16 v[60:63], v[224:227], v[152:155], v[60:63]\n"
"v_mfma_f32_16x16x32_bf16 v[64:67], v[224:227], v[168:171], v[64:67]\n"
"s_add_u32 s71, s82, 8192\n"
"buffer_load_dwordx4 v[128:131], v253, s[44:47], s71 offen sc1\n"
"v_mfma_f32_16x16x32_bf16 v[68:71], v[224:227], v[184:187], v[68:71]\n"
"ds_read_b64_tr_b16 v[224:225], v250 offset:25600\n"
"ds_read_b64_tr_b16 v[226:227], v250 offset:29696\n"
"s_waitcnt lgkmcnt(12)\n"
"v_mfma_f32_16x16x32_bf16 v[72:75], v[228:231], v[152:155], v[72:75]\n"
"v_mfma_f32_16x16x32_bf16 v[76:79], v[228:231], v[168:171], v[76:79]\n"
"v_mfma_f32_16x16x32_bf16 v[80:83], v[228:231], v[184:187], v[80:83]\n"
"ds_read_b64_tr_b16 v[228:229], v249 offset:26112\n"
"ds_read_b64_tr_b16 v[230:231], v249 offset:30208\n"
"v_mfma_f32_16x16x32_bf16 v[84:87], v[232:235], v[152:155], v[84:87]\n"
"v_mfma_f32_16x16x32_bf16 v[88:91], v[232:235], v[168:171], v[88:91]\n"
"v_mfma_f32_16x16x32_bf16 v[92:95], v[232:235], v[184:187], v[92:95]\n"
"ds_read_b64_tr_b16 v[232:233], v250 offset:26112\n"
"ds_read_b64_tr_b16 v[234:235], v250 offset:30208\n"
"s_waitcnt lgkmcnt(12)\n"
"s_waitcnt vmcnt(5)\n"
"v_mfma_f32_16x16x32_bf16 v[0:3], v[204:207], v[156:159], v[0:3]\n"
"s_waitcnt vmcnt(4)\n"
"v_mfma_f32_16x16x32_bf16 v[4:7], v[204:207], v[172:175], v[4:7]\n"
"s_cmp_le_u32 s60, 1\n"
"s_cselect_b32 s82, 2, 1\n"
"s_sub_u32 s82, s82, s60\n"
"s_mul_i32 s82, s82, 98304\n"
"s_add_u32 s82, s82, s73\n"
"s_add_u32 s82, s82, 49152\n"
"buffer_load_dwordx4 v[100:103], v253, s[44:47], s82 offen offset:1024 sc1\n"
"s_waitcnt vmcnt(4)\n"
"v_mfma_f32_16x16x32_bf16 v[8:11], v[204:207], v[188:191], v[8:11]\n"
"v_add_u32 v249, -98304, v249\n"
"v_add_u32 v250, -98304, v250\n"
"ds_read_b64_tr_b16 v[204:205], v249 offset:0\n"
"ds_read_b64_tr_b16 v[206:207], v249 offset:4096\n"
"v_mfma_f32_16x16x32_bf16 v[12:15], v[208:211], v[156:159], v[12:15]\n"
"v_mfma_f32_16x16x32_bf16 v[16:19], v[208:211], v[172:175], v[16:19]\n"
"v_mfma_f32_16x16x32_bf16 v[20:23], v[208:211], v[188:191], v[20:23]\n"
"ds_read_b64_tr_b16 v[208:209], v250 offset:0\n"
"ds_read_b64_tr_b16 v[210:211], v250 offset:4096\n"
"s_waitcnt lgkmcnt(12)\n"
"v_mfma_f32_16x16x32_bf16 v[24:27], v[212:215], v[156:159], v[24:27]\n"
"v_mfma_f32_16x16x32_bf16 v[28:31], v[212:215], v[172:175], v[28:31]\n"
"v_mfma_f32_16x16x32_bf16 v[32:35], v[212:215], v[188:191], v[32:35]\n"
"ds_read_b64_tr_b16 v[212:213], v249 offset:512\n"
"ds_read_b64_tr_b16 v[214:215], v249 offset:4608\n"
"v_mfma_f32_16x16x32_bf16 v[36:39], v[216:219], v[156:159], v[36:39]\n"
"v_mfma_f32_16x16x32_bf16 v[40:43], v[216:219], v[172:175], v[40:43]\n"
"s_add_u32 s71, s82, 4096\n"
"buffer_load_dwordx4 v[116:119], v253, s[44:47], s71 offen offset:1024 sc1\n"
"v_mfma_f32_16x16x32_bf16 v[44:47], v[216:219], v[188:191], v[44:47]\n"
"ds_read_b64_tr_b16 v[216:217], v250 offset:512\n"
"ds_read_b64_tr_b16 v[218:219], v250 offset:4608\n"
"s_waitcnt lgkmcnt(12)\n"
"v_mfma_f32_16x16x32_bf16 v[48:51], v[220:223], v[156:159], v[48:51]\n"
"v_mfma_f32_16x16x32_bf16 v[52:55], v[220:223], v[172:175], v[52:55]\n"
"v_mfma_f32_16x16x32_bf16 v[56:59], v[220:223], v[188:191], v[56:59]\n"
"ds_read_b64_tr_b16 v[220:221], v249 offset:1024\n"
"ds_read_b64_tr_b16 v[222:223], v249 offset:5120\n"
"v_mfma_f32_16x16x32_bf16 v[60:63], v[224:227], v[156:159], v[60:63]\n"
"v_mfma_f32_16x16x32_bf16 v[64:67], v[224:227], v[172:175], v[64:67]\n"
"s_add_u32 s71, s82, 8192\n"
"buffer_load_dwordx4 v[132:135], v253, s[44:47], s71 offen offset:1024 sc1\n"
"v_mfma_f32_16x16x32_bf16 v[68:71], v[224:227], v[188:191], v[68:71]\n"
"ds_read_b64_tr_b16 v[224:225], v250 offset:1024\n"
"ds_read_b64_tr_b16 v[226:227], v250 offset:5120\n"
"s_waitcnt lgkmcnt(12)\n"
"v_mfma_f32_16x16x32_bf16 v[72:75], v[228:231], v[156:159], v[72:75]\n"
"v_mfma_f32_16x16x32_bf16 v[76:79], v[228:231], v[172:175], v[76:79]\n"
"v_mfma_f32_16x16x32_bf16 v[80:83], v[228:231], v[188:191], v[80:83]\n"
"ds_read_b64_tr_b16 v[228:229], v249 offset:1536\n"
"ds_read_b64_tr_b16 v[230:231], v249 offset:5632\n"
"v_mfma_f32_16x16x32_bf16 v[84:87], v[232:235], v[156:159], v[84:87]\n"
"v_mfma_f32_16x16x32_bf16 v[88:91], v[232:235], v[172:175], v[88:91]\n"
"v_mfma_f32_16x16x32_bf16 v[92:95], v[232:235], v[188:191], v[92:95]\n"
"ds_read_b64_tr_b16 v[232:233], v250 offset:1536\n"
"ds_read_b64_tr_b16 v[234:235], v250 offset:5632\n"
"s_barrier\n"
"; step 36\n"
"s_waitcnt lgkmcnt(12)\n"
"s_waitcnt vmcnt(5)\n"
"v_mfma_f32_16x16x32_bf16 v[0:3], v[204:207], v[96:99], v[0:3]\n"
"s_waitcnt vmcnt(4)\n"
"v_mfma_f32_16x16x32_bf16 v[4:7], v[204:207], v[112:115], v[4:7]\n"
"s_cmp_le_u32 s60, 1\n"
"s_cselect_b32 s82, 2, 1\n"
"s_sub_u32 s82, s82, s60\n"
"s_mul_i32 s82, s82, 98304\n"
"s_add_u32 s82, s82, s73\n"
"s_add_u32 s82, s82, 49152\n"
"buffer_load_dwordx4 v[104:107], v253, s[44:47], s82 offen offset:2048 sc1\n"
"s_waitcnt vmcnt(4)\n"
"v_mfma_f32_16x16x32_bf16 v[8:11], v[204:207], v[128:131], v[8:11]\n"
"ds_read_b64_tr_b16 v[204:205], v249 offset:8192\n"
"ds_read_b64_tr_b16 v[206:207], v249 offset:12288\n"
"v_mfma_f32_16x16x32_bf16 v[12:15], v[208:211], v[96:99], v[12:15]\n"
"v_mfma_f32_16x16x32_bf16 v[16:19], v[208:211], v[112:115], v[16:19]\n"
"v_mfma_f32_16x16x32_bf16 v[20:23], v[208:211], v[128:131], v[20:23]\n"
"ds_read_b64_tr_b16 v[208:209], v250 offset:8192\n"
"ds_read_b64_tr_b16 v[210:211], v250 offset:12288\n"
"s_waitcnt lgkmcnt(12)\n"
"v_mfma_f32_16x16x32_bf16 v[24:27], v[212:215], v[96:99], v[24:27]\n"
"v_mfma_f32_16x16x32_bf16 v[28:31], v[212:215], v[112:115], v[28:31]\n"
"s_add_u32 s71, s82, 4096\n"
"buffer_load_dwordx4 v[120:123], v253, s[44:47], s71 offen offset:2048 sc1\n"
"v_mfma_f32_16x16x32_bf16 v[32:35], v[212:215], v[128:131], v[32:35]\n"
"ds_read_b64_tr_b16 v[212:213], v249 offset:8704\n"
"ds_read_b64_tr_b16 v[214:215], v249 offset:12800\n"
"v_mfma_f32_16x16x32_bf16 v[36:39], v[216:219], v[96:99], v[36:39]\n"
"v_mfma_f32_16x16x32_bf16 v[40:43], v[216:219], v[112:115], v[40:43]\n"
"v_mfma_f32_16x16x32_bf16 v[44:47], v[216:219], v[128:131], v[44:47]\n"
"ds_read_b64_tr_b16 v[216:217], v250 offset:8704\n"
"ds_read_b64_tr_b16 v[218:219], v250 offset:12800\n"
"s_waitcnt lgkmcnt(12)\n"
"v_mfma_f32_16x16x32_bf16 v[48:51], v[220:223], v[96:99], v[48:51]\n"
"v_mfma_f32_16x16x32_bf16 v[52:55], v[220:223], v[112:115], v[52:55]\n"
"s_add_u32 s71, s82, 8192\n"
"buffer_load_dwordx4 v[136:139], v253, s[44:47], s71 offen offset:2048 sc1\n"
"v_mfma_f32_16x16x32_bf16 v[56:59], v[220:223], v[128:131], v[56:59]\n"
"ds_read_b64_tr_b16 v[220:221], v249 offset:9216\n"
"ds_read_b64_tr_b16 v[222:223], v249 offset:13312\n"
"v_mfma_f32_16x16x32_bf16 v[60:63], v[224:227], v[96:99], v[60:63]\n"
"v_mfma_f32_16x16x32_bf16 v[64:67], v[224:227], v[112:115], v[64:67]\n"
"v_mfma_f32_16x16x32_bf16 v[68:71], v[224:227], v[128:131], v[68:71]\n"
"ds_read_b64_tr_b16 v[224:225], v250 offset:9216\n"
"ds_read_b64_tr_b16 v[226:227], v250 offset:13312\n"
"s_waitcnt lgkmcnt(8)\n"
"s_waitcnt vmcnt(5)\n"
"v_mfma_f32_16x16x32_bf16 v[0:3], v[204:207], v[100:103], v[0:3]\n"
"s_waitcnt vmcnt(4)\n"
"v_mfma_f32_16x16x32_bf16 v[4:7], v[204:207], v[116:119], v[4:7]\n"
"s_cmp_le_u32 s60, 1\n"
"s_cselect_b32 s82, 2, 1\n"
"s_sub_u32 s82, s82, s60\n"
"s_mul_i32 s82, s82, 98304\n"
"s_add_u32 s82, s82, s73\n"
"s_add_u32 s82, s82, 49152\n"
"buffer_load_dwordx4 v[108:111], v253, s[44:47], s82 offen offset:3072 sc1\n"
"s_waitcnt vmcnt(4)\n"
"v_mfma_f32_16x16x32_bf16 v[8:11], v[204:207], v[132:135], v[8:11]\n"
"ds_read_b64_tr_b16 v[204:205], v249 offset:16384\n"
"ds_read_b64_tr_b16 v[206:207], v249 offset:20480\n"
"v_mfma_f32_16x16x32_bf16 v[12:15], v[208:211], v[100:103], v[12:15]\n"
"v_mfma_f32_16x16x32_bf16 v[16:19], v[208:211], v[116:119], v[16:19]\n"
"v_mfma_f32_16x16x32_bf16 v[20:23], v[208:211], v[132:135], v[20:23]\n"
"ds_read_b64_tr_b16 v[208:209], v250 offset:16384\n"
"ds_read_b64_tr_b16 v[210:211], v250 offset:20480\n"
"s_waitcnt lgkmcnt(8)\n"
"v_mfma_f32_16x16x32_bf16 v[24:27], v[212:215], v[100:103], v[24:27]\n"
"v_mfma_f32_16x16x32_bf16 v[28:31], v[212:215], v[116:119], v[28:31]\n"
"s_add_u32 s71, s82, 4096\n"
"buffer_load_dwordx4 v[124:127], v253, s[44:47], s71 offen offset:3072 sc1\n"
"v_mfma_f32_16x16x32_bf16 v[32:35], v[212:215], v[132:135], v[32:35]\n"
"ds_read_b64_tr_b16 v[212:213], v249 offset:16896\n"
"ds_read_b64_tr_b16 v[214:215], v249 offset:20992\n"
"v_mfma_f32_16x16x32_bf16 v[36:39], v[216:219], v[100:103], v[36:39]\n"
"v_mfma_f32_16x16x32_bf16 v[40:43], v[216:219], v[116:119], v[40:43]\n"
"v_mfma_f32_16x16x32_bf16 v[44:47], v[216:219], v[132:135], v[44:47]\n"
"ds_read_b64_tr_b16 v[216:217], v250 offset:16896\n"
"ds_read_b64_tr_b16 v[218:219], v250 offset:20992\n"
"s_waitcnt lgkmcnt(8)\n"
"v_mfma_f32_16x16x32_bf16 v[48:51], v[220:223], v[100:103], v[48:51]\n"
"v_mfma_f32_16x16x32_bf16 v[52:55], v[220:223], v[116:119], v[52:55]\n"
"s_add_u32 s71, s82, 8192\n"
"buffer_load_dwordx4 v[140:143], v253, s[44:47], s71 offen offset:3072 sc1\n"
"v_mfma_f32_16x16x32_bf16 v[56:59], v[220:223], v[132:135], v[56:59]\n"
"ds_read_b64_tr_b16 v[220:221], v249 offset:17408\n"
"ds_read_b64_tr_b16 v[222:223], v249 offset:21504\n"
"v_mfma_f32_16x16x32_bf16 v[60:63], v[224:227], v[100:103], v[60:63]\n"
"v_mfma_f32_16x16x32_bf16 v[64:67], v[224:227], v[116:119], v[64:67]\n"
"v_mfma_f32_16x16x32_bf16 v[68:71], v[224:227], v[132:135], v[68:71]\n"
"ds_read_b64_tr_b16 v[224:225], v250 offset:17408\n"
"ds_read_b64_tr_b16 v[226:227], v250 offset:21504\n"
"s_waitcnt lgkmcnt(8)\n"
"s_waitcnt vmcnt(5)\n"
"v_mfma_f32_16x16x32_bf16 v[0:3], v[204:207], v[104:107], v[0:3]\n"
"s_waitcnt vmcnt(4)\n"
"v_mfma_f32_16x16x32_bf16 v[4:7], v[204:207], v[120:123], v[4:7]\n"
"s_cmp_le_u32 s60, 2\n"
"s_cselect_b32 s82, 3, 2\n"
"s_sub_u32 s82, s82, s60\n"
"s_mul_i32 s82, s82, 98304\n"
"s_add_u32 s82, s82, s73\n"
"s_add_u32 s82, s82, 49152\n"
"buffer_load_dwordx4 v[144:147], v253, s[44:47], s82 offen sc1\n"
"s_waitcnt vmcnt(4)\n"
"v_mfma_f32_16x16x32_bf16 v[8:11], v[204:207], v[136:139], v[8:11]\n"
"v_add_u32 v249, 65536, v249\n"
"v_add_u32 v250, 65536, v250\n"
"ds_read_b64_tr_b16 v[204:205], v249 offset:0\n"
"ds_read_b64_tr_b16 v[206:207], v249 offset:4096\n"
"v_mfma_f32_16x16x32_bf16 v[12:15], v[208:211], v[104:107], v[12:15]\n"
"v_mfma_f32_16x16x32_bf16 v[16:19], v[208:211], v[120:123], v[16:19]\n"
"v_mfma_f32_16x16x32_bf16 v[20:23], v[208:211], v[136:139], v[20:23]\n"
"ds_read_b64_tr_b16 v[208:209], v250 offset:0\n"
"ds_read_b64_tr_b16 v[210:211], v250 offset:4096\n"
"s_waitcnt lgkmcnt(8)\n"
"v_mfma_f32_16x16x32_bf16 v[24:27], v[212:215], v[104:107], v[24:27]\n"
"v_mfma_f32_16x16x32_bf16 v[28:31], v[212:215], v[120:123], v[28:31]\n"
"s_add_u32 s71, s82, 4096\n"
"buffer_load_dwordx4 v[160:163], v253, s[44:47], s71 offen sc1\n"
"v_mfma_f32_16x16x32_bf16 v[32:35], v[212:215], v[136:139], v[32:35]\n"
"ds_read_b64_tr_b16 v[212:213], v249 offset:512\n"
"ds_read_b64_tr_b16 v[214:215], v249 offset:4608\n"
"v_mfma_f32_16x16x32_bf16 v[36:39], v[216:219], v[104:107], v[36:39]\n"
"v_mfma_f32_16x16x32_bf16 v[40:43], v[216:219], v[120:123], v[40:43]\n"
"v_mfma_f32_16x16x32_bf16 v[44:47], v[216:219], v[136:139], v[44:47]\n"
"ds_read_b64_tr_b16 v[216:217], v250 offset:512\n"
"ds_read_b64_tr_b16 v[218:219], v250 offset:4608\n"
"s_waitcnt lgkmcnt(8)\n"
"v_mfma_f32_16x16x32_bf16 v[48:51], v[220:223], v[104:107], v[48:51]\n"
"v_mfma_f32_16x16x32_bf16 v[52:55], v[220:223], v[120:123], v[52:55]\n"
"s_add_u32 s71, s82, 8192\n"
"buffer_load_dwordx4 v[176:179], v253, s[44:47], s71 offen sc1\n"
"v_mfma_f32_16x16x32_bf16 v[56:59], v[220:223], v[136:139], v[56:59]\n"
"ds_read_b64_tr_b16 v[220:221], v249 offset:1024\n"
"ds_read_b64_tr_b16 v[222:223], v249 offset:5120\n"
"v_mfma_f32_16x16x32_bf16 v[60:63], v[224:227], v[104:107], v[60:63]\n"
"v_mfma_f32_16x16x32_bf16 v[64:67], v[224:227], v[120:123], v[64:67]\n"
"v_mfma_f32_16x16x32_bf16 v[68:71], v[224:227], v[136:139], v[68:71]\n"
"ds_read_b64_tr_b16 v[224:225], v250 offset:1024\n"
"ds_read_b64_tr_b16 v[226:227], v250 offset:5120\n"
"s_waitcnt lgkmcnt(8)\n"
"s_waitcnt vmcnt(5)\n"
"v_mfma_f32_16x16x32_bf16 v[0:3], v[204:207], v[108:111], v[0:3]\n"
"s_waitcnt vmcnt(4)\n"
"v_mfma_f32_16x16x32_bf16 v[4:7], v[204:207], v[124:127], v[4:7]\n"
"s_cmp_le_u32 s60, 2\n"
"s_cselect_b32 s82, 3, 2\n"
"s_sub_u32 s82, s82, s60\n"
"s_mul_i32 s82, s82, 98304\n"
"s_add_u32 s82, s82, s73\n"
"s_add_u32 s82, s82, 49152\n"
"buffer_load_dwordx4 v[148:151], v253, s[44:47], s82 offen offset:1024 sc1\n"
"s_waitcnt vmcnt(4)\n"
"v_mfma_f32_16x16x32_bf16 v[8:11], v[204:207], v[140:143], v[8:11]\n"
"v_add_u32 v249, 32768, v249\n"
"v_add_u32 v250, 32768, v250\n"
"ds_read_b64_tr_b16 v[204:205], v249 offset:32768\n"
"ds_read_b64_tr_b16 v[206:207], v249 offset:36864\n"
"v_mfma_f32_16x16x32_bf16 v[12:15], v[208:211], v[108:111], v[12:15]\n"
"v_mfma_f32_16x16x32_bf16 v[16:19], v[208:211], v[124:127], v[16:19]\n"
"v_mfma_f32_16x16x32_bf16 v[20:23], v[208:211], v[140:143], v[20:23]\n"
"ds_read_b64_tr_b16 v[208:209], v250 offset:32768\n"
"ds_read_b64_tr_b16 v[210:211], v250 offset:36864\n"
"s_waitcnt lgkmcnt(8)\n"
"v_mfma_f32_16x16x32_bf16 v[24:27], v[212:215], v[108:111], v[24:27]\n"
"v_mfma_f32_16x16x32_bf16 v[28:31], v[212:215], v[124:127], v[28:31]\n"
"s_add_u32 s71, s82, 4096\n"
"buffer_load_dwordx4 v[164:167], v253, s[44:47], s71 offen offset:1024 sc1\n"
"v_mfma_f32_16x16x32_bf16 v[32:35], v[212:215], v[140:143], v[32:35]\n"
"ds_read_b64_tr_b16 v[212:213], v249 offset:33280\n"
"ds_read_b64_tr_b16 v[214:215], v249 offset:37376\n"
"v_mfma_f32_16x16x32_bf16 v[36:39], v[216:219], v[108:111], v[36:39]\n"
"v_mfma_f32_16x16x32_bf16 v[40:43], v[216:219], v[124:127], v[40:43]\n"
"v_mfma_f32_16x16x32_bf16 v[44:47], v[216:219], v[140:143], v[44:47]\n"
"ds_read_b64_tr_b16 v[216:217], v250 offset:33280\n"
"ds_read_b64_tr_b16 v[218:219], v250 offset:37376\n"
"s_waitcnt lgkmcnt(8)\n"
"v_mfma_f32_16x16x32_bf16 v[48:51], v[220:223], v[108:111], v[48:51]\n"
"v_mfma_f32_16x16x32_bf16 v[52:55], v[220:223], v[124:127], v[52:55]\n"
"s_add_u32 s71, s82, 8192\n"
"buffer_load_dwordx4 v[180:183], v253, s[44:47], s71 offen offset:1024 sc1\n"
"v_mfma_f32_16x16x32_bf16 v[56:59], v[220:223], v[140:143], v[56:59]\n"
"ds_read_b64_tr_b16 v[220:221], v249 offset:33792\n"
"ds_read_b64_tr_b16 v[222:223], v249 offset:37888\n"
"v_mfma_f32_16x16x32_bf16 v[60:63], v[224:227], v[108:111], v[60:63]\n"
"v_mfma_f32_16x16x32_bf16 v[64:67], v[224:227], v[124:127], v[64:67]\n"
"v_mfma_f32_16x16x32_bf16 v[68:71], v[224:227], v[140:143], v[68:71]\n"
"ds_read_b64_tr_b16 v[224:225], v250 offset:33792\n"
"ds_read_b64_tr_b16 v[226:227], v250 offset:37888\n"
"s_barrier\n"
"; step 37\n"
"s_waitcnt lgkmcnt(8)\n"
"s_waitcnt vmcnt(5)\n"
"v_mfma_f32_16x16x32_bf16 v[0:3], v[204:207], v[144:147], v[0:3]\n"
"s_waitcnt vmcnt(4)\n"
"v_mfma_f32_16x16x32_bf16 v[4:7], v[204:207], v[160:163], v[4:7]\n"
"s_cmp_le_u32 s60, 2\n"
"s_cselect_b32 s82, 3, 2\n"
"s_sub_u32 s82, s82, s60\n"
"s_mul_i32 s82, s82, 98304\n"
"s_add_u32 s82, s82, s73\n"
"s_add_u32 s82, s82, 49152\n"
"buffer_load_dwordx4 v[152:155], v253, s[44:47], s82 offen offset:2048 sc1\n"
"s_waitcnt vmcnt(4)\n"
"v_mfma_f32_16x16x32_bf16 v[8:11], v[204:207], v[176:179], v[8:11]\n"
"ds_read_b64_tr_b16 v[204:205], v249 offset:40960\n"
"ds_read_b64_tr_b16 v[206:207], v249 offset:45056\n"
"v_mfma_f32_16x16x32_bf16 v[12:15], v[208:211], v[144:147], v[12:15]\n"
"v_mfma_f32_16x16x32_bf16 v[16:19], v[208:211], v[160:163], v[16:19]\n"
"v_mfma_f32_16x16x32_bf16 v[20:23], v[208:211], v[176:179], v[20:23]\n"
"ds_read_b64_tr_b16 v[208:209], v250 offset:40960\n"
"ds_read_b64_tr_b16 v[210:211], v250 offset:45056\n"
"s_waitcnt lgkmcnt(8)\n"
"v_mfma_f32_16x16x32_bf16 v[24:27], v[212:215], v[144:147], v[24:27]\n"
"v_mfma_f32_16x16x32_bf16 v[28:31], v[212:215], v[160:163], v[28:31]\n"
"s_add_u32 s71, s82, 4096\n"
"buffer_load_dwordx4 v[168:171], v253, s[44:47], s71 offen offset:2048 sc1\n"
"v_mfma_f32_16x16x32_bf16 v[32:35], v[212:215], v[176:179], v[32:35]\n"
"ds_read_b64_tr_b16 v[212:213], v249 offset:41472\n"
"ds_read_b64_tr_b16 v[214:215], v249 offset:45568\n"
"v_mfma_f32_16x16x32_bf16 v[36:39], v[216:219], v[144:147], v[36:39]\n"
"v_mfma_f32_16x16x32_bf16 v[40:43], v[216:219], v[160:163], v[40:43]\n"
"v_mfma_f32_16x16x32_bf16 v[44:47], v[216:219], v[176:179], v[44:47]\n"
"ds_read_b64_tr_b16 v[216:217], v250 offset:41472\n"
"ds_read_b64_tr_b16 v[218:219], v250 offset:45568\n"
"s_waitcnt lgkmcnt(8)\n"
"v_mfma_f32_16x16x32_bf16 v[48:51], v[220:223], v[144:147], v[48:51]\n"
"v_mfma_f32_16x16x32_bf16 v[52:55], v[220:223], v[160:163], v[52:55]\n"
"s_add_u32 s71, s82, 8192\n"
"buffer_load_dwordx4 v[184:187], v253, s[44:47], s71 offen offset:2048 sc1\n"
"v_mfma_f32_16x16x32_bf16 v[56:59], v[220:223], v[176:179], v[56:59]\n"
"ds_read_b64_tr_b16 v[220:221], v249 offset:41984\n"
"ds_read_b64_tr_b16 v[222:223], v249 offset:46080\n"
"v_mfma_f32_16x16x32_bf16 v[60:63], v[224:227], v[144:147], v[60:63]\n"
"v_mfma_f32_16x16x32_bf16 v[64:67], v[224:227], v[160:163], v[64:67]\n"
"v_mfma_f32_16x16x32_bf16 v[68:71], v[224:227], v[176:179], v[68:71]\n"
"ds_read_b64_tr_b16 v[224:225], v250 offset:41984\n"
"ds_read_b64_tr_b16 v[226:227], v250 offset:46080\n"
"s_waitcnt lgkmcnt(8)\n"
"s_waitcnt vmcnt(5)\n"
"v_mfma_f32_16x16x32_bf16 v[0:3], v[204:207], v[148:151], v[0:3]\n"
"s_waitcnt vmcnt(4)\n"
"v_mfma_f32_16x16x32_bf16 v[4:7], v[204:207], v[164:167], v[4:7]\n"
"s_cmp_le_u32 s60, 2\n"
"s_cselect_b32 s82, 3, 2\n"
"s_sub_u32 s82, s82, s60\n"
"s_mul_i32 s82, s82, 98304\n"
"s_add_u32 s82, s82, s73\n"
"s_add_u32 s82, s82, 49152\n"
"buffer_load_dwordx4 v[156:159], v253, s[44:47], s82 offen offset:3072 sc1\n"
"s_waitcnt vmcnt(4)\n"
"v_mfma_f32_16x16x32_bf16 v[8:11], v[204:207], v[180:183], v[8:11]\n"
"ds_read_b64_tr_b16 v[204:205], v249 offset:49152\n"
"ds_read_b64_tr_b16 v[206:207], v249 offset:53248\n"
"v_mfma_f32_16x16x32_bf16 v[12:15], v[208:211], v[148:151], v[12:15]\n"
"v_mfma_f32_16x16x32_bf16 v[16:19], v[208:211], v[164:167], v[16:19]\n"
"v_mfma_f32_16x16x32_bf16 v[20:23], v[208:211], v[180:183], v[20:23]\n"
"ds_read_b64_tr_b16 v[208:209], v250 offset:49152\n"
"ds_read_b64_tr_b16 v[210:211], v250 offset:53248\n"
"s_waitcnt lgkmcnt(8)\n"
"v_mfma_f32_16x16x32_bf16 v[24:27], v[212:215], v[148:151], v[24:27]\n"
"v_mfma_f32_16x16x32_bf16 v[28:31], v[212:215], v[164:167], v[28:31]\n"
"s_add_u32 s71, s82, 4096\n"
"buffer_load_dwordx4 v[172:175], v253, s[44:47], s71 offen offset:3072 sc1\n"
"v_mfma_f32_16x16x32_bf16 v[32:35], v[212:215], v[180:183], v[32:35]\n"
"ds_read_b64_tr_b16 v[212:213], v249 offset:49664\n"
"ds_read_b64_tr_b16 v[214:215], v249 offset:53760\n"
"v_mfma_f32_16x16x32_bf16 v[36:39], v[216:219], v[148:151], v[36:39]\n"
"v_mfma_f32_16x16x32_bf16 v[40:43], v[216:219], v[164:167], v[40:43]\n"
"v_mfma_f32_16x16x32_bf16 v[44:47], v[216:219], v[180:183], v[44:47]\n"
"ds_read_b64_tr_b16 v[216:217], v250 offset:49664\n"
"ds_read_b64_tr_b16 v[218:219], v250 offset:53760\n"
"s_waitcnt lgkmcnt(8)\n"
"v_mfma_f32_16x16x32_bf16 v[48:51], v[220:223], v[148:151], v[48:51]\n"
"v_mfma_f32_16x16x32_bf16 v[52:55], v[220:223], v[164:167], v[52:55]\n"
"s_add_u32 s71, s82, 8192\n"
"buffer_load_dwordx4 v[188:191], v253, s[44:47], s71 offen offset:3072 sc1\n"
"v_mfma_f32_16x16x32_bf16 v[56:59], v[220:223], v[180:183], v[56:59]\n"
"ds_read_b64_tr_b16 v[220:221], v249 offset:50176\n"
"ds_read_b64_tr_b16 v[222:223], v249 offset:54272\n"
"v_mfma_f32_16x16x32_bf16 v[60:63], v[224:227], v[148:151], v[60:63]\n"
"v_mfma_f32_16x16x32_bf16 v[64:67], v[224:227], v[164:167], v[64:67]\n"
"v_mfma_f32_16x16x32_bf16 v[68:71], v[224:227], v[180:183], v[68:71]\n"
"ds_read_b64_tr_b16 v[224:225], v250 offset:50176\n"
"ds_read_b64_tr_b16 v[226:227], v250 offset:54272\n"
"s_waitcnt lgkmcnt(8)\n"
"s_waitcnt vmcnt(5)\n"
"v_mfma_f32_16x16x32_bf16 v[0:3], v[204:207], v[152:155], v[0:3]\n"
"s_waitcnt vmcnt(4)\n"
"v_mfma_f32_16x16x32_bf16 v[4:7], v[204:207], v[168:171], v[4:7]\n"
"s_waitcnt vmcnt(3)\n"
"v_mfma_f32_16x16x32_bf16 v[8:11], v[204:207], v[184:187], v[8:11]\n"
"ds_read_b64_tr_b16 v[204:205], v249 offset:57344\n"
"ds_read_b64_tr_b16 v[206:207], v249 offset:61440\n"
"v_mfma_f32_16x16x32_bf16 v[12:15], v[208:211], v[152:155], v[12:15]\n"
"v_mfma_f32_16x16x32_bf16 v[16:19], v[208:211], v[168:171], v[16:19]\n"
"v_mfma_f32_16x16x32_bf16 v[20:23], v[208:211], v[184:187], v[20:23]\n"
"ds_read_b64_tr_b16 v[208:209], v250 offset:57344\n"
"ds_read_b64_tr_b16 v[210:211], v250 offset:61440\n"
"s_waitcnt lgkmcnt(8)\n"
"v_mfma_f32_16x16x32_bf16 v[24:27], v[212:215], v[152:155], v[24:27]\n"
"v_mfma_f32_16x16x32_bf16 v[28:31], v[212:215], v[168:171], v[28:31]\n"
"v_mfma_f32_16x16x32_bf16 v[32:35], v[212:215], v[184:187], v[32:35]\n"
"ds_read_b64_tr_b16 v[212:213], v249 offset:57856\n"
"ds_read_b64_tr_b16 v[214:215], v249 offset:61952\n"
"v_mfma_f32_16x16x32_bf16 v[36:39], v[216:219], v[152:155], v[36:39]\n"
"v_mfma_f32_16x16x32_bf16 v[40:43], v[216:219], v[168:171], v[40:43]\n"
"v_mfma_f32_16x16x32_bf16 v[44:47], v[216:219], v[184:187], v[44:47]\n"
"ds_read_b64_tr_b16 v[216:217], v250 offset:57856\n"
"ds_read_b64_tr_b16 v[218:219], v250 offset:61952\n"
"s_waitcnt lgkmcnt(8)\n"
"v_mfma_f32_16x16x32_bf16 v[48:51], v[220:223], v[152:155], v[48:51]\n"
"v_mfma_f32_16x16x32_bf16 v[52:55], v[220:223], v[168:171], v[52:55]\n"
"v_mfma_f32_16x16x32_bf16 v[56:59], v[220:223], v[184:187], v[56:59]\n"
"ds_read_b64_tr_b16 v[220:221], v249 offset:58368\n"
"ds_read_b64_tr_b16 v[222:223], v249 offset:62464\n"
"v_mfma_f32_16x16x32_bf16 v[60:63], v[224:227], v[152:155], v[60:63]\n"
"v_mfma_f32_16x16x32_bf16 v[64:67], v[224:227], v[168:171], v[64:67]\n"
"v_mfma_f32_16x16x32_bf16 v[68:71], v[224:227], v[184:187], v[68:71]\n"
"ds_read_b64_tr_b16 v[224:225], v250 offset:58368\n"
"ds_read_b64_tr_b16 v[226:227], v250 offset:62464\n"
"s_lshl_b32 s65, s58, 2\n"
"s_add_u32 s65, s65, s60\n"
"s_lshl_b32 s65, s65, 2\n"
"s_add_u32 s65, s65, s56\n"
"s_mul_i32 s65, s65, 49152\n"
"s_add_u32 s65, s65, 25231360\n"
"s_mul_i32 s70, s59, 3072\n"
"s_add_u32 s77, s65, s70\n"
"s_waitcnt lgkmcnt(8)\n"
"s_waitcnt vmcnt(2)\n"
"v_mfma_f32_16x16x32_bf16 v[0:3], v[204:207], v[156:159], v[0:3]\n"
"s_waitcnt vmcnt(1)\n"
"v_mfma_f32_16x16x32_bf16 v[4:7], v[204:207], v[172:175], v[4:7]\n"
"s_waitcnt vmcnt(0)\n"
"v_mfma_f32_16x16x32_bf16 v[8:11], v[204:207], v[188:191], v[8:11]\n"
"v_add_u32 v249, -98304, v249\n"
"v_add_u32 v250, -98304, v250\n"
"ds_read_b64_tr_b16 v[204:205], v249 offset:9728\n"
"ds_read_b64_tr_b16 v[206:207], v249 offset:13824\n"
"v_mfma_f32_16x16x32_bf16 v[12:15], v[208:211], v[156:159], v[12:15]\n"
"v_mfma_f32_16x16x32_bf16 v[16:19], v[208:211], v[172:175], v[16:19]\n"
"v_mfma_f32_16x16x32_bf16 v[20:23], v[208:211], v[188:191], v[20:23]\n"
"ds_read_b64_tr_b16 v[208:209], v250 offset:9728\n"
"ds_read_b64_tr_b16 v[210:211], v250 offset:13824\n"
"s_waitcnt lgkmcnt(8)\n"
"v_mfma_f32_16x16x32_bf16 v[24:27], v[212:215], v[156:159], v[24:27]\n"
"s_xor_b32 s74, s34, 0\n"
"s_mul_i32 s74, s74, 12288\n"
"s_add_u32 s74, s74, s77\n"
"v_cvt_pk_bf16_f32 v192, v0, v1\n"
"v_mfma_f32_16x16x32_bf16 v[28:31], v[212:215], v[172:175], v[28:31]\n"
"v_cvt_pk_bf16_f32 v193, v2, v3\n"
"v_cvt_pk_bf16_f32 v194, v12, v13\n"
"v_cvt_pk_bf16_f32 v195, v14, v15\n"
"buffer_store_dwordx4 v[192:195], v253, s[48:51], s74 offen sc1\n"
"v_mfma_f32_16x16x32_bf16 v[32:35], v[212:215], v[188:191], v[32:35]\n"
"ds_read_b64_tr_b16 v[212:213], v249 offset:17920\n"
"ds_read_b64_tr_b16 v[214:215], v249 offset:22016\n"
"v_cvt_pk_bf16_f32 v196, v4, v5\n"
"v_mfma_f32_16x16x32_bf16 v[36:39], v[216:219], v[156:159], v[36:39]\n"
"v_cvt_pk_bf16_f32 v197, v6, v7\n"
"v_cvt_pk_bf16_f32 v198, v16, v17\n"
"v_cvt_pk_bf16_f32 v199, v18, v19\n"
"buffer_store_dwordx4 v[196:199], v253, s[48:51], s74 offen offset:1024 sc1\n"
"v_mfma_f32_16x16x32_bf16 v[40:43], v[216:219], v[172:175], v[40:43]\n"
"v_cvt_pk_bf16_f32 v200, v8, v9\n"
"v_cvt_pk_bf16_f32 v201, v10, v11\n"
"v_cvt_pk_bf16_f32 v202, v20, v21\n"
"v_mfma_f32_16x16x32_bf16 v[44:47], v[216:219], v[188:191], v[44:47]\n"
"ds_read_b64_tr_b16 v[216:217], v250 offset:17920\n"
"ds_read_b64_tr_b16 v[218:219], v250 offset:22016\n"
"v_cvt_pk_bf16_f32 v203, v22, v23\n"
"buffer_store_dwordx4 v[200:203], v253, s[48:51], s74 offen offset:2048 sc1\n"
"s_waitcnt lgkmcnt(8)\n"
"v_mfma_f32_16x16x32_bf16 v[48:51], v[220:223], v[156:159], v[48:51]\n"
"s_xor_b32 s74, s34, 1\n"
"s_mul_i32 s74, s74, 12288\n"
"s_add_u32 s74, s74, s77\n"
"v_cvt_pk_bf16_f32 v192, v24, v25\n"
"v_mfma_f32_16x16x32_bf16 v[52:55], v[220:223], v[172:175], v[52:55]\n"
"v_cvt_pk_bf16_f32 v193, v26, v27\n"
"v_cvt_pk_bf16_f32 v194, v36, v37\n"
"v_cvt_pk_bf16_f32 v195, v38, v39\n"
"buffer_store_dwordx4 v[192:195], v253, s[48:51], s74 offen sc1\n"
"v_mfma_f32_16x16x32_bf16 v[56:59], v[220:223], v[188:191], v[56:59]\n"
"v_add_u32 v249, 65536, v249\n"
"v_add_u32 v250, 65536, v250\n"
"ds_read_b64_tr_b16 v[220:221], v249 offset:1536\n"
"ds_read_b64_tr_b16 v[222:223], v249 offset:5632\n"
"v_cvt_pk_bf16_f32 v196, v28, v29\n"
"v_mfma_f32_16x16x32_bf16 v[60:63], v[224:227], v[156:159], v[60:63]\n"
"v_cvt_pk_bf16_f32 v197, v30, v31\n"
"v_cvt_pk_bf16_f32 v198, v40, v41\n"
"v_cvt_pk_bf16_f32 v199, v42, v43\n"
"buffer_store_dwordx4 v[196:199], v253, s[48:51], s74 offen offset:1024 sc1\n"
"v_mfma_f32_16x16x32_bf16 v[64:67], v[224:227], v[172:175], v[64:67]\n"
"v_cvt_pk_bf16_f32 v200, v32, v33\n"
"v_cvt_pk_bf16_f32 v201, v34, v35\n"
"v_cvt_pk_bf16_f32 v202, v44, v45\n"
"v_mfma_f32_16x16x32_bf16 v[68:71], v[224:227], v[188:191], v[68:71]\n"
"ds_read_b64_tr_b16 v[224:225], v250 offset:1536\n"
"ds_read_b64_tr_b16 v[226:227], v250 offset:5632\n"
"v_cvt_pk_bf16_f32 v203, v46, v47\n"
"buffer_store_dwordx4 v[200:203], v253, s[48:51], s74 offen offset:2048 sc1\n"
"; step 38\n"
"v_mfma_f32_16x16x32_bf16 v[72:75], v[228:231], v[96:99], v[72:75]\n"
"v_mfma_f32_16x16x32_bf16 v[76:79], v[228:231], v[112:115], v[76:79]\n"
"s_xor_b32 s74, s34, 2\n"
"s_mul_i32 s74, s74, 12288\n"
"s_add_u32 s74, s74, s77\n"
"v_cvt_pk_bf16_f32 v192, v48, v49\n"
"v_cvt_pk_bf16_f32 v193, v50, v51\n"
"v_mfma_f32_16x16x32_bf16 v[80:83], v[228:231], v[128:131], v[80:83]\n"
"v_add_u32 v249, 32768, v249\n"
"v_add_u32 v250, 32768, v250\n"
"ds_read_b64_tr_b16 v[228:229], v249 offset:34304\n"
"ds_read_b64_tr_b16 v[230:231], v249 offset:38400\n"
"v_mfma_f32_16x16x32_bf16 v[84:87], v[232:235], v[96:99], v[84:87]\n"
"v_cvt_pk_bf16_f32 v194, v60, v61\n"
"v_cvt_pk_bf16_f32 v195, v62, v63\n"
"v_mfma_f32_16x16x32_bf16 v[88:91], v[232:235], v[112:115], v[88:91]\n"
"buffer_store_dwordx4 v[192:195], v253, s[48:51], s74 offen sc1\n"
"v_cvt_pk_bf16_f32 v196, v52, v53\n"
"v_mfma_f32_16x16x32_bf16 v[92:95], v[232:235], v[128:131], v[92:95]\n"
"ds_read_b64_tr_b16 v[232:233], v250 offset:34304\n"
"ds_read_b64_tr_b16 v[234:235], v250 offset:38400\n"
"s_waitcnt lgkmcnt(14)\n"
"v_mfma_f32_16x16x32_bf16 v[72:75], v[204:207], v[100:103], v[72:75]\n"
"v_cvt_pk_bf16_f32 v197, v54, v55\n"
"v_cvt_pk_bf16_f32 v198, v64, v65\n"
"v_mfma_f32_16x16x32_bf16 v[76:79], v[204:207], v[116:119], v[76:79]\n"
"v_cvt_pk_bf16_f32 v199, v66, v67\n"
"buffer_store_dwordx4 v[196:199], v253, s[48:51], s74 offen offset:1024 sc1\n"
"v_cvt_pk_bf16_f32 v200, v56, v57\n"
"v_mfma_f32_16x16x32_bf16 v[80:83], v[204:207], v[132:135], v[80:83]\n"
"ds_read_b64_tr_b16 v[204:205], v249 offset:42496\n"
"ds_read_b64_tr_b16 v[206:207], v249 offset:46592\n"
"s_waitcnt lgkmcnt(14)\n"
"v_mfma_f32_16x16x32_bf16 v[84:87], v[208:211], v[100:103], v[84:87]\n"
"v_cvt_pk_bf16_f32 v201, v58, v59\n"
"v_cvt_pk_bf16_f32 v202, v68, v69\n"
"v_mfma_f32_16x16x32_bf16 v[88:91], v[208:211], v[116:119], v[88:91]\n"
"v_cvt_pk_bf16_f32 v203, v70, v71\n"
"buffer_store_dwordx4 v[200:203], v253, s[48:51], s74 offen offset:2048 sc1\n"
"v_mfma_f32_16x16x32_bf16 v[92:95], v[208:211], v[132:135], v[92:95]\n"
"ds_read_b64_tr_b16 v[208:209], v250 offset:42496\n"
"ds_read_b64_tr_b16 v[210:211], v250 offset:46592\n"
"s_waitcnt lgkmcnt(14)\n"
"v_mfma_f32_16x16x32_bf16 v[72:75], v[212:215], v[104:107], v[72:75]\n"
"v_bfe_u32 v0, v253, 4, 4\n"
"v_mfma_f32_16x16x32_bf16 v[76:79], v[212:215], v[120:123], v[76:79]\n"
"v_mfma_f32_16x16x32_bf16 v[80:83], v[212:215], v[136:139], v[80:83]\n"
"ds_read_b64_tr_b16 v[212:213], v249 offset:50688\n"
"ds_read_b64_tr_b16 v[214:215], v249 offset:54784\n"
"s_waitcnt lgkmcnt(14)\n"
"v_mfma_f32_16x16x32_bf16 v[84:87], v[216:219], v[104:107], v[84:87]\n"
"v_mfma_f32_16x16x32_bf16 v[88:91], v[216:219], v[120:123], v[88:91]\n"
"v_lshrrev_b32 v1, 8, v253\n"
"v_mfma_f32_16x16x32_bf16 v[92:95], v[216:219], v[136:139], v[92:95]\n"
"ds_read_b64_tr_b16 v[216:217], v250 offset:50688\n"
"ds_read_b64_tr_b16 v[218:219], v250 offset:54784\n"
"s_waitcnt lgkmcnt(14)\n"
"v_mfma_f32_16x16x32_bf16 v[72:75], v[220:223], v[108:111], v[72:75]\n"
"v_mfma_f32_16x16x32_bf16 v[76:79], v[220:223], v[124:127], v[76:79]\n"
"v_mfma_f32_16x16x32_bf16 v[80:83], v[220:223], v[140:143], v[80:83]\n"
"ds_read_b64_tr_b16 v[220:221], v249 offset:58880\n"
"ds_read_b64_tr_b16 v[222:223], v249 offset:62976\n"
"s_waitcnt lgkmcnt(14)\n"
"v_mfma_f32_16x16x32_bf16 v[84:87], v[224:227], v[108:111], v[84:87]\n"
"v_mfma_f32_16x16x32_bf16 v[88:91], v[224:227], v[124:127], v[88:91]\n"
"v_mfma_f32_16x16x32_bf16 v[92:95], v[224:227], v[140:143], v[92:95]\n"
"ds_read_b64_tr_b16 v[224:225], v250 offset:58880\n"
"ds_read_b64_tr_b16 v[226:227], v250 offset:62976\n"
"s_waitcnt lgkmcnt(14)\n"
"v_mfma_f32_16x16x32_bf16 v[72:75], v[228:231], v[144:147], v[72:75]\n"
"v_mfma_f32_16x16x32_bf16 v[76:79], v[228:231], v[160:163], v[76:79]\n"
"v_mfma_f32_16x16x32_bf16 v[80:83], v[228:231], v[176:179], v[80:83]\n"
"v_lshlrev_b32 v1, 2, v1\n"
"s_waitcnt lgkmcnt(12)\n"
"v_mfma_f32_16x16x32_bf16 v[84:87], v[232:235], v[144:147], v[84:87]\n"
"v_sub_u32 v0, v0, v1\n"
"v_mfma_f32_16x16x32_bf16 v[88:91], v[232:235], v[160:163], v[88:91]\n"
"v_mfma_f32_16x16x32_bf16 v[92:95], v[232:235], v[176:179], v[92:95]\n"
"v_mov_b32 v24, 0\n"
"s_waitcnt lgkmcnt(10)\n"
"v_mfma_f32_16x16x32_bf16 v[72:75], v[204:207], v[148:151], v[72:75]\n"
"v_mov_b32 v25, 0\n"
"v_mfma_f32_16x16x32_bf16 v[76:79], v[204:207], v[164:167], v[76:79]\n"
"v_mfma_f32_16x16x32_bf16 v[80:83], v[204:207], v[180:183], v[80:83]\n"
"v_mov_b32 v28, 0\n"
"s_waitcnt lgkmcnt(8)\n"
"v_mfma_f32_16x16x32_bf16 v[84:87], v[208:211], v[148:151], v[84:87]\n"
"v_mov_b32 v29, 0\n"
"v_mfma_f32_16x16x32_bf16 v[88:91], v[208:211], v[164:167], v[88:91]\n"
"v_mov_b32 v1, 16256\n"
"v_mfma_f32_16x16x32_bf16 v[92:95], v[208:211], v[180:183], v[92:95]\n"
"s_waitcnt lgkmcnt(6)\n"
"v_mfma_f32_16x16x32_bf16 v[72:75], v[212:215], v[152:155], v[72:75]\n"
"v_mov_b32 v3, 1065353216\n"
"v_mfma_f32_16x16x32_bf16 v[76:79], v[212:215], v[168:171], v[76:79]\n"
"v_cmp_eq_u32 vcc, 0, v0\n"
"v_mfma_f32_16x16x32_bf16 v[80:83], v[212:215], v[184:187], v[80:83]\n"
"s_nop 0\n"
"v_cndmask_b32 v26, v24, v1, vcc\n"
"s_waitcnt lgkmcnt(4)\n"
"v_mfma_f32_16x16x32_bf16 v[84:87], v[216:219], v[152:155], v[84:87]\n"
"v_mfma_f32_16x16x32_bf16 v[88:91], v[216:219], v[168:171], v[88:91]\n"
"v_cmp_eq_u32 vcc, 1, v0\n"
"v_mfma_f32_16x16x32_bf16 v[92:95], v[216:219], v[184:187], v[92:95]\n"
"s_nop 0\n"
"v_cndmask_b32 v26, v26, v3, vcc\n"
"s_waitcnt lgkmcnt(2)\n"
"v_mfma_f32_16x16x32_bf16 v[72:75], v[220:223], v[156:159], v[72:75]\n"
"v_mfma_f32_16x16x32_bf16 v[76:79], v[220:223], v[172:175], v[76:79]\n"
"v_cmp_eq_u32 vcc, 2, v0\n"
"v_mfma_f32_16x16x32_bf16 v[80:83], v[220:223], v[188:191], v[80:83]\n"
"s_nop 0\n"
"v_cndmask_b32 v27, v24, v1, vcc\n"
"s_waitcnt lgkmcnt(0)\n"
"v_mfma_f32_16x16x32_bf16 v[84:87], v[224:227], v[156:159], v[84:87]\n"
"v_cmp_eq_u32 vcc, 3, v0\n"
"v_mfma_f32_16x16x32_bf16 v[88:91], v[224:227], v[172:175], v[88:91]\n"
"v_mfma_f32_16x16x32_bf16 v[92:95], v[224:227], v[188:191], v[92:95]\n"
"v_cndmask_b32 v27, v27, v3, vcc\n"
"s_lshl_b32 s71, s58, 2\n"
"s_add_u32 s71, s71, s60\n"
"s_lshl_b32 s71, s71, 2\n"
"s_add_u32 s71, s71, s56\n"
"s_lshl_b32 s71, s71, 6\n"
"s_add_u32 s71, s71, 36864\n"
"s_lshl_b32 s70, s59, 3\n"
"s_add_u32 s71, s71, s70\n"
"v_mov_b32 v244, s80\n"
"v_mov_b32 v245, s81\n"
"v_cmp_eq_u32 vcc, 0, v253\n"
"v_mov_b32 v243, 2147483647\n"
"s_nop 0\n"
"v_cndmask_b32 v242, v243, v253, vcc\n"
"s_waitcnt vmcnt(0)\n"
"buffer_store_dwordx2 v[244:245], v242, s[48:51], s71 offen sc1\n"
"s_lshl_b32 s62, s58, 2\n"
"s_add_u32 s62, s62, s60\n"
"s_lshl_b32 s62, s62, 2\n"
"s_add_u32 s62, s62, s56\n"
"s_lshl_b32 s62, s62, 6\n"
"s_add_u32 s62, s62, 36864\n"
"v_lshrrev_b32 v242, 1, v253\n"
"v_and_b32 v242, 24, v242\n"
"buffer_load_dwordx2 v[244:245], v242, s[48:51], s62 offen sc1\n"
"v_lshrrev_b32 v3, 4, v253\n"
"v_and_b32 v2, 15, v3\n"
"v_lshlrev_b32 v2, 10, v2\n"
"v_lshrrev_b32 v3, 4, v3\n"
"v_lshl_add_u32 v2, v3, 4, v2\n"
"s_mul_i32 s69, s58, 192\n"
"s_mul_i32 s70, s56, 48\n"
"s_add_u32 s69, s69, s70\n"
"s_lshl_b32 s69, s69, 10\n"
"s_lshl_b32 s70, s60, 8\n"
"s_add_u32 s69, s69, s70\n"
"s_lshl_b32 s70, s59, 6\n"
"s_add_u32 s69, s69, s70\n"
"s_mul_i32 s70, s59, 12288\n"
"s_add_u32 s65, s65, s70\n"
"s_mov_b32 s74, 262144\n"
"s_waitcnt vmcnt(0)\n"
"v_cmp_ne_u64 vcc, s[80:81], v[244:245]\n"
"s_cbranch_vccz .Lpolld2\n"
".Lpoll2:\n"
"buffer_load_dwordx2 v[244:245], v242, s[48:51], s62 offen sc1\n"
"s_waitcnt vmcnt(0)\n"
"v_cmp_ne_u64 vcc, s[80:81], v[244:245]\n"
"s_cbranch_vccz .Lpolld2\n"
"s_sleep 2\n"
"s_sub_u32 s74, s74, 1\n"
"s_cbranch_scc0 .Lpoll2\n"
".Lpolld2:\n"
"s_add_u32 s70, s59, 1\n"
"s_and_b32 s70, s70, 3\n"
"s_mul_i32 s70, s70, 3072\n"
"s_add_u32 s71, s65, s70\n"
"buffer_load_dwordx4 v[96:99], v253, s[48:51], s71 offen sc1\n"
"buffer_load_dwordx4 v[100:103], v253, s[48:51], s71 offen offset:1024 sc1\n"
"buffer_load_dwordx4 v[104:107], v253, s[48:51], s71 offen offset:2048 sc1\n"
"s_add_u32 s70, s59, 2\n"
"s_and_b32 s70, s70, 3\n"
"s_mul_i32 s70, s70, 3072\n"
"s_add_u32 s71, s65, s70\n"
"buffer_load_dwordx4 v[108:111], v253, s[48:51], s71 offen sc1\n"
"buffer_load_dwordx4 v[112:115], v253, s[48:51], s71 offen offset:1024 sc1\n"
"buffer_load_dwordx4 v[116:119], v253, s[48:51], s71 offen offset:2048 sc1\n"
"s_add_u32 s70, s59, 3\n"
"s_and_b32 s70, s70, 3\n"
"s_mul_i32 s70, s70, 3072\n"
"s_add_u32 s71, s65, s70\n"
"buffer_load_dwordx4 v[120:123], v253, s[48:51], s71 offen sc1\n"
"buffer_load_dwordx4 v[124:127], v253, s[48:51], s71 offen offset:1024 sc1\n"
"buffer_load_dwordx4 v[128:131], v253, s[48:51], s71 offen offset:2048 sc1\n"
"s_mov_b32 s70, 4294901760\n"
"s_waitcnt vmcnt(8)\n"
"v_mfma_f32_16x16x32_bf16 v[72:75], v[26:29], v[96:99], v[72:75]\n"
"v_mfma_f32_16x16x32_bf16 v[84:87], v[24:27], v[96:99], v[84:87]\n"
"s_waitcnt vmcnt(7)\n"
"v_mfma_f32_16x16x32_bf16 v[76:79], v[26:29], v[100:103], v[76:79]\n"
"v_mfma_f32_16x16x32_bf16 v[88:91], v[24:27], v[100:103], v[88:91]\n"
"s_waitcnt vmcnt(6)\n"
"v_mfma_f32_16x16x32_bf16 v[80:83], v[26:29], v[104:107], v[80:83]\n"
"v_mfma_f32_16x16x32_bf16 v[92:95], v[24:27], v[104:107], v[92:95]\n"
"s_waitcnt vmcnt(5)\n"
"v_mfma_f32_16x16x32_bf16 v[72:75], v[26:29], v[108:111], v[72:75]\n"
"v_mfma_f32_16x16x32_bf16 v[84:87], v[24:27], v[108:111], v[84:87]\n"
"s_waitcnt vmcnt(4)\n"
"v_mfma_f32_16x16x32_bf16 v[76:79], v[26:29], v[112:115], v[76:79]\n"
"v_mfma_f32_16x16x32_bf16 v[88:91], v[24:27], v[112:115], v[88:91]\n"
"s_waitcnt vmcnt(3)\n"
"v_mfma_f32_16x16x32_bf16 v[80:83], v[26:29], v[116:119], v[80:83]\n"
"v_mfma_f32_16x16x32_bf16 v[92:95], v[24:27], v[116:119], v[92:95]\n"
"s_waitcnt vmcnt(2)\n"
"v_mfma_f32_16x16x32_bf16 v[72:75], v[26:29], v[120:123], v[72:75]\n"
"v_mfma_f32_16x16x32_bf16 v[84:87], v[24:27], v[120:123], v[84:87]\n"
"s_waitcnt vmcnt(1)\n"
"v_mfma_f32_16x16x32_bf16 v[76:79], v[26:29], v[124:127], v[76:79]\n"
"v_mfma_f32_16x16x32_bf16 v[88:91], v[24:27], v[124:127], v[88:91]\n"
"s_waitcnt vmcnt(0)\n"
"v_mfma_f32_16x16x32_bf16 v[80:83], v[26:29], v[128:131], v[80:83]\n"
"v_mfma_f32_16x16x32_bf16 v[92:95], v[24:27], v[128:131], v[92:95]\n"
"s_nop 0\n"
"v_cvt_pk_bf16_f32 v4, v72, v73\n"
"v_cvt_pk_bf16_f32 v6, v84, v85\n"
"v_cvt_pk_bf16_f32 v5, v74, v75\n"
"v_cvt_pk_bf16_f32 v7, v86, v87\n"
"v_cvt_pk_bf16_f32 v8, v76, v77\n"
"v_cvt_pk_bf16_f32 v10, v88, v89\n"
"v_cvt_pk_bf16_f32 v9, v78, v79\n"
"v_cvt_pk_bf16_f32 v11, v90, v91\n"
"v_cvt_pk_bf16_f32 v12, v80, v81\n"
"v_cvt_pk_bf16_f32 v14, v92, v93\n"
"v_cvt_pk_bf16_f32 v13, v82, v83\n"
"v_cvt_pk_bf16_f32 v15, v94, v95\n"
"v_permlane32_swap_b32 v4, v6\n"
"v_permlane32_swap_b32 v5, v7\n"
"v_permlane32_swap_b32 v8, v10\n"
"v_permlane32_swap_b32 v9, v11\n"
"v_permlane32_swap_b32 v12, v14\n"
"v_permlane32_swap_b32 v13, v15\n"
"v_permlane16_swap_b32 v4, v6\n"
"v_permlane16_swap_b32 v5, v7\n"
"v_permlane16_swap_b32 v8, v10\n"
"v_permlane16_swap_b32 v9, v11\n"
"v_permlane16_swap_b32 v12, v14\n"
"v_permlane16_swap_b32 v13, v15\n"
"buffer_store_dwordx4 v[4:7], v2, s[52:55], s69 offen\n"
"s_add_u32 s69, s69, 16384\n"
"buffer_store_dwordx4 v[8:11], v2, s[52:55], s69 offen\n"
"s_add_u32 s69, s69, 16384\n"
"buffer_store_dwordx4 v[12:15], v2, s[52:55], s69 offen\n"
"s_or_b32 s70, s59, s60\n"
"s_or_b32 s70, s70, s56\n"
"s_cbranch_scc1 .Lnonce\n"
"s_lshl_b32 s71, s58, 6\n"
"v_mov_b32 v244, s80\n"
"v_mov_b32 v245, s81\n"
"v_cmp_eq_u32 vcc, 0, v253\n"
"v_mov_b32 v243, 2147483647\n"
"s_nop 0\n"
"v_cndmask_b32 v242, v243, v253, vcc\n"
"buffer_store_dwordx2 v[244:245], v242, s[48:51], s71 offen sc1\n"
".Lnonce:\n"
"s_waitcnt vmcnt(0)\n"
"s_branch .Lend\n"
".LY:\n"
"s_lshl_b32 s70, s58, 5\n"
"s_add_u32 s70, s70, s69\n"
"s_lshl_b32 s71, s70, 17\n"
"s_mov_b64 s[20:21], s[6:7]\n"
"s_add_u32 s20, s20, s71\n"
"s_addc_u32 s21, s21, 0\n"
"s_and_b32 s21, s21, 65535\n"
"s_mov_b32 s22, 262144\n"
"s_mov_b32 s23, 131072\n"
"s_mov_b64 s[24:25], s[8:9]\n"
"s_add_u32 s24, s24, s71\n"
"s_addc_u32 s25, s25, 0\n"
"s_and_b32 s25, s25, 65535\n"
"s_mov_b32 s26, 262144\n"
"s_mov_b32 s27, 131072\n"
"s_add_u32 s70, s70, s57\n"
"s_mul_i32 s73, s70, 12\n"
"s_mul_i32 s70, s56, 3\n"
"s_add_u32 s73, s73, s70\n"
"s_lshl_b32 s73, s73, 12\n"
"s_mov_b64 s[40:41], s[12:13]\n"
"s_and_b32 s41, s41, 65535\n"
"s_mov_b32 s42, 4194304\n"
"s_mov_b32 s43, 131072\n"
"s_mov_b64 s[52:53], s[14:15]\n"
"s_and_b32 s53, s53, 65535\n"
"s_mov_b32 s54, 3145728\n"
"s_mov_b32 s55, 131072\n"
"s_mov_b64 s[78:79], s[16:17]\n"
"s_mov_b64 s[44:45], s[16:17]\n"
"s_add_u32 s44, s44, 65536\n"
"s_addc_u32 s45, s45, 0\n"
"s_and_b32 s45, s45, 65535\n"
"s_mov_b32 s46, 25165824\n"
"s_mov_b32 s47, 131072\n"
"s_mov_b64 s[48:49], s[16:17]\n"
"s_and_b32 s49, s49, 65535\n"
"s_mov_b32 s50, 37814272\n"
"s_mov_b32 s51, 131072\n"
"s_mov_b32 s76, 1040356334\n"
"s_lshl_b32 s77, s56, 11\n"
"s_lshl_b32 s66, s59, 20\n"
"s_lshl_b32 s71, s60, 8\n"
"s_add_u32 s66, s66, s71\n"
"s_lshl_b32 s71, s56, 13\n"
"s_add_u32 s66, s66, s71\n"
"s_lshl_b32 s67, s60, 18\n"
"s_xor_b32 s34, s59, 3\n"
"s_lshl_b32 s33, s34, 6\n"
"s_and_b32 s33, s33, 128\n"
"s_or_b32 s33, s33, s66\n"
"v_mov_b32 v1, 0\n"
"s_lshl_b32 s70, s58, 6\n"
"buffer_load_dwordx2 v[244:245], v1, s[48:51], s70 offen sc1\n"
"v_lshrrev_b32 v1, 4, v0\n"
"v_bfe_u32 v4, v0, 2, 1\n"
"v_lshlrev_b32 v4, 1, v4\n"
"v_xor_b32 v4, v4, v1\n"
"v_lshlrev_b32 v4, 4, v4\n"
"v_and_b32 v5, 7, v0\n"
"v_lshl_add_u32 v4, v5, 6, v4\n"
"v_bfe_u32 v5, v0, 3, 1\n"
"v_lshl_add_u32 v248, v5, 11, v4\n"
"v_lshl_add_u32 v201, v5, 10, v4\n"
"v_add_u32 v201, s75, v201\n"
"v_lshrrev_b32 v1, 5, v0\n"
"v_bfe_u32 v4, v0, 2, 3\n"
"v_lshlrev_b32 v4, 6, v4\n"
"v_lshl_add_u32 v4, v1, 11, v4\n"
"v_lshl_add_u32 v200, v7, 4, v4\n"
"v_bfe_u32 v5, v0, 4, 1\n"
"v_lshlrev_b32 v5, 1, v5\n"
"v_bfe_u32 v6, v0, 1, 1\n"
"v_xor_b32 v5, v5, v6\n"
"v_lshl_add_u32 v4, v5, 4, v4\n"
"v_and_b32 v6, 1, v0\n"
"v_lshl_add_u32 v249, v6, 3, v4\n"
"v_xor_b32 v250, 32, v249\n"
"v_lshlrev_b32 v253, 4, v0\n"
"s_lshl_b32 s70, s34, 6\n"
"s_and_b32 s70, s70, 64\n"
"v_xor_b32 v255, s70, v255\n"
"s_getpc_b64 s[82:83]\n"
".Lpcref:\n"
"s_sub_u32 s82, s82, .Lpcref-.Lstart\n"
"s_subb_u32 s83, s83, 0\n"
"v_lshlrev_b32 v196, 7, v0\n"
"s_lshl_b32 s70, s56, 13\n"
"v_add_u32 v196, s70, v196\n"
"v_min_u32 v195, .Lend-.Lstart-4, v196\n"
"global_load_dword v197, v195, s[82:83]\n"
"v_add_u32 v196, 32768, v196\n"
"v_min_u32 v195, .Lend-.Lstart-4, v196\n"
"global_load_dword v198, v195, s[82:83]\n"
"s_waitcnt vmcnt(11)\n"
"s_barrier\n"
"; step -18\n"
"v_add_u32 v200, 98304, v200\n"
"ds_read_b64_tr_b16 v[204:205], v200 offset:8192\n"
"ds_read_b64_tr_b16 v[206:207], v200 offset:12288\n"
"ds_read_b64_tr_b16 v[208:209], v200 offset:8200\n"
"ds_read_b64_tr_b16 v[210:211], v200 offset:12296\n"
"ds_read_b64_tr_b16 v[212:213], v200 offset:8704\n"
"ds_read_b64_tr_b16 v[214:215], v200 offset:12800\n"
"ds_read_b64_tr_b16 v[216:217], v200 offset:8712\n"
"ds_read_b64_tr_b16 v[218:219], v200 offset:12808\n"
"ds_read_b64_tr_b16 v[220:221], v200 offset:9216\n"
"ds_read_b64_tr_b16 v[222:223], v200 offset:13312\n"
"ds_read_b64_tr_b16 v[224:225], v200 offset:9224\n"
"ds_read_b64_tr_b16 v[226:227], v200 offset:13320\n"
"ds_read_b64_tr_b16 v[228:229], v200 offset:9728\n"
"ds_read_b64_tr_b16 v[230:231], v200 offset:13824\n"
"ds_read_b64_tr_b16 v[232:233], v200 offset:9736\n"
"ds_read_b64_tr_b16 v[234:235], v200 offset:13832\n"
"v_add_u32 v201, 98304, v201\n"
"ds_read_b128 v[144:147], v201 offset:16384\n"
"ds_read_b128 v[148:151], v201 offset:18432\n"
"ds_read_b128 v[152:155], v201 offset:20480\n"
"ds_read_b128 v[156:159], v201 offset:16896\n"
"ds_read_b128 v[160:163], v201 offset:18944\n"
"ds_read_b128 v[164:167], v201 offset:20992\n"
"s_add_u32 s71, s62, 786432\n"
"s_add_u32 m0, s61, 73728\n"
"s_and_b32 s71, s71, 4194303\n"
"s_add_u32 s71, s71, s64\n"
"buffer_load_dwordx4 v202, s[36:39], s71 offen lds\n"
"s_add_u32 m0, s61, 74624\n"
"s_nop 0\n"
"buffer_load_dwordx4 v202, s[36:39], s71 offen offset:128 lds\n"
"s_add_u32 s71, s62, 786432\n"
"s_add_u32 m0, s61, 24320\n"
"s_and_b32 s71, s71, 4194303\n"
"s_add_u32 s71, s71, s64\n"
"buffer_load_dwordx4 v202, s[36:39], s71 offen offset:256 lds\n"
"s_add_u32 m0, s61, 25216\n"
"s_nop 0\n"
"buffer_load_dwordx4 v202, s[36:39], s71 offen offset:384 lds\n"
"s_waitcnt vmcnt(11)\n"
"s_barrier\n"
"; step -17\n"
"s_waitcnt lgkmcnt(15)\n"
"s_waitcnt lgkmcnt(5)\n"
"v_mfma_f32_16x16x32_bf16 v[0:3], v[204:207], v[144:147], 0\n"
"s_waitcnt lgkmcnt(4)\n"
"v_mfma_f32_16x16x32_bf16 v[4:7], v[204:207], v[148:151], 0\n"
"s_waitcnt lgkmcnt(3)\n"
"v_mfma_f32_16x16x32_bf16 v[8:11], v[204:207], v[152:155], 0\n"
"v_add_u32 v200, -98304, v200\n"
"ds_read_b64_tr_b16 v[204:205], v200 offset:8192\n"
"ds_read_b64_tr_b16 v[206:207], v200 offset:12288\n"
"v_mfma_f32_16x16x32_bf16 v[12:15], v[208:211], v[144:147], 0\n"
"v_mfma_f32_16x16x32_bf16 v[16:19], v[208:211], v[148:151], 0\n"
"s_add_u32 s71, s62, 1048576\n"
"s_add_u32 m0, s61, 81920\n"
"s_and_b32 s71, s71, 4194303\n"
"s_add_u32 s71, s71, s64\n"
"buffer_load_dwordx4 v202, s[36:39], s71 offen lds\n"
"s_add_u32 m0, s61, 82816\n"
"s_nop 0\n"
"buffer_load_dwordx4 v202, s[36:39], s71 offen offset:128 lds\n"
"v_mfma_f32_16x16x32_bf16 v[20:23], v[208:211], v[152:155], 0\n"
"ds_read_b64_tr_b16 v[208:209], v200 offset:8200\n"
"ds_read_b64_tr_b16 v[210:211], v200 offset:12296\n"
"v_mfma_f32_16x16x32_bf16 v[24:27], v[212:215], v[144:147], 0\n"
"v_mfma_f32_16x16x32_bf16 v[28:31], v[212:215], v[148:151], 0\n"
"v_mfma_f32_16x16x32_bf16 v[32:35], v[212:215], v[152:155], 0\n"
"ds_read_b64_tr_b16 v[212:213], v200 offset:8704\n"
"ds_read_b64_tr_b16 v[214:215], v200 offset:12800\n"
"v_mfma_f32_16x16x32_bf16 v[36:39], v[216:219], v[144:147], 0\n"
"v_mfma_f32_16x16x32_bf16 v[40:43], v[216:219], v[148:151], 0\n"
"v_mfma_f32_16x16x32_bf16 v[44:47], v[216:219], v[152:155], 0\n"
"ds_read_b64_tr_b16 v[216:217], v200 offset:8712\n"
"ds_read_b64_tr_b16 v[218:219], v200 offset:12808\n"
"v_mfma_f32_16x16x32_bf16 v[48:51], v[220:223], v[144:147], 0\n"
"v_mfma_f32_16x16x32_bf16 v[52:55], v[220:223], v[148:151], 0\n"
"v_mfma_f32_16x16x32_bf16 v[56:59], v[220:223], v[152:155], 0\n"
"ds_read_b64_tr_b16 v[220:221], v200 offset:9216\n"
"ds_read_b64_tr_b16 v[222:223], v200 offset:13312\n"
"v_mfma_f32_16x16x32_bf16 v[60:63], v[224:227], v[144:147], 0\n"
"v_mfma_f32_16x16x32_bf16 v[64:67], v[224:227], v[148:151], 0\n"
"s_add_u32 s71, s62, 1048576\n"
"s_add_u32 m0, s61, 89856\n"
"s_and_b32 s71, s71, 4194303\n"
"s_add_u32 s71, s71, s64\n"
"buffer_load_dwordx4 v202, s[36:39], s71 offen offset:256 lds\n"
"s_add_u32 m0, s61, 90752\n"
"s_nop 0\n"
"buffer_load_dwordx4 v202, s[36:39], s71 offen offset:384 lds\n"
"v_mfma_f32_16x16x32_bf16 v[68:71], v[224:227], v[152:155], 0\n"
"ds_read_b64_tr_b16 v[224:225], v200 offset:9224\n"
"ds_read_b64_tr_b16 v[226:227], v200 offset:13320\n"
"v_mfma_f32_16x16x32_bf16 v[72:75], v[228:231], v[144:147], 0\n"
"v_mfma_f32_16x16x32_bf16 v[76:79], v[228:231], v[148:151], 0\n"
"v_mfma_f32_16x16x32_bf16 v[80:83], v[228:231], v[152:155], 0\n"
"ds_read_b64_tr_b16 v[228:229], v200 offset:9728\n"
"ds_read_b64_tr_b16 v[230:231], v200 offset:13824\n"
"v_mfma_f32_16x16x32_bf16 v[84:87], v[232:235], v[144:147], 0\n"
"v_mfma_f32_16x16x32_bf16 v[88:91], v[232:235], v[148:151], 0\n"
"v_mfma_f32_16x16x32_bf16 v[92:95], v[232:235], v[152:155], 0\n"
"ds_read_b64_tr_b16 v[232:233], v200 offset:9736\n"
"ds_read_b64_tr_b16 v[234:235], v200 offset:13832\n"
"s_waitcnt vmcnt(11)\n"
"s_waitcnt lgkmcnt(15)\n"
"s_barrier\n"
"; step -16\n"
"s_waitcnt lgkmcnt(12)\n"
"v_mfma_f32_16x16x32_bf16 v[0:3], v[204:207], v[156:159], v[0:3]\n"
"v_mfma_f32_16x16x32_bf16 v[4:7], v[204:207], v[160:163], v[4:7]\n"
"ds_read_b128 v[144:147], v201 offset:40960\n"
"ds_read_b128 v[148:151], v201 offset:43008\n"
"ds_read_b128 v[152:155], v201 offset:45056\n"
"v_mfma_f32_16x16x32_bf16 v[8:11], v[204:207], v[164:167], v[8:11]\n"
"ds_read_b64_tr_b16 v[204:205], v200 offset:16384\n"
"ds_read_b64_tr_b16 v[206:207], v200 offset:20480\n"
"v_mfma_f32_16x16x32_bf16 v[12:15], v[208:211], v[156:159], v[12:15]\n"
"v_mfma_f32_16x16x32_bf16 v[16:19], v[208:211], v[160:163], v[16:19]\n"
"v_mfma_f32_16x16x32_bf16 v[20:23], v[208:211], v[164:167], v[20:23]\n"
"ds_read_b64_tr_b16 v[208:209], v200 offset:16392\n"
"ds_read_b64_tr_b16 v[210:211], v200 offset:20488\n"
"s_waitcnt lgkmcnt(15)\n"
"v_mfma_f32_16x16x32_bf16 v[24:27], v[212:215], v[156:159], v[24:27]\n"
"v_mfma_f32_16x16x32_bf16 v[28:31], v[212:215], v[160:163], v[28:31]\n"
"v_mfma_f32_16x16x32_bf16 v[32:35], v[212:215], v[164:167], v[32:35]\n"
"ds_read_b64_tr_b16 v[212:213], v200 offset:16896\n"
"ds_read_b64_tr_b16 v[214:215], v200 offset:20992\n"
"v_mfma_f32_16x16x32_bf16 v[36:39], v[216:219], v[156:159], v[36:39]\n"
"s_add_u32 s71, s62, 1310720\n"
"s_add_u32 m0, s61, 98304\n"
"s_and_b32 s71, s71, 4194303\n"
"s_add_u32 s71, s71, s64\n"
"buffer_load_dwordx4 v202, s[36:39], s71 offen lds\n"
"s_add_u32 m0, s61, 99200\n"
"s_nop 0\n"
"buffer_load_dwordx4 v202, s[36:39], s71 offen offset:128 lds\n"
"v_mfma_f32_16x16x32_bf16 v[40:43], v[216:219], v[160:163], v[40:43]\n"
"v_mfma_f32_16x16x32_bf16 v[44:47], v[216:219], v[164:167], v[44:47]\n"
"ds_read_b64_tr_b16 v[216:217], v200 offset:16904\n"
"ds_read_b64_tr_b16 v[218:219], v200 offset:21000\n"
"s_waitcnt lgkmcnt(15)\n"
"v_mfma_f32_16x16x32_bf16 v[48:51], v[220:223], v[156:159], v[48:51]\n"
"v_mfma_f32_16x16x32_bf16 v[52:55], v[220:223], v[160:163], v[52:55]\n"
"v_mfma_f32_16x16x32_bf16 v[56:59], v[220:223], v[164:167], v[56:59]\n"
"ds_read_b64_tr_b16 v[220:221], v200 offset:17408\n"
"ds_read_b64_tr_b16 v[222:223], v200 offset:21504\n"
"v_mfma_f32_16x16x32_bf16 v[60:63], v[224:227], v[156:159], v[60:63]\n"
"v_mfma_f32_16x16x32_bf16 v[64:67], v[224:227], v[160:163], v[64:67]\n"
"v_mfma_f32_16x16x32_bf16 v[68:71], v[224:227], v[164:167], v[68:71]\n"
"ds_read_b64_tr_b16 v[224:225], v200 offset:17416\n"
"ds_read_b64_tr_b16 v[226:227], v200 offset:21512\n"
"s_waitcnt lgkmcnt(15)\n"
"v_mfma_f32_16x16x32_bf16 v[72:75], v[228:231], v[156:159], v[72:75]\n"
"s_add_u32 s71, s62, 1310720\n"
"s_add_u32 m0, s61, 106240\n"
"s_and_b32 s71, s71, 4194303\n"
"s_add_u32 s71, s71, s64\n"
"buffer_load_dwordx4 v202, s[36:39], s71 offen offset:256 lds\n"
"s_add_u32 m0, s61, 107136\n"
"s_nop 0\n"
"buffer_load_dwordx4 v202, s[36:39], s71 offen offset:384 lds\n"
"v_mfma_f32_16x16x32_bf16 v[76:79], v[228:231], v[160:163], v[76:79]\n"
"v_mfma_f32_16x16x32_bf16 v[80:83], v[228:231], v[164:167], v[80:83]\n"
"ds_read_b64_tr_b16 v[228:229], v200 offset:17920\n"
"ds_read_b64_tr_b16 v[230:231], v200 offset:22016\n"
"v_mfma_f32_16x16x32_bf16 v[84:87], v[232:235], v[156:159], v[84:87]\n"
"v_mfma_f32_16x16x32_bf16 v[88:91], v[232:235], v[160:163], v[88:91]\n"
"v_mfma_f32_16x16x32_bf16 v[92:95], v[232:235], v[164:167], v[92:95]\n"
"ds_read_b64_tr_b16 v[232:233], v200 offset:17928\n"
"ds_read_b64_tr_b16 v[234:235], v200 offset:22024\n"
"ds_read_b128 v[156:159], v201 offset:41472\n"
"ds_read_b128 v[160:163], v201 offset:43520\n"
"ds_read_b128 v[164:167], v201 offset:45568\n"
"s_waitcnt vmcnt(8)\n"
"s_barrier\n"
"; step -15\n"
"s_waitcnt lgkmcnt(15)\n"
"v_mfma_f32_16x16x32_bf16 v[0:3], v[204:207], v[144:147], v[0:3]\n"
"v_mfma_f32_16x16x32_bf16 v[4:7], v[204:207], v[148:151], v[4:7]\n"
"v_mfma_f32_16x16x32_bf16 v[8:11], v[204:207], v[152:155], v[8:11]\n"
"ds_read_b64_tr_b16 v[204:205], v200 offset:24576\n"
"ds_read_b64_tr_b16 v[206:207], v200 offset:28672\n"
"v_mfma_f32_16x16x32_bf16 v[12:15], v[208:211], v[144:147], v[12:15]\n"
"v_mfma_f32_16x16x32_bf16 v[16:19], v[208:211], v[148:151], v[16:19]\n"
"s_add_u32 s71, s62, 1572864\n"
"s_add_u32 m0, s61, 0\n"
"s_and_b32 s71, s71, 4194303\n"
"s_add_u32 s71, s71, s64\n"
"buffer_load_dwordx4 v202, s[36:39], s71 offen lds\n"
"s_add_u32 m0, s61, 896\n"
"s_nop 0\n"
"buffer_load_dwordx4 v202, s[36:39], s71 offen offset:128 lds\n"
"v_mfma_f32_16x16x32_bf16 v[20:23], v[208:211], v[152:155], v[20:23]\n"
"ds_read_b64_tr_b16 v[208:209], v200 offset:24584\n"
"ds_read_b64_tr_b16 v[210:211], v200 offset:28680\n"
"s_waitcnt lgkmcnt(15)\n"
"v_mfma_f32_16x16x32_bf16 v[24:27], v[212:215], v[144:147], v[24:27]\n"
"v_mfma_f32_16x16x32_bf16 v[28:31], v[212:215], v[148:151], v[28:31]\n"
"v_mfma_f32_16x16x32_bf16 v[32:35], v[212:215], v[152:155], v[32:35]\n"
"ds_read_b64_tr_b16 v[212:213], v200 offset:25088\n"
"ds_read_b64_tr_b16 v[214:215], v200 offset:29184\n"
"v_mfma_f32_16x16x32_bf16 v[36:39], v[216:219], v[144:147], v[36:39]\n"
"v_mfma_f32_16x16x32_bf16 v[40:43], v[216:219], v[148:151], v[40:43]\n"
"v_mfma_f32_16x16x32_bf16 v[44:47], v[216:219], v[152:155], v[44:47]\n"
"ds_read_b64_tr_b16 v[216:217], v200 offset:25096\n"
"ds_read_b64_tr_b16 v[218:219], v200 offset:29192\n"
"s_waitcnt lgkmcnt(15)\n"
"v_mfma_f32_16x16x32_bf16 v[48:51], v[220:223], v[144:147], v[48:51]\n"
"v_mfma_f32_16x16x32_bf16 v[52:55], v[220:223], v[148:151], v[52:55]\n"
"v_mfma_f32_16x16x32_bf16 v[56:59], v[220:223], v[152:155], v[56:59]\n"
"ds_read_b64_tr_b16 v[220:221], v200 offset:25600\n"
"ds_read_b64_tr_b16 v[222:223], v200 offset:29696\n"
"v_mfma_f32_16x16x32_bf16 v[60:63], v[224:227], v[144:147], v[60:63]\n"
"v_mfma_f32_16x16x32_bf16 v[64:67], v[224:227], v[148:151], v[64:67]\n"
"s_add_u32 s71, s62, 1572864\n"
"s_add_u32 m0, s61, 7936\n"
"s_and_b32 s71, s71, 4194303\n"
"s_add_u32 s71, s71, s64\n"
"buffer_load_dwordx4 v202, s[36:39], s71 offen offset:256 lds\n"
"s_add_u32 m0, s61, 8832\n"
"s_nop 0\n"
"buffer_load_dwordx4 v202, s[36:39], s71 offen offset:384 lds\n"
"v_mfma_f32_16x16x32_bf16 v[68:71], v[224:227], v[152:155], v[68:71]\n"
"ds_read_b64_tr_b16 v[224:225], v200 offset:25608\n"
"ds_read_b64_tr_b16 v[226:227], v200 offset:29704\n"
"s_waitcnt lgkmcnt(15)\n"
"v_mfma_f32_16x16x32_bf16 v[72:75], v[228:231], v[144:147], v[72:75]\n"
"v_mfma_f32_16x16x32_bf16 v[76:79], v[228:231], v[148:151], v[76:79]\n"
"v_mfma_f32_16x16x32_bf16 v[80:83], v[228:231], v[152:155], v[80:83]\n"
"ds_read_b64_tr_b16 v[228:229], v200 offset:26112\n"
"ds_read_b64_tr_b16 v[230:231], v200 offset:30208\n"
"v_mfma_f32_16x16x32_bf16 v[84:87], v[232:235], v[144:147], v[84:87]\n"
"v_mfma_f32_16x16x32_bf16 v[88:91], v[232:235], v[148:151], v[88:91]\n"
"v_mfma_f32_16x16x32_bf16 v[92:95], v[232:235], v[152:155], v[92:95]\n"
"ds_read_b64_tr_b16 v[232:233], v200 offset:26120\n"
"ds_read_b64_tr_b16 v[234:235], v200 offset:30216\n"
"s_waitcnt vmcnt(8)\n"
"s_waitcnt lgkmcnt(15)\n"
"s_barrier\n"
"; step -14\n"
"s_waitcnt lgkmcnt(12)\n"
"v_mfma_f32_16x16x32_bf16 v[0:3], v[204:207], v[156:159], v[0:3]\n"
"v_mfma_f32_16x16x32_bf16 v[4:7], v[204:207], v[160:163], v[4:7]\n"
"v_add_u32 v201, -98304, v201\n"
"ds_read_b128 v[144:147], v201 offset:40960\n"
"ds_read_b128 v[148:151], v201 offset:43008\n"
"ds_read_b128 v[152:155], v201 offset:45056\n"
"v_mfma_f32_16x16x32_bf16 v[8:11], v[204:207], v[164:167], v[8:11]\n"
"v_add_u32 v200, 90112, v200\n"
"ds_read_b64_tr_b16 v[204:205], v200 offset:0\n"
"ds_read_b64_tr_b16 v[206:207], v200 offset:4096\n"
"v_mfma_f32_16x16x32_bf16 v[12:15], v[208:211], v[156:159], v[12:15]\n"
"v_mfma_f32_16x16x32_bf16 v[16:19], v[208:211], v[160:163], v[16:19]\n"
"v_mfma_f32_16x16x32_bf16 v[20:23], v[208:211], v[164:167], v[20:23]\n"
"ds_read_b64_tr_b16 v[208:209], v200 offset:8\n"
"ds_read_b64_tr_b16 v[210:211], v200 offset:4104\n"
"s_waitcnt lgkmcnt(15)\n"
"v_mfma_f32_16x16x32_bf16 v[24:27], v[212:215], v[156:159], v[24:27]\n"
"v_mfma_f32_16x16x32_bf16 v[28:31], v[212:215], v[160:163], v[28:31]\n"
"v_mfma_f32_16x16x32_bf16 v[32:35], v[212:215], v[164:167], v[32:35]\n"
"ds_read_b64_tr_b16 v[212:213], v200 offset:512\n"
"ds_read_b64_tr_b16 v[214:215], v200 offset:4608\n"
"v_mfma_f32_16x16x32_bf16 v[36:39], v[216:219], v[156:159], v[36:39]\n"
"s_add_u32 s71, s62, 1835008\n"
"s_add_u32 m0, s61, 65536\n"
"s_and_b32 s71, s71, 4194303\n"
"s_add_u32 s71, s71, s64\n"
"buffer_load_dwordx4 v202, s[36:39], s71 offen lds\n"
"s_add_u32 m0, s61, 66432\n"
"s_nop 0\n"
"buffer_load_dwordx4 v202, s[36:39], s71 offen offset:128 lds\n"
"v_mfma_f32_16x16x32_bf16 v[40:43], v[216:219], v[160:163], v[40:43]\n"
"v_mfma_f32_16x16x32_bf16 v[44:47], v[216:219], v[164:167], v[44:47]\n"
"ds_read_b64_tr_b16 v[216:217], v200 offset:520\n"
"ds_read_b64_tr_b16 v[218:219], v200 offset:4616\n"
"s_waitcnt lgkmcnt(15)\n"
"v_mfma_f32_16x16x32_bf16 v[48:51], v[220:223], v[156:159], v[48:51]\n"
"v_mfma_f32_16x16x32_bf16 v[52:55], v[220:223], v[160:163], v[52:55]\n"
"v_mfma_f32_16x16x32_bf16 v[56:59], v[220:223], v[164:167], v[56:59]\n"
"ds_read_b64_tr_b16 v[220:221], v200 offset:1024\n"
"ds_read_b64_tr_b16 v[222:223], v200 offset:5120\n"
"v_mfma_f32_16x16x32_bf16 v[60:63], v[224:227], v[156:159], v[60:63]\n"
"v_mfma_f32_16x16x32_bf16 v[64:67], v[224:227], v[160:163], v[64:67]\n"
"v_mfma_f32_16x16x32_bf16 v[68:71], v[224:227], v[164:167], v[68:71]\n"
"ds_read_b64_tr_b16 v[224:225], v200 offset:1032\n"
"ds_read_b64_tr_b16 v[226:227], v200 offset:5128\n"
"s_waitcnt lgkmcnt(15)\n"
"v_mfma_f32_16x16x32_bf16 v[72:75], v[228:231], v[156:159], v[72:75]\n"
"s_add_u32 s71, s62, 1835008\n"
"s_add_u32 m0, s61, 16128\n"
"s_and_b32 s71, s71, 4194303\n"
"s_add_u32 s71, s71, s64\n"
"buffer_load_dwordx4 v202, s[36:39], s71 offen offset:256 lds\n"
"s_add_u32 m0, s61, 17024\n"
"s_nop 0\n"
"buffer_load_dwordx4 v202, s[36:39], s71 offen offset:384 lds\n"
"v_mfma_f32_16x16x32_bf16 v[76:79], v[228:231], v[160:163], v[76:79]\n"
"v_mfma_f32_16x16x32_bf16 v[80:83], v[228:231], v[164:167], v[80:83]\n"
"ds_read_b64_tr_b16 v[228:229], v200 offset:1536\n"
"ds_read_b64_tr_b16 v[230:231], v200 offset:5632\n"
"v_mfma_f32_16x16x32_bf16 v[84:87], v[232:235], v[156:159], v[84:87]\n"
"v_mfma_f32_16x16x32_bf16 v[88:91], v[232:235], v[160:163], v[88:91]\n"
"v_mfma_f32_16x16x32_bf16 v[92:95], v[232:235], v[164:167], v[92:95]\n"
"ds_read_b64_tr_b16 v[232:233], v200 offset:1544\n"
"ds_read_b64_tr_b16 v[234:235], v200 offset:5640\n"
"ds_read_b128 v[156:159], v201 offset:41472\n"
"ds_read_b128 v[160:163], v201 offset:43520\n"
"ds_read_b128 v[164:167], v201 offset:45568\n"
"s_waitcnt vmcnt(8)\n"
"s_barrier\n"
"; step -13\n"
"s_waitcnt lgkmcnt(15)\n"
"v_mfma_f32_16x16x32_bf16 v[0:3], v[204:207], v[144:147], v[0:3]\n"
"v_mfma_f32_16x16x32_bf16 v[4:7], v[204:207], v[148:151], v[4:7]\n"
"v_mfma_f32_16x16x32_bf16 v[8:11], v[204:207], v[152:155], v[8:11]\n"
"ds_read_b64_tr_b16 v[204:205], v200 offset:16384\n"
"ds_read_b64_tr_b16 v[206:207], v200 offset:20480\n"
"v_mfma_f32_16x16x32_bf16 v[12:15], v[208:211], v[144:147], v[12:15]\n"
"v_mfma_f32_16x16x32_bf16 v[16:19], v[208:211], v[148:151], v[16:19]\n"
"s_add_u32 s71, s62, 2097152\n"
"s_add_u32 m0, s61, 73728\n"
"s_and_b32 s71, s71, 4194303\n"
"s_add_u32 s71, s71, s64\n"
"buffer_load_dwordx4 v202, s[36:39], s71 offen lds\n"
"s_add_u32 m0, s61, 74624\n"
"s_nop 0\n"
"buffer_load_dwordx4 v202, s[36:39], s71 offen offset:128 lds\n"
"v_mfma_f32_16x16x32_bf16 v[20:23], v[208:211], v[152:155], v[20:23]\n"
"ds_read_b64_tr_b16 v[208:209], v200 offset:16392\n"
"ds_read_b64_tr_b16 v[210:211], v200 offset:20488\n"
"s_waitcnt lgkmcnt(15)\n"
"v_mfma_f32_16x16x32_bf16 v[24:27], v[212:215], v[144:147], v[24:27]\n"
"v_mfma_f32_16x16x32_bf16 v[28:31], v[212:215], v[148:151], v[28:31]\n"
"v_mfma_f32_16x16x32_bf16 v[32:35], v[212:215], v[152:155], v[32:35]\n"
"ds_read_b64_tr_b16 v[212:213], v200 offset:16896\n"
"ds_read_b64_tr_b16 v[214:215], v200 offset:20992\n"
"v_mfma_f32_16x16x32_bf16 v[36:39], v[216:219], v[144:147], v[36:39]\n"
"v_mfma_f32_16x16x32_bf16 v[40:43], v[216:219], v[148:151], v[40:43]\n"
"v_mfma_f32_16x16x32_bf16 v[44:47], v[216:219], v[152:155], v[44:47]\n"
"ds_read_b64_tr_b16 v[216:217], v200 offset:16904\n"
"ds_read_b64_tr_b16 v[218:219], v200 offset:21000\n"
"s_waitcnt lgkmcnt(15)\n"
"v_mfma_f32_16x16x32_bf16 v[48:51], v[220:223], v[144:147], v[48:51]\n"
"v_mfma_f32_16x16x32_bf16 v[52:55], v[220:223], v[148:151], v[52:55]\n"
"v_mfma_f32_16x16x32_bf16 v[56:59], v[220:223], v[152:155], v[56:59]\n"
"ds_read_b64_tr_b16 v[220:221], v200 offset:17408\n"
"ds_read_b64_tr_b16 v[222:223], v200 offset:21504\n"
"v_mfma_f32_16x16x32_bf16 v[60:63], v[224:227], v[144:147], v[60:63]\n"
"v_mfma_f32_16x16x32_bf16 v[64:67], v[224:227], v[148:151], v[64:67]\n"
"s_add_u32 s71, s62, 2097152\n"
"s_add_u32 m0, s61, 24320\n"
"s_and_b32 s71, s71, 4194303\n"
"s_add_u32 s71, s71, s64\n"
"buffer_load_dwordx4 v202, s[36:39], s71 offen offset:256 lds\n"
"s_add_u32 m0, s61, 25216\n"
"s_nop 0\n"
"buffer_load_dwordx4 v202, s[36:39], s71 offen offset:384 lds\n"
"v_mfma_f32_16x16x32_bf16 v[68:71], v[224:227], v[152:155], v[68:71]\n"
"ds_read_b64_tr_b16 v[224:225], v200 offset:17416\n"
"ds_read_b64_tr_b16 v[226:227], v200 offset:21512\n"
"s_waitcnt lgkmcnt(15)\n"
"v_mfma_f32_16x16x32_bf16 v[72:75], v[228:231], v[144:147], v[72:75]\n"
"v_mfma_f32_16x16x32_bf16 v[76:79], v[228:231], v[148:151], v[76:79]\n"
"v_mfma_f32_16x16x32_bf16 v[80:83], v[228:231], v[152:155], v[80:83]\n"
"ds_read_b64_tr_b16 v[228:229], v200 offset:17920\n"
"ds_read_b64_tr_b16 v[230:231], v200 offset:22016\n"
"v_mfma_f32_16x16x32_bf16 v[84:87], v[232:235], v[144:147], v[84:87]\n"
"v_mfma_f32_16x16x32_bf16 v[88:91], v[232:235], v[148:151], v[88:91]\n"
"v_mfma_f32_16x16x32_bf16 v[92:95], v[232:235], v[152:155], v[92:95]\n"
"ds_read_b64_tr_b16 v[232:233], v200 offset:17928\n"
"ds_read_b64_tr_b16 v[234:235], v200 offset:22024\n"
"s_waitcnt vmcnt(8)\n"
"s_waitcnt lgkmcnt(15)\n"
"s_barrier\n"
"; step -12\n"
"s_waitcnt lgkmcnt(12)\n"
"v_mfma_f32_16x16x32_bf16 v[0:3], v[204:207], v[156:159], v[0:3]\n"
"v_mfma_f32_16x16x32_bf16 v[4:7], v[204:207], v[160:163], v[4:7]\n"
"v_add_u32 v201, 98304, v201\n"
"ds_read_b128 v[144:147], v201 offset:16384\n"
"ds_read_b128 v[148:151], v201 offset:18432\n"
"ds_read_b128 v[152:155], v201 offset:20480\n"
"v_mfma_f32_16x16x32_bf16 v[8:11], v[204:207], v[164:167], v[8:11]\n"
"v_add_u32 v200, -90112, v200\n"
"ds_read_b64_tr_b16 v[204:205], v200 offset:8192\n"
"ds_read_b64_tr_b16 v[206:207], v200 offset:12288\n"
"v_mfma_f32_16x16x32_bf16 v[12:15], v[208:211], v[156:159], v[12:15]\n"
"v_mfma_f32_16x16x32_bf16 v[16:19], v[208:211], v[160:163], v[16:19]\n"
"v_mfma_f32_16x16x32_bf16 v[20:23], v[208:211], v[164:167], v[20:23]\n"
"ds_read_b64_tr_b16 v[208:209], v200 offset:8200\n"
"ds_read_b64_tr_b16 v[210:211], v200 offset:12296\n"
"s_waitcnt lgkmcnt(15)\n"
"v_mfma_f32_16x16x32_bf16 v[24:27], v[212:215], v[156:159], v[24:27]\n"
"v_mfma_f32_16x16x32_bf16 v[28:31], v[212:215], v[160:163], v[28:31]\n"
"v_mfma_f32_16x16x32_bf16 v[32:35], v[212:215], v[164:167], v[32:35]\n"
"ds_read_b64_tr_b16 v[212:213], v200 offset:8704\n"
"ds_read_b64_tr_b16 v[214:215], v200 offset:12800\n"
"v_mfma_f32_16x16x32_bf16 v[36:39], v[216:219], v[156:159], v[36:39]\n"
"s_add_u32 s71, s62, 2359296\n"
"s_add_u32 m0, s61, 81920\n"
"s_and_b32 s71, s71, 4194303\n"
"s_add_u32 s71, s71, s64\n"
"buffer_load_dwordx4 v202, s[36:39], s71 offen lds\n"
"s_add_u32 m0, s61, 82816\n"
"s_nop 0\n"
"buffer_load_dwordx4 v202, s[36:39], s71 offen offset:128 lds\n"
"v_mfma_f32_16x16x32_bf16 v[40:43], v[216:219], v[160:163], v[40:43]\n"
"v_mfma_f32_16x16x32_bf16 v[44:47], v[216:219], v[164:167], v[44:47]\n"
"ds_read_b64_tr_b16 v[216:217], v200 offset:8712\n"
"ds_read_b64_tr_b16 v[218:219], v200 offset:12808\n"
"s_waitcnt lgkmcnt(15)\n"
"v_mfma_f32_16x16x32_bf16 v[48:51], v[220:223], v[156:159], v[48:51]\n"
"v_mfma_f32_16x16x32_bf16 v[52:55], v[220:223], v[160:163], v[52:55]\n"
"v_mfma_f32_16x16x32_bf16 v[56:59], v[220:223], v[164:167], v[56:59]\n"
"ds_read_b64_tr_b16 v[220:221], v200 offset:9216\n"
"ds_read_b64_tr_b16 v[222:223], v200 offset:13312\n"
"v_mfma_f32_16x16x32_bf16 v[60:63], v[224:227], v[156:159], v[60:63]\n"
"v_mfma_f32_16x16x32_bf16 v[64:67], v[224:227], v[160:163], v[64:67]\n"
"v_mfma_f32_16x16x32_bf16 v[68:71], v[224:227], v[164:167], v[68:71]\n"
"ds_read_b64_tr_b16 v[224:225], v200 offset:9224\n"
"ds_read_b64_tr_b16 v[226:227], v200 offset:13320\n"
"s_waitcnt lgkmcnt(15)\n"
"v_mfma_f32_16x16x32_bf16 v[72:75], v[228:231], v[156:159], v[72:75]\n"
"s_add_u32 s71, s62, 2359296\n"
"s_add_u32 m0, s61, 89856\n"
"s_and_b32 s71, s71, 4194303\n"
"s_add_u32 s71, s71, s64\n"
"buffer_load_dwordx4 v202, s[36:39], s71 offen offset:256 lds\n"
"s_add_u32 m0, s61, 90752\n"
"s_nop 0\n"
"buffer_load_dwordx4 v202, s[36:39], s71 offen offset:384 lds\n"
"v_mfma_f32_16x16x32_bf16 v[76:79], v[228:231], v[160:163], v[76:79]\n"
"v_mfma_f32_16x16x32_bf16 v[80:83], v[228:231], v[164:167], v[80:83]\n"
"ds_read_b64_tr_b16 v[228:229], v200 offset:9728\n"
"ds_read_b64_tr_b16 v[230:231], v200 offset:13824\n"
"v_mfma_f32_16x16x32_bf16 v[84:87], v[232:235], v[156:159], v[84:87]\n"
"v_mfma_f32_16x16x32_bf16 v[88:91], v[232:235], v[160:163], v[88:91]\n"
"v_mfma_f32_16x16x32_bf16 v[92:95], v[232:235], v[164:167], v[92:95]\n"
"ds_read_b64_tr_b16 v[232:233], v200 offset:9736\n"
"ds_read_b64_tr_b16 v[234:235], v200 offset:13832\n"
"ds_read_b128 v[156:159], v201 offset:16896\n"
"ds_read_b128 v[160:163], v201 offset:18944\n"
"ds_read_b128 v[164:167], v201 offset:20992\n"
"s_waitcnt vmcnt(8)\n"
"s_barrier\n"
"; step -11\n"
"s_waitcnt lgkmcnt(15)\n"
"v_mfma_f32_16x16x32_bf16 v[0:3], v[204:207], v[144:147], v[0:3]\n"
"v_mfma_f32_16x16x32_bf16 v[4:7], v[204:207], v[148:151], v[4:7]\n"
"v_mfma_f32_16x16x32_bf16 v[8:11], v[204:207], v[152:155], v[8:11]\n"
"ds_read_b64_tr_b16 v[204:205], v200 offset:16384\n"
"ds_read_b64_tr_b16 v[206:207], v200 offset:20480\n"
"v_mfma_f32_16x16x32_bf16 v[12:15], v[208:211], v[144:147], v[12:15]\n"
"s_add_u32 s71, s62, 2621440\n"
"s_add_u32 m0, s61, 98304\n"
"s_and_b32 s71, s71, 4194303\n"
"s_add_u32 s71, s71, s64\n"
"buffer_load_dwordx4 v202, s[36:39], s71 offen lds\n"
"s_add_u32 m0, s61, 99200\n"
"s_nop 0\n"
"buffer_load_dwordx4 v202, s[36:39], s71 offen offset:128 lds\n"
"v_mfma_f32_16x16x32_bf16 v[16:19], v[208:211], v[148:151], v[16:19]\n"
"v_mfma_f32_16x16x32_bf16 v[20:23], v[208:211], v[152:155], v[20:23]\n"
"ds_read_b64_tr_b16 v[208:209], v200 offset:16392\n"
"ds_read_b64_tr_b16 v[210:211], v200 offset:20488\n"
"s_waitcnt lgkmcnt(15)\n"
"v_mfma_f32_16x16x32_bf16 v[24:27], v[212:215], v[144:147], v[24:27]\n"
"v_mfma_f32_16x16x32_bf16 v[28:31], v[212:215], v[148:151], v[28:31]\n"
"v_mfma_f32_16x16x32_bf16 v[32:35], v[212:215], v[152:155], v[32:35]\n"
"ds_read_b64_tr_b16 v[212:213], v200 offset:16896\n"
"ds_read_b64_tr_b16 v[214:215], v200 offset:20992\n"
"v_mfma_f32_16x16x32_bf16 v[36:39], v[216:219], v[144:147], v[36:39]\n"
"v_mfma_f32_16x16x32_bf16 v[40:43], v[216:219], v[148:151], v[40:43]\n"
"s_add_u32 s71, s62, 2621440\n"
"s_add_u32 m0, s61, 106240\n"
"s_and_b32 s71, s71, 4194303\n"
"s_add_u32 s71, s71, s64\n"
"buffer_load_dwordx4 v202, s[36:39], s71 offen offset:256 lds\n"
"s_add_u32 m0, s61, 107136\n"
"s_nop 0\n"
"buffer_load_dwordx4 v202, s[36:39], s71 offen offset:384 lds\n"
"v_mfma_f32_16x16x32_bf16 v[44:47], v[216:219], v[152:155], v[44:47]\n"
"ds_read_b64_tr_b16 v[216:217], v200 offset:16904\n"
"ds_read_b64_tr_b16 v[218:219], v200 offset:21000\n"
"s_waitcnt lgkmcnt(15)\n"
"v_mfma_f32_16x16x32_bf16 v[48:51], v[220:223], v[144:147], v[48:51]\n"
"v_mfma_f32_16x16x32_bf16 v[52:55], v[220:223], v[148:151], v[52:55]\n"
"v_mfma_f32_16x16x32_bf16 v[56:59], v[220:223], v[152:155], v[56:59]\n"
"ds_read_b64_tr_b16 v[220:221], v200 offset:17408\n"
"ds_read_b64_tr_b16 v[222:223], v200 offset:21504\n"
"v_mfma_f32_16x16x32_bf16 v[60:63], v[224:227], v[144:147], v[60:63]\n"
"v_mfma_f32_16x16x32_bf16 v[64:67], v[224:227], v[148:151], v[64:67]\n"
"v_mfma_f32_16x16x32_bf16 v[68:71], v[224:227], v[152:155], v[68:71]\n"
"ds_read_b64_tr_b16 v[224:225], v200 offset:17416\n"
"ds_read_b64_tr_b16 v[226:227], v200 offset:21512\n"
"s_waitcnt lgkmcnt(15)\n"
"v_mfma_f32_16x16x32_bf16 v[72:75], v[228:231], v[144:147], v[72:75]\n"
"v_mfma_f32_16x16x32_bf16 v[76:79], v[228:231], v[148:151], v[76:79]\n"
"s_add_u32 m0, s61, 32768\n"
"s_add_u32 s71, s77, 0\n"
"buffer_load_dwordx4 v254, s[20:23], s71 offen nt lds\n"
"s_add_u32 m0, s61, 33664\n"
"s_nop 0\n"
"buffer_load_dwordx4 v254, s[20:23], s71 offen offset:128 nt lds\n"
"v_mfma_f32_16x16x32_bf16 v[80:83], v[228:231], v[152:155], v[80:83]\n"
"ds_read_b64_tr_b16 v[228:229], v200 offset:17920\n"
"ds_read_b64_tr_b16 v[230:231], v200 offset:22016\n"
"v_mfma_f32_16x16x32_bf16 v[84:87], v[232:235], v[144:147], v[84:87]\n"
"v_mfma_f32_16x16x32_bf16 v[88:91], v[232:235], v[148:151], v[88:91]\n"
"v_mfma_f32_16x16x32_bf16 v[92:95], v[232:235], v[152:155], v[92:95]\n"
"ds_read_b64_tr_b16 v[232:233], v200 offset:17928\n"
"ds_read_b64_tr_b16 v[234:235], v200 offset:22024\n"
"s_waitcnt vmcnt(10)\n"
"s_waitcnt lgkmcnt(15)\n"
"s_barrier\n"
"; step -10\n"
"s_waitcnt lgkmcnt(12)\n"
"v_mfma_f32_16x16x32_bf16 v[0:3], v[204:207], v[156:159], v[0:3]\n"
"v_mfma_f32_16x16x32_bf16 v[4:7], v[204:207], v[160:163], v[4:7]\n"
"ds_read_b128 v[144:147], v201 offset:40960\n"
"ds_read_b128 v[148:151], v201 offset:43008\n"
"ds_read_b128 v[152:155], v201 offset:45056\n"
"v_mfma_f32_16x16x32_bf16 v[8:11], v[204:207], v[164:167], v[8:11]\n"
"ds_read_b64_tr_b16 v[204:205], v200 offset:24576\n"
"ds_read_b64_tr_b16 v[206:207], v200 offset:28672\n"
"v_mfma_f32_16x16x32_bf16 v[12:15], v[208:211], v[156:159], v[12:15]\n"
"v_mfma_f32_16x16x32_bf16 v[16:19], v[208:211], v[160:163], v[16:19]\n"
"v_mfma_f32_16x16x32_bf16 v[20:23], v[208:211], v[164:167], v[20:23]\n"
"ds_read_b64_tr_b16 v[208:209], v200 offset:24584\n"
"ds_read_b64_tr_b16 v[210:211], v200 offset:28680\n"
"s_waitcnt lgkmcnt(15)\n"
"v_mfma_f32_16x16x32_bf16 v[24:27], v[212:215], v[156:159], v[24:27]\n"
"v_mfma_f32_16x16x32_bf16 v[28:31], v[212:215], v[160:163], v[28:31]\n"
"v_mfma_f32_16x16x32_bf16 v[32:35], v[212:215], v[164:167], v[32:35]\n"
"ds_read_b64_tr_b16 v[212:213], v200 offset:25088\n"
"ds_read_b64_tr_b16 v[214:215], v200 offset:29184\n"
"v_mfma_f32_16x16x32_bf16 v[36:39], v[216:219], v[156:159], v[36:39]\n"
"s_add_u32 s71, s62, 2883584\n"
"s_add_u32 m0, s61, 0\n"
"s_and_b32 s71, s71, 4194303\n"
"s_add_u32 s71, s71, s64\n"
"buffer_load_dwordx4 v202, s[36:39], s71 offen lds\n"
"s_add_u32 m0, s61, 896\n"
"s_nop 0\n"
"buffer_load_dwordx4 v202, s[36:39], s71 offen offset:128 lds\n"
"v_mfma_f32_16x16x32_bf16 v[40:43], v[216:219], v[160:163], v[40:43]\n"
"v_mfma_f32_16x16x32_bf16 v[44:47], v[216:219], v[164:167], v[44:47]\n"
"ds_read_b64_tr_b16 v[216:217], v200 offset:25096\n"
"ds_read_b64_tr_b16 v[218:219], v200 offset:29192\n"
"s_waitcnt lgkmcnt(15)\n"
"v_mfma_f32_16x16x32_bf16 v[48:51], v[220:223], v[156:159], v[48:51]\n"
"v_mfma_f32_16x16x32_bf16 v[52:55], v[220:223], v[160:163], v[52:55]\n"
"v_mfma_f32_16x16x32_bf16 v[56:59], v[220:223], v[164:167], v[56:59]\n"
"ds_read_b64_tr_b16 v[220:221], v200 offset:25600\n"
"ds_read_b64_tr_b16 v[222:223], v200 offset:29696\n"
"v_mfma_f32_16x16x32_bf16 v[60:63], v[224:227], v[156:159], v[60:63]\n"
"v_mfma_f32_16x16x32_bf16 v[64:67], v[224:227], v[160:163], v[64:67]\n"
"v_mfma_f32_16x16x32_bf16 v[68:71], v[224:227], v[164:167], v[68:71]\n"
"ds_read_b64_tr_b16 v[224:225], v200 offset:25608\n"
"ds_read_b64_tr_b16 v[226:227], v200 offset:29704\n"
"s_waitcnt lgkmcnt(15)\n"
"v_mfma_f32_16x16x32_bf16 v[72:75], v[228:231], v[156:159], v[72:75]\n"
"s_add_u32 s71, s62, 2883584\n"
"s_add_u32 m0, s61, 7936\n"
"s_and_b32 s71, s71, 4194303\n"
"s_add_u32 s71, s71, s64\n"
"buffer_load_dwordx4 v202, s[36:39], s71 offen offset:256 lds\n"
"s_add_u32 m0, s61, 8832\n"
"s_nop 0\n"
"buffer_load_dwordx4 v202, s[36:39], s71 offen offset:384 lds\n"
"v_mfma_f32_16x16x32_bf16 v[76:79], v[228:231], v[160:163], v[76:79]\n"
"v_mfma_f32_16x16x32_bf16 v[80:83], v[228:231], v[164:167], v[80:83]\n"
"ds_read_b64_tr_b16 v[228:229], v200 offset:26112\n"
"ds_read_b64_tr_b16 v[230:231], v200 offset:30208\n"
"v_mfma_f32_16x16x32_bf16 v[84:87], v[232:235], v[156:159], v[84:87]\n"
"v_mfma_f32_16x16x32_bf16 v[88:91], v[232:235], v[160:163], v[88:91]\n"
"v_mfma_f32_16x16x32_bf16 v[92:95], v[232:235], v[164:167], v[92:95]\n"
"ds_read_b64_tr_b16 v[232:233], v200 offset:26120\n"
"ds_read_b64_tr_b16 v[234:235], v200 offset:30216\n"
"ds_read_b128 v[156:159], v201 offset:41472\n"
"ds_read_b128 v[160:163], v201 offset:43520\n"
"ds_read_b128 v[164:167], v201 offset:45568\n"
"s_waitcnt vmcnt(10)\n"
"s_barrier\n"
"; step -9\n"
"s_waitcnt lgkmcnt(15)\n"
"v_mfma_f32_16x16x32_bf16 v[0:3], v[204:207], v[144:147], v[0:3]\n"
"v_mfma_f32_16x16x32_bf16 v[4:7], v[204:207], v[148:151], v[4:7]\n"
"v_mfma_f32_16x16x32_bf16 v[8:11], v[204:207], v[152:155], v[8:11]\n"
"v_add_u32 v200, 90112, v200\n"
"ds_read_b64_tr_b16 v[204:205], v200 offset:0\n"
"ds_read_b64_tr_b16 v[206:207], v200 offset:4096\n"
"v_mfma_f32_16x16x32_bf16 v[12:15], v[208:211], v[144:147], v[12:15]\n"
"v_mfma_f32_16x16x32_bf16 v[16:19], v[208:211], v[148:151], v[16:19]\n"
"s_add_u32 s71, s62, 3145728\n"
"s_add_u32 m0, s61, 65536\n"
"s_and_b32 s71, s71, 4194303\n"
"s_add_u32 s71, s71, s64\n"
"buffer_load_dwordx4 v202, s[36:39], s71 offen lds\n"
"s_add_u32 m0, s61, 66432\n"
"s_nop 0\n"
"buffer_load_dwordx4 v202, s[36:39], s71 offen offset:128 lds\n"
"v_mfma_f32_16x16x32_bf16 v[20:23], v[208:211], v[152:155], v[20:23]\n"
"ds_read_b64_tr_b16 v[208:209], v200 offset:8\n"
"ds_read_b64_tr_b16 v[210:211], v200 offset:4104\n"
"s_waitcnt lgkmcnt(15)\n"
"v_mfma_f32_16x16x32_bf16 v[24:27], v[212:215], v[144:147], v[24:27]\n"
"v_mfma_f32_16x16x32_bf16 v[28:31], v[212:215], v[148:151], v[28:31]\n"
"v_mfma_f32_16x16x32_bf16 v[32:35], v[212:215], v[152:155], v[32:35]\n"
"ds_read_b64_tr_b16 v[212:213], v200 offset:512\n"
"ds_read_b64_tr_b16 v[214:215], v200 offset:4608\n"
"v_mfma_f32_16x16x32_bf16 v[36:39], v[216:219], v[144:147], v[36:39]\n"
"v_mfma_f32_16x16x32_bf16 v[40:43], v[216:219], v[148:151], v[40:43]\n"
"v_mfma_f32_16x16x32_bf16 v[44:47], v[216:219], v[152:155], v[44:47]\n"
"ds_read_b64_tr_b16 v[216:217], v200 offset:520\n"
"ds_read_b64_tr_b16 v[218:219], v200 offset:4616\n"
"s_waitcnt lgkmcnt(15)\n"
"v_mfma_f32_16x16x32_bf16 v[48:51], v[220:223], v[144:147], v[48:51]\n"
"v_mfma_f32_16x16x32_bf16 v[52:55], v[220:223], v[148:151], v[52:55]\n"
"v_mfma_f32_16x16x32_bf16 v[56:59], v[220:223], v[152:155], v[56:59]\n"
"ds_read_b64_tr_b16 v[220:221], v200 offset:1024\n"
"ds_read_b64_tr_b16 v[222:223], v200 offset:5120\n"
"v_mfma_f32_16x16x32_bf16 v[60:63], v[224:227], v[144:147], v[60:63]\n"
"v_mfma_f32_16x16x32_bf16 v[64:67], v[224:227], v[148:151], v[64:67]\n"
"s_add_u32 s71, s62, 3145728\n"
"s_add_u32 m0, s61, 16128\n"
"s_and_b32 s71, s71, 4194303\n"
"s_add_u32 s71, s71, s64\n"
"buffer_load_dwordx4 v202, s[36:39], s71 offen offset:256 lds\n"
"s_add_u32 m0, s61, 17024\n"
"s_nop 0\n"
"buffer_load_dwordx4 v202, s[36:39], s71 offen offset:384 lds\n"
"v_mfma_f32_16x16x32_bf16 v[68:71], v[224:227], v[152:155], v[68:71]\n"
"ds_read_b64_tr_b16 v[224:225], v200 offset:1032\n"
"ds_read_b64_tr_b16 v[226:227], v200 offset:5128\n"
"s_waitcnt lgkmcnt(15)\n"
"v_mfma_f32_16x16x32_bf16 v[72:75], v[228:231], v[144:147], v[72:75]\n"
"v_mfma_f32_16x16x32_bf16 v[76:79], v[228:231], v[148:151], v[76:79]\n"
"v_mfma_f32_16x16x32_bf16 v[80:83], v[228:231], v[152:155], v[80:83]\n"
"ds_read_b64_tr_b16 v[228:229], v200 offset:1536\n"
"ds_read_b64_tr_b16 v[230:231], v200 offset:5632\n"
"v_mfma_f32_16x16x32_bf16 v[84:87], v[232:235], v[144:147], v[84:87]\n"
"v_mfma_f32_16x16x32_bf16 v[88:91], v[232:235], v[148:151], v[88:91]\n"
"v_mfma_f32_16x16x32_bf16 v[92:95], v[232:235], v[152:155], v[92:95]\n"
"ds_read_b64_tr_b16 v[232:233], v200 offset:1544\n"
"ds_read_b64_tr_b16 v[234:235], v200 offset:5640\n"
"s_waitcnt vmcnt(10)\n"
"s_waitcnt lgkmcnt(15)\n"
"s_barrier\n"
"; step -8\n"
"s_waitcnt lgkmcnt(12)\n"
"v_mfma_f32_16x16x32_bf16 v[0:3], v[204:207], v[156:159], v[0:3]\n"
"v_mfma_f32_16x16x32_bf16 v[4:7], v[204:207], v[160:163], v[4:7]\n"
"v_add_u32 v201, -98304, v201\n"
"ds_read_b128 v[144:147], v201 offset:40960\n"
"ds_read_b128 v[148:151], v201 offset:43008\n"
"ds_read_b128 v[152:155], v201 offset:45056\n"
"v_mfma_f32_16x16x32_bf16 v[8:11], v[204:207], v[164:167], v[8:11]\n"
"ds_read_b64_tr_b16 v[204:205], v200 offset:16384\n"
"ds_read_b64_tr_b16 v[206:207], v200 offset:20480\n"
"v_mfma_f32_16x16x32_bf16 v[12:15], v[208:211], v[156:159], v[12:15]\n"
"v_mfma_f32_16x16x32_bf16 v[16:19], v[208:211], v[160:163], v[16:19]\n"
"v_mfma_f32_16x16x32_bf16 v[20:23], v[208:211], v[164:167], v[20:23]\n"
"ds_read_b64_tr_b16 v[208:209], v200 offset:16392\n"
"ds_read_b64_tr_b16 v[210:211], v200 offset:20488\n"
"s_waitcnt lgkmcnt(15)\n"
"v_mfma_f32_16x16x32_bf16 v[24:27], v[212:215], v[156:159], v[24:27]\n"
"v_mfma_f32_16x16x32_bf16 v[28:31], v[212:215], v[160:163], v[28:31]\n"
"v_mfma_f32_16x16x32_bf16 v[32:35], v[212:215], v[164:167], v[32:35]\n"
"ds_read_b64_tr_b16 v[212:213], v200 offset:16896\n"
"ds_read_b64_tr_b16 v[214:215], v200 offset:20992\n"
"v_mfma_f32_16x16x32_bf16 v[36:39], v[216:219], v[156:159], v[36:39]\n"
"s_add_u32 s71, s62, 3407872\n"
"s_add_u32 m0, s61, 73728\n"
"s_and_b32 s71, s71, 4194303\n"
"s_add_u32 s71, s71, s64\n"
"buffer_load_dwordx4 v202, s[36:39], s71 offen lds\n"
"s_add_u32 m0, s61, 74624\n"
"s_nop 0\n"
"buffer_load_dwordx4 v202, s[36:39], s71 offen offset:128 lds\n"
"v_mfma_f32_16x16x32_bf16 v[40:43], v[216:219], v[160:163], v[40:43]\n"
"v_mfma_f32_16x16x32_bf16 v[44:47], v[216:219], v[164:167], v[44:47]\n"
"ds_read_b64_tr_b16 v[216:217], v200 offset:16904\n"
"ds_read_b64_tr_b16 v[218:219], v200 offset:21000\n"
"s_waitcnt lgkmcnt(15)\n"
"v_mfma_f32_16x16x32_bf16 v[48:51], v[220:223], v[156:159], v[48:51]\n"
"v_mfma_f32_16x16x32_bf16 v[52:55], v[220:223], v[160:163], v[52:55]\n"
"v_mfma_f32_16x16x32_bf16 v[56:59], v[220:223], v[164:167], v[56:59]\n"
"ds_read_b64_tr_b16 v[220:221], v200 offset:17408\n"
"ds_read_b64_tr_b16 v[222:223], v200 offset:21504\n"
"v_mfma_f32_16x16x32_bf16 v[60:63], v[224:227], v[156:159], v[60:63]\n"
"v_mfma_f32_16x16x32_bf16 v[64:67], v[224:227], v[160:163], v[64:67]\n"
"v_mfma_f32_16x16x32_bf16 v[68:71], v[224:227], v[164:167], v[68:71]\n"
"ds_read_b64_tr_b16 v[224:225], v200 offset:17416\n"
"ds_read_b64_tr_b16 v[226:227], v200 offset:21512\n"
"s_waitcnt lgkmcnt(15)\n"
"v_mfma_f32_16x16x32_bf16 v[72:75], v[228:231], v[156:159], v[72:75]\n"
"s_add_u32 s71, s62, 3407872\n"
"s_add_u32 m0, s61, 24320\n"
"s_and_b32 s71, s71, 4194303\n"
"s_add_u32 s71, s71, s64\n"
"buffer_load_dwordx4 v202, s[36:39], s71 offen offset:256 lds\n"
"s_add_u32 m0, s61, 25216\n"
"s_nop 0\n"
"buffer_load_dwordx4 v202, s[36:39], s71 offen offset:384 lds\n"
"v_mfma_f32_16x16x32_bf16 v[76:79], v[228:231], v[160:163], v[76:79]\n"
"v_mfma_f32_16x16x32_bf16 v[80:83], v[228:231], v[164:167], v[80:83]\n"
"ds_read_b64_tr_b16 v[228:229], v200 offset:17920\n"
"ds_read_b64_tr_b16 v[230:231], v200 offset:22016\n"
"v_mfma_f32_16x16x32_bf16 v[84:87], v[232:235], v[156:159], v[84:87]\n"
"v_mfma_f32_16x16x32_bf16 v[88:91], v[232:235], v[160:163], v[88:91]\n"
"v_mfma_f32_16x16x32_bf16 v[92:95], v[232:235], v[164:167], v[92:95]\n"
"ds_read_b64_tr_b16 v[232:233], v200 offset:17928\n"
"ds_read_b64_tr_b16 v[234:235], v200 offset:22024\n"
"ds_read_b128 v[156:159], v201 offset:41472\n"
"ds_read_b128 v[160:163], v201 offset:43520\n"
"ds_read_b128 v[164:167], v201 offset:45568\n"
"s_waitcnt vmcnt(8)\n"
"s_barrier\n"
"; step -7\n"
"s_waitcnt lgkmcnt(15)\n"
"v_mfma_f32_16x16x32_bf16 v[0:3], v[204:207], v[144:147], v[0:3]\n"
"v_mfma_f32_16x16x32_bf16 v[4:7], v[204:207], v[148:151], v[4:7]\n"
"v_mfma_f32_16x16x32_bf16 v[8:11], v[204:207], v[152:155], v[8:11]\n"
"v_add_u32 v200, -90112, v200\n"
"ds_read_b64_tr_b16 v[204:205], v200 offset:8192\n"
"ds_read_b64_tr_b16 v[206:207], v200 offset:12288\n"
"v_mfma_f32_16x16x32_bf16 v[12:15], v[208:211], v[144:147], v[12:15]\n"
"v_mfma_f32_16x16x32_bf16 v[16:19], v[208:211], v[148:151], v[16:19]\n"
"s_add_u32 s71, s62, 3670016\n"
"s_add_u32 m0, s61, 81920\n"
"s_and_b32 s71, s71, 4194303\n"
"s_add_u32 s71, s71, s64\n"
"buffer_load_dwordx4 v202, s[36:39], s71 offen lds\n"
"s_add_u32 m0, s61, 82816\n"
"s_nop 0\n"
"buffer_load_dwordx4 v202, s[36:39], s71 offen offset:128 lds\n"
"v_mfma_f32_16x16x32_bf16 v[20:23], v[208:211], v[152:155], v[20:23]\n"
"ds_read_b64_tr_b16 v[208:209], v200 offset:8200\n"
"ds_read_b64_tr_b16 v[210:211], v200 offset:12296\n"
"s_waitcnt lgkmcnt(15)\n"
"v_mfma_f32_16x16x32_bf16 v[24:27], v[212:215], v[144:147], v[24:27]\n"
"v_mfma_f32_16x16x32_bf16 v[28:31], v[212:215], v[148:151], v[28:31]\n"
"v_mfma_f32_16x16x32_bf16 v[32:35], v[212:215], v[152:155], v[32:35]\n"
"ds_read_b64_tr_b16 v[212:213], v200 offset:8704\n"
"ds_read_b64_tr_b16 v[214:215], v200 offset:12800\n"
"v_mfma_f32_16x16x32_bf16 v[36:39], v[216:219], v[144:147], v[36:39]\n"
"v_mfma_f32_16x16x32_bf16 v[40:43], v[216:219], v[148:151], v[40:43]\n"
"v_mfma_f32_16x16x32_bf16 v[44:47], v[216:219], v[152:155], v[44:47]\n"
"ds_read_b64_tr_b16 v[216:217], v200 offset:8712\n"
"ds_read_b64_tr_b16 v[218:219], v200 offset:12808\n"
"s_waitcnt lgkmcnt(15)\n"
"v_mfma_f32_16x16x32_bf16 v[48:51], v[220:223], v[144:147], v[48:51]\n"
"v_mfma_f32_16x16x32_bf16 v[52:55], v[220:223], v[148:151], v[52:55]\n"
"v_mfma_f32_16x16x32_bf16 v[56:59], v[220:223], v[152:155], v[56:59]\n"
"ds_read_b64_tr_b16 v[220:221], v200 offset:9216\n"
"ds_read_b64_tr_b16 v[222:223], v200 offset:13312\n"
"v_mfma_f32_16x16x32_bf16 v[60:63], v[224:227], v[144:147], v[60:63]\n"
"v_mfma_f32_16x16x32_bf16 v[64:67], v[224:227], v[148:151], v[64:67]\n"
"s_add_u32 s71, s62, 3670016\n"
"s_add_u32 m0, s61, 89856\n"
"s_and_b32 s71, s71, 4194303\n"
"s_add_u32 s71, s71, s64\n"
"buffer_load_dwordx4 v202, s[36:39], s71 offen offset:256 lds\n"
"s_add_u32 m0, s61, 90752\n"
"s_nop 0\n"
"buffer_load_dwordx4 v202, s[36:39], s71 offen offset:384 lds\n"
"v_mfma_f32_16x16x32_bf16 v[68:71], v[224:227], v[152:155], v[68:71]\n"
"ds_read_b64_tr_b16 v[224:225], v200 offset:9224\n"
"ds_read_b64_tr_b16 v[226:227], v200 offset:13320\n"
"s_waitcnt lgkmcnt(15)\n"
"v_mfma_f32_16x16x32_bf16 v[72:75], v[228:231], v[144:147], v[72:75]\n"
"v_mfma_f32_16x16x32_bf16 v[76:79], v[228:231], v[148:151], v[76:79]\n"
"v_mfma_f32_16x16x32_bf16 v[80:83], v[228:231], v[152:155], v[80:83]\n"
"ds_read_b64_tr_b16 v[228:229], v200 offset:9728\n"
"ds_read_b64_tr_b16 v[230:231], v200 offset:13824\n"
"v_mfma_f32_16x16x32_bf16 v[84:87], v[232:235], v[144:147], v[84:87]\n"
"v_mfma_f32_16x16x32_bf16 v[88:91], v[232:235], v[148:151], v[88:91]\n"
"v_mfma_f32_16x16x32_bf16 v[92:95], v[232:235], v[152:155], v[92:95]\n"
"ds_read_b64_tr_b16 v[232:233], v200 offset:9736\n"
"ds_read_b64_tr_b16 v[234:235], v200 offset:13832\n"
"s_waitcnt vmcnt(8)\n"
"s_waitcnt lgkmcnt(15)\n"
"s_barrier\n"
"; step -6\n"
"s_waitcnt lgkmcnt(12)\n"
"v_mfma_f32_16x16x32_bf16 v[0:3], v[204:207], v[156:159], v[0:3]\n"
"v_add_u32 v201, 98304, v201\n"
"ds_read_b128 v[144:147], v201 offset:16384\n"
"ds_read_b128 v[148:151], v201 offset:18432\n"
"ds_read_b128 v[152:155], v201 offset:20480\n"
"v_mfma_f32_16x16x32_bf16 v[4:7], v[204:207], v[160:163], v[4:7]\n"
"v_mfma_f32_16x16x32_bf16 v[8:11], v[204:207], v[164:167], v[8:11]\n"
"ds_read_b64_tr_b16 v[204:205], v200 offset:16384\n"
"ds_read_b64_tr_b16 v[206:207], v200 offset:20480\n"
"v_mfma_f32_16x16x32_bf16 v[12:15], v[208:211], v[156:159], v[12:15]\n"
"v_mfma_f32_16x16x32_bf16 v[16:19], v[208:211], v[160:163], v[16:19]\n"
"s_add_u32 s71, s62, 3932160\n"
"s_add_u32 m0, s61, 98304\n"
"s_and_b32 s71, s71, 4194303\n"
"s_add_u32 s71, s71, s64\n"
"buffer_load_dwordx4 v202, s[36:39], s71 offen lds\n"
"s_add_u32 m0, s61, 99200\n"
"s_nop 0\n"
"buffer_load_dwordx4 v202, s[36:39], s71 offen offset:128 lds\n"
"v_mfma_f32_16x16x32_bf16 v[20:23], v[208:211], v[164:167], v[20:23]\n"
"ds_read_b64_tr_b16 v[208:209], v200 offset:16392\n"
"ds_read_b64_tr_b16 v[210:211], v200 offset:20488\n"
"s_waitcnt lgkmcnt(15)\n"
"v_mfma_f32_16x16x32_bf16 v[24:27], v[212:215], v[156:159], v[24:27]\n"
"v_mfma_f32_16x16x32_bf16 v[28:31], v[212:215], v[160:163], v[28:31]\n"
"v_mfma_f32_16x16x32_bf16 v[32:35], v[212:215], v[164:167], v[32:35]\n"
"ds_read_b64_tr_b16 v[212:213], v200 offset:16896\n"
"ds_read_b64_tr_b16 v[214:215], v200 offset:20992\n"
"v_mfma_f32_16x16x32_bf16 v[36:39], v[216:219], v[156:159], v[36:39]\n"
"s_add_u32 s71, s62, 3932160\n"
"s_add_u32 m0, s61, 106240\n"
"s_and_b32 s71, s71, 4194303\n"
"s_add_u32 s71, s71, s64\n"
"buffer_load_dwordx4 v202, s[36:39], s71 offen offset:256 lds\n"
"s_add_u32 m0, s61, 107136\n"
"s_nop 0\n"
"buffer_load_dwordx4 v202, s[36:39], s71 offen offset:384 lds\n"
"v_mfma_f32_16x16x32_bf16 v[40:43], v[216:219], v[160:163], v[40:43]\n"
"v_mfma_f32_16x16x32_bf16 v[44:47], v[216:219], v[164:167], v[44:47]\n"
"ds_read_b64_tr_b16 v[216:217], v200 offset:16904\n"
"ds_read_b64_tr_b16 v[218:219], v200 offset:21000\n"
"s_waitcnt lgkmcnt(15)\n"
"v_mfma_f32_16x16x32_bf16 v[48:51], v[220:223], v[156:159], v[48:51]\n"
"s_add_u32 m0, s61, 49152\n"
"s_add_u32 s71, s77, 0\n"
"buffer_load_dwordx4 v254, s[24:27], s71 offen nt lds\n"
"s_add_u32 m0, s61, 50048\n"
"s_nop 0\n"
"buffer_load_dwordx4 v254, s[24:27], s71 offen offset:128 nt lds\n"
"v_mfma_f32_16x16x32_bf16 v[52:55], v[220:223], v[160:163], v[52:55]\n"
"v_mfma_f32_16x16x32_bf16 v[56:59], v[220:223], v[164:167], v[56:59]\n"
"ds_read_b64_tr_b16 v[220:221], v200 offset:17408\n"
"ds_read_b64_tr_b16 v[222:223], v200 offset:21504\n"
"v_mfma_f32_16x16x32_bf16 v[60:63], v[224:227], v[156:159], v[60:63]\n"
"v_mfma_f32_16x16x32_bf16 v[64:67], v[224:227], v[160:163], v[64:67]\n"
"s_add_u32 m0, s61, 40960\n"
"s_add_u32 s71, s77, 8192\n"
"buffer_load_dwordx4 v254, s[20:23], s71 offen nt lds\n"
"s_add_u32 m0, s61, 41856\n"
"s_nop 0\n"
"buffer_load_dwordx4 v254, s[20:23], s71 offen offset:128 nt lds\n"
"v_mfma_f32_16x16x32_bf16 v[68:71], v[224:227], v[164:167], v[68:71]\n"
"ds_read_b64_tr_b16 v[224:225], v200 offset:17416\n"
"ds_read_b64_tr_b16 v[226:227], v200 offset:21512\n"
"s_waitcnt lgkmcnt(15)\n"
"v_mfma_f32_16x16x32_bf16 v[72:75], v[228:231], v[156:159], v[72:75]\n"
"v_mfma_f32_16x16x32_bf16 v[76:79], v[228:231], v[160:163], v[76:79]\n"
"v_mfma_f32_16x16x32_bf16 v[80:83], v[228:231], v[164:167], v[80:83]\n"
"ds_read_b64_tr_b16 v[228:229], v200 offset:17920\n"
"ds_read_b64_tr_b16 v[230:231], v200 offset:22016\n"
"v_mfma_f32_16x16x32_bf16 v[84:87], v[232:235], v[156:159], v[84:87]\n"
"s_add_u32 m0, s61, 57344\n"
"s_add_u32 s71, s77, 8192\n"
"buffer_load_dwordx4 v254, s[24:27], s71 offen nt lds\n"
"s_add_u32 m0, s61, 58240\n"
"s_nop 0\n"
"buffer_load_dwordx4 v254, s[24:27], s71 offen offset:128 nt lds\n"
"v_mfma_f32_16x16x32_bf16 v[88:91], v[232:235], v[160:163], v[88:91]\n"
"v_mfma_f32_16x16x32_bf16 v[92:95], v[232:235], v[164:167], v[92:95]\n"
"ds_read_b64_tr_b16 v[232:233], v200 offset:17928\n"
"ds_read_b64_tr_b16 v[234:235], v200 offset:22024\n"
"ds_read_b128 v[156:159], v201 offset:16896\n"
"ds_read_b128 v[160:163], v201 offset:18944\n"
"ds_read_b128 v[164:167], v201 offset:20992\n"
"s_waitcnt vmcnt(14)\n"
"s_barrier\n"
"; step -5\n"
"s_waitcnt lgkmcnt(15)\n"
"v_mfma_f32_16x16x32_bf16 v[0:3], v[204:207], v[144:147], v[0:3]\n"
"v_mfma_f32_16x16x32_bf16 v[4:7], v[204:207], v[148:151], v[4:7]\n"
"v_mfma_f32_16x16x32_bf16 v[8:11], v[204:207], v[152:155], v[8:11]\n"
"ds_read_b64_tr_b16 v[204:205], v200 offset:24576\n"
"ds_read_b64_tr_b16 v[206:207], v200 offset:28672\n"
"v_mfma_f32_16x16x32_bf16 v[12:15], v[208:211], v[144:147], v[12:15]\n"
"v_mfma_f32_16x16x32_bf16 v[16:19], v[208:211], v[148:151], v[16:19]\n"
"s_add_u32 m0, s61, 0\n"
"s_add_u32 s71, s77, 16384\n"
"buffer_load_dwordx4 v254, s[20:23], s71 offen nt lds\n"
"s_add_u32 m0, s61, 896\n"
"s_nop 0\n"
"buffer_load_dwordx4 v254, s[20:23], s71 offen offset:128 nt lds\n"
"v_mfma_f32_16x16x32_bf16 v[20:23], v[208:211], v[152:155], v[20:23]\n"
"ds_read_b64_tr_b16 v[208:209], v200 offset:24584\n"
"ds_read_b64_tr_b16 v[210:211], v200 offset:28680\n"
"s_waitcnt lgkmcnt(15)\n"
"v_mfma_f32_16x16x32_bf16 v[24:27], v[212:215], v[144:147], v[24:27]\n"
"v_mfma_f32_16x16x32_bf16 v[28:31], v[212:215], v[148:151], v[28:31]\n"
"v_mfma_f32_16x16x32_bf16 v[32:35], v[212:215], v[152:155], v[32:35]\n"
"ds_read_b64_tr_b16 v[212:213], v200 offset:25088\n"
"ds_read_b64_tr_b16 v[214:215], v200 offset:29184\n"
"v_mfma_f32_16x16x32_bf16 v[36:39], v[216:219], v[144:147], v[36:39]\n"
"v_mfma_f32_16x16x32_bf16 v[40:43], v[216:219], v[148:151], v[40:43]\n"
"v_mfma_f32_16x16x32_bf16 v[44:47], v[216:219], v[152:155], v[44:47]\n"
"ds_read_b64_tr_b16 v[216:217], v200 offset:25096\n"
"ds_read_b64_tr_b16 v[218:219], v200 offset:29192\n"
"s_waitcnt lgkmcnt(15)\n"
"v_mfma_f32_16x16x32_bf16 v[48:51], v[220:223], v[144:147], v[48:51]\n"
"v_mfma_f32_16x16x32_bf16 v[52:55], v[220:223], v[148:151], v[52:55]\n"
"v_mfma_f32_16x16x32_bf16 v[56:59], v[220:223], v[152:155], v[56:59]\n"
"ds_read_b64_tr_b16 v[220:221], v200 offset:25600\n"
"ds_read_b64_tr_b16 v[222:223], v200 offset:29696\n"
"v_mfma_f32_16x16x32_bf16 v[60:63], v[224:227], v[144:147], v[60:63]\n"
"v_mfma_f32_16x16x32_bf16 v[64:67], v[224:227], v[148:151], v[64:67]\n"
"s_add_u32 m0, s61, 8192\n"
"s_add_u32 s71, s77, 24576\n"
"buffer_load_dwordx4 v254, s[20:23], s71 offen nt lds\n"
"s_add_u32 m0, s61, 9088\n"
"s_nop 0\n"
"buffer_load_dwordx4 v254, s[20:23], s71 offen offset:128 nt lds\n"
"v_mfma_f32_16x16x32_bf16 v[68:71], v[224:227], v[152:155], v[68:71]\n"
"ds_read_b64_tr_b16 v[224:225], v200 offset:25608\n"
"ds_read_b64_tr_b16 v[226:227], v200 offset:29704\n"
"s_waitcnt lgkmcnt(15)\n"
"v_mfma_f32_16x16x32_bf16 v[72:75], v[228:231], v[144:147], v[72:75]\n"
"v_mfma_f32_16x16x32_bf16 v[76:79], v[228:231], v[148:151], v[76:79]\n"
"v_mfma_f32_16x16x32_bf16 v[80:83], v[228:231], v[152:155], v[80:83]\n"
"ds_read_b64_tr_b16 v[228:229], v200 offset:26112\n"
"ds_read_b64_tr_b16 v[230:231], v200 offset:30208\n"
"v_mfma_f32_16x16x32_bf16 v[84:87], v[232:235], v[144:147], v[84:87]\n"
"v_mfma_f32_16x16x32_bf16 v[88:91], v[232:235], v[148:151], v[88:91]\n"
"v_mfma_f32_16x16x32_bf16 v[92:95], v[232:235], v[152:155], v[92:95]\n"
"ds_read_b64_tr_b16 v[232:233], v200 offset:26120\n"
"ds_read_b64_tr_b16 v[234:235], v200 offset:30216\n"
"s_waitcnt vmcnt(14)\n"
"s_waitcnt lgkmcnt(15)\n"
"s_barrier\n"
"; step -4\n"
"s_waitcnt lgkmcnt(12)\n"
"v_mfma_f32_16x16x32_bf16 v[0:3], v[204:207], v[156:159], v[0:3]\n"
"v_mfma_f32_16x16x32_bf16 v[4:7], v[204:207], v[160:163], v[4:7]\n"
"ds_read_b128 v[144:147], v201 offset:40960\n"
"ds_read_b128 v[148:151], v201 offset:43008\n"
"ds_read_b128 v[152:155], v201 offset:45056\n"
"v_mfma_f32_16x16x32_bf16 v[8:11], v[204:207], v[164:167], v[8:11]\n"
"v_add_u32 v200, 90112, v200\n"
"ds_read_b64_tr_b16 v[204:205], v200 offset:0\n"
"ds_read_b64_tr_b16 v[206:207], v200 offset:4096\n"
"v_mfma_f32_16x16x32_bf16 v[12:15], v[208:211], v[156:159], v[12:15]\n"
"v_mfma_f32_16x16x32_bf16 v[16:19], v[208:211], v[160:163], v[16:19]\n"
"v_mfma_f32_16x16x32_bf16 v[20:23], v[208:211], v[164:167], v[20:23]\n"
"ds_read_b64_tr_b16 v[208:209], v200 offset:8\n"
"ds_read_b64_tr_b16 v[210:211], v200 offset:4104\n"
"s_waitcnt lgkmcnt(15)\n"
"v_mfma_f32_16x16x32_bf16 v[24:27], v[212:215], v[156:159], v[24:27]\n"
"v_mfma_f32_16x16x32_bf16 v[28:31], v[212:215], v[160:163], v[28:31]\n"
"v_mfma_f32_16x16x32_bf16 v[32:35], v[212:215], v[164:167], v[32:35]\n"
"ds_read_b64_tr_b16 v[212:213], v200 offset:512\n"
"ds_read_b64_tr_b16 v[214:215], v200 offset:4608\n"
"v_mfma_f32_16x16x32_bf16 v[36:39], v[216:219], v[156:159], v[36:39]\n"
"s_add_u32 m0, s61, 65536\n"
"s_add_u32 s71, s77, 16384\n"
"buffer_load_dwordx4 v254, s[24:27], s71 offen nt lds\n"
"s_add_u32 m0, s61, 66432\n"
"s_nop 0\n"
"buffer_load_dwordx4 v254, s[24:27], s71 offen offset:128 nt lds\n"
"v_mfma_f32_16x16x32_bf16 v[40:43], v[216:219], v[160:163], v[40:43]\n"
"v_mfma_f32_16x16x32_bf16 v[44:47], v[216:219], v[164:167], v[44:47]\n"
"ds_read_b64_tr_b16 v[216:217], v200 offset:520\n"
"ds_read_b64_tr_b16 v[218:219], v200 offset:4616\n"
"s_waitcnt lgkmcnt(15)\n"
"v_mfma_f32_16x16x32_bf16 v[48:51], v[220:223], v[156:159], v[48:51]\n"
"v_mfma_f32_16x16x32_bf16 v[52:55], v[220:223], v[160:163], v[52:55]\n"
"v_mfma_f32_16x16x32_bf16 v[56:59], v[220:223], v[164:167], v[56:59]\n"
"ds_read_b64_tr_b16 v[220:221], v200 offset:1024\n"
"ds_read_b64_tr_b16 v[222:223], v200 offset:5120\n"
"v_mfma_f32_16x16x32_bf16 v[60:63], v[224:227], v[156:159], v[60:63]\n"
"v_mfma_f32_16x16x32_bf16 v[64:67], v[224:227], v[160:163], v[64:67]\n"
"v_mfma_f32_16x16x32_bf16 v[68:71], v[224:227], v[164:167], v[68:71]\n"
"ds_read_b64_tr_b16 v[224:225], v200 offset:1032\n"
"ds_read_b64_tr_b16 v[226:227], v200 offset:5128\n"
"s_waitcnt lgkmcnt(15)\n"
"v_mfma_f32_16x16x32_bf16 v[72:75], v[228:231], v[156:159], v[72:75]\n"
"s_add_u32 m0, s61, 16384\n"
"s_add_u32 s71, s77, 32768\n"
"buffer_load_dwordx4 v254, s[20:23], s71 offen nt lds\n"
"s_add_u32 m0, s61, 17280\n"
"s_nop 0\n"
"buffer_load_dwordx4 v254, s[20:23], s71 offen offset:128 nt lds\n"
"v_mfma_f32_16x16x32_bf16 v[76:79], v[228:231], v[160:163], v[76:79]\n"
"v_mfma_f32_16x16x32_bf16 v[80:83], v[228:231], v[164:167], v[80:83]\n"
"ds_read_b64_tr_b16 v[228:229], v200 offset:1536\n"
"ds_read_b64_tr_b16 v[230:231], v200 offset:5632\n"
"v_mfma_f32_16x16x32_bf16 v[84:87], v[232:235], v[156:159], v[84:87]\n"
"v_mfma_f32_16x16x32_bf16 v[88:91], v[232:235], v[160:163], v[88:91]\n"
"v_mfma_f32_16x16x32_bf16 v[92:95], v[232:235], v[164:167], v[92:95]\n"
"ds_read_b64_tr_b16 v[232:233], v200 offset:1544\n"
"ds_read_b64_tr_b16 v[234:235], v200 offset:5640\n"
"ds_read_b128 v[156:159], v201 offset:41472\n"
"ds_read_b128 v[160:163], v201 offset:43520\n"
"ds_read_b128 v[164:167], v201 offset:45568\n"
"s_waitcnt vmcnt(14)\n"
"s_barrier\n"
"; step -3\n"
"s_waitcnt lgkmcnt(15)\n"
"v_mfma_f32_16x16x32_bf16 v[0:3], v[204:207], v[144:147], v[0:3]\n"
"v_mfma_f32_16x16x32_bf16 v[4:7], v[204:207], v[148:151], v[4:7]\n"
"v_mfma_f32_16x16x32_bf16 v[8:11], v[204:207], v[152:155], v[8:11]\n"
"ds_read_b64_tr_b16 v[204:205], v200 offset:16384\n"
"ds_read_b64_tr_b16 v[206:207], v200 offset:20480\n"
"v_mfma_f32_16x16x32_bf16 v[12:15], v[208:211], v[144:147], v[12:15]\n"
"v_mfma_f32_16x16x32_bf16 v[16:19], v[208:211], v[148:151], v[16:19]\n"
"s_add_u32 m0, s61, 73728\n"
"s_add_u32 s71, s77, 24576\n"
"buffer_load_dwordx4 v254, s[24:27], s71 offen nt lds\n"
"s_add_u32 m0, s61, 74624\n"
"s_nop 0\n"
"buffer_load_dwordx4 v254, s[24:27], s71 offen offset:128 nt lds\n"
"v_mfma_f32_16x16x32_bf16 v[20:23], v[208:211], v[152:155], v[20:23]\n"
"ds_read_b64_tr_b16 v[208:209], v200 offset:16392\n"
"ds_read_b64_tr_b16 v[210:211], v200 offset:20488\n"
"s_waitcnt lgkmcnt(15)\n"
"v_mfma_f32_16x16x32_bf16 v[24:27], v[212:215], v[144:147], v[24:27]\n"
"v_mfma_f32_16x16x32_bf16 v[28:31], v[212:215], v[148:151], v[28:31]\n"
"v_mfma_f32_16x16x32_bf16 v[32:35], v[212:215], v[152:155], v[32:35]\n"
"ds_read_b64_tr_b16 v[212:213], v200 offset:16896\n"
"ds_read_b64_tr_b16 v[214:215], v200 offset:20992\n"
"v_mfma_f32_16x16x32_bf16 v[36:39], v[216:219], v[144:147], v[36:39]\n"
"v_mfma_f32_16x16x32_bf16 v[40:43], v[216:219], v[148:151], v[40:43]\n"
"v_mfma_f32_16x16x32_bf16 v[44:47], v[216:219], v[152:155], v[44:47]\n"
"ds_read_b64_tr_b16 v[216:217], v200 offset:16904\n"
"ds_read_b64_tr_b16 v[218:219], v200 offset:21000\n"
"s_waitcnt lgkmcnt(15)\n"
"v_mfma_f32_16x16x32_bf16 v[48:51], v[220:223], v[144:147], v[48:51]\n"
"v_mfma_f32_16x16x32_bf16 v[52:55], v[220:223], v[148:151], v[52:55]\n"
"v_mfma_f32_16x16x32_bf16 v[56:59], v[220:223], v[152:155], v[56:59]\n"
"ds_read_b64_tr_b16 v[220:221], v200 offset:17408\n"
"ds_read_b64_tr_b16 v[222:223], v200 offset:21504\n"
"v_mfma_f32_16x16x32_bf16 v[60:63], v[224:227], v[144:147], v[60:63]\n"
"v_mfma_f32_16x16x32_bf16 v[64:67], v[224:227], v[148:151], v[64:67]\n"
"s_add_u32 m0, s61, 24576\n"
"s_add_u32 s71, s77, 40960\n"
"buffer_load_dwordx4 v254, s[20:23], s71 offen nt lds\n"
"s_add_u32 m0, s61, 25472\n"
"s_nop 0\n"
"buffer_load_dwordx4 v254, s[20:23], s71 offen offset:128 nt lds\n"
"v_mfma_f32_16x16x32_bf16 v[68:71], v[224:227], v[152:155], v[68:71]\n"
"ds_read_b64_tr_b16 v[224:225], v200 offset:17416\n"
"ds_read_b64_tr_b16 v[226:227], v200 offset:21512\n"
"s_waitcnt lgkmcnt(15)\n"
"v_mfma_f32_16x16x32_bf16 v[72:75], v[228:231], v[144:147], v[72:75]\n"
"v_mfma_f32_16x16x32_bf16 v[76:79], v[228:231], v[148:151], v[76:79]\n"
"v_mfma_f32_16x16x32_bf16 v[80:83], v[228:231], v[152:155], v[80:83]\n"
"ds_read_b64_tr_b16 v[228:229], v200 offset:17920\n"
"ds_read_b64_tr_b16 v[230:231], v200 offset:22016\n"
"v_mfma_f32_16x16x32_bf16 v[84:87], v[232:235], v[144:147], v[84:87]\n"
"v_mfma_f32_16x16x32_bf16 v[88:91], v[232:235], v[148:151], v[88:91]\n"
"v_mfma_f32_16x16x32_bf16 v[92:95], v[232:235], v[152:155], v[92:95]\n"
"ds_read_b64_tr_b16 v[232:233], v200 offset:17928\n"
"ds_read_b64_tr_b16 v[234:235], v200 offset:22024\n"
"s_waitcnt lgkmcnt(15)\n"
"s_barrier\n"
"; step -2\n"
"s_waitcnt lgkmcnt(12)\n"
"v_mfma_f32_16x16x32_bf16 v[0:3], v[204:207], v[156:159], v[0:3]\n"
"v_mfma_f32_16x16x32_bf16 v[4:7], v[204:207], v[160:163], v[4:7]\n"
"v_mfma_f32_16x16x32_bf16 v[8:11], v[204:207], v[164:167], v[8:11]\n"
"v_mfma_f32_16x16x32_bf16 v[12:15], v[208:211], v[156:159], v[12:15]\n"
"v_mfma_f32_16x16x32_bf16 v[16:19], v[208:211], v[160:163], v[16:19]\n"
"v_mfma_f32_16x16x32_bf16 v[20:23], v[208:211], v[164:167], v[20:23]\n"
"s_add_u32 m0, s61, 81920\n"
"s_add_u32 s71, s77, 32768\n"
"buffer_load_dwordx4 v254, s[24:27], s71 offen nt lds\n"
"s_add_u32 m0, s61, 82816\n"
"s_nop 0\n"
"buffer_load_dwordx4 v254, s[24:27], s71 offen offset:128 nt lds\n"
"s_waitcnt lgkmcnt(8)\n"
"v_mfma_f32_16x16x32_bf16 v[24:27], v[212:215], v[156:159], v[24:27]\n"
"v_mfma_f32_16x16x32_bf16 v[28:31], v[212:215], v[160:163], v[28:31]\n"
"v_mfma_f32_16x16x32_bf16 v[32:35], v[212:215], v[164:167], v[32:35]\n"
"v_mfma_f32_16x16x32_bf16 v[36:39], v[216:219], v[156:159], v[36:39]\n"
"v_mfma_f32_16x16x32_bf16 v[40:43], v[216:219], v[160:163], v[40:43]\n"
"v_mfma_f32_16x16x32_bf16 v[44:47], v[216:219], v[164:167], v[44:47]\n"
"s_waitcnt lgkmcnt(4)\n"
"v_mfma_f32_16x16x32_bf16 v[48:51], v[220:223], v[156:159], v[48:51]\n"
"v_mfma_f32_16x16x32_bf16 v[52:55], v[220:223], v[160:163], v[52:55]\n"
"v_mfma_f32_16x16x32_bf16 v[56:59], v[220:223], v[164:167], v[56:59]\n"
"v_mfma_f32_16x16x32_bf16 v[60:63], v[224:227], v[156:159], v[60:63]\n"
"v_mfma_f32_16x16x32_bf16 v[64:67], v[224:227], v[160:163], v[64:67]\n"
"v_mfma_f32_16x16x32_bf16 v[68:71], v[224:227], v[164:167], v[68:71]\n"
"s_add_u32 m0, s61, 90112\n"
"s_add_u32 s71, s77, 40960\n"
"buffer_load_dwordx4 v254, s[24:27], s71 offen nt lds\n"
"s_add_u32 m0, s61, 91008\n"
"s_nop 0\n"
"buffer_load_dwordx4 v254, s[24:27], s71 offen offset:128 nt lds\n"
"s_waitcnt lgkmcnt(0)\n"
"v_mfma_f32_16x16x32_bf16 v[72:75], v[228:231], v[156:159], v[72:75]\n"
"v_mfma_f32_16x16x32_bf16 v[76:79], v[228:231], v[160:163], v[76:79]\n"
"v_mfma_f32_16x16x32_bf16 v[80:83], v[228:231], v[164:167], v[80:83]\n"
"v_mfma_f32_16x16x32_bf16 v[84:87], v[232:235], v[156:159], v[84:87]\n"
"v_mfma_f32_16x16x32_bf16 v[88:91], v[232:235], v[160:163], v[88:91]\n"
"v_mfma_f32_16x16x32_bf16 v[92:95], v[232:235], v[164:167], v[92:95]\n"
"s_waitcnt vmcnt(18)\n"
"s_barrier\n"
"; step -1\n"
"s_waitcnt vmcnt(14)\n"
"s_barrier\n"
"; step 0\n"
"s_add_u32 m0, s61, 32768\n"
"s_add_u32 s71, s77, 49152\n"
"buffer_load_dwordx4 v254, s[20:23], s71 offen nt lds\n"
"s_add_u32 m0, s61, 33664\n"
"s_nop 0\n"
"buffer_load_dwordx4 v254, s[20:23], s71 offen offset:128 nt lds\n"
"s_and_b32 s71, s58, 7\n"
"s_lshl_b32 s71, s71, 17\n"
"s_mul_i32 s70, s56, 24576\n"
"s_add_u32 s71, s71, s70\n"
"s_add_u32 s71, s71, s66\n"
"v_lshrrev_b32 v196, 5, v253\n"
"v_lshlrev_b32 v196, 10, v196\n"
"v_and_b32 v197, 16, v253\n"
"v_lshl_add_u32 v196, v197, 3, v196\n"
"buffer_load_dword v199, v196, s[40:43], s71 offen\n"
"s_waitcnt vmcnt(15)\n"
"s_barrier\n"
"; step 1\n"
"s_add_u32 m0, s61, 49152\n"
"s_add_u32 s71, s77, 49152\n"
"buffer_load_dwordx4 v254, s[24:27], s71 offen nt lds\n"
"s_add_u32 m0, s61, 50048\n"
"s_nop 0\n"
"buffer_load_dwordx4 v254, s[24:27], s71 offen offset:128 nt lds\n"
"s_add_u32 m0, s61, 40960\n"
"s_add_u32 s71, s77, 57344\n"
"buffer_load_dwordx4 v254, s[20:23], s71 offen nt lds\n"
"s_add_u32 m0, s61, 41856\n"
"s_nop 0\n"
"buffer_load_dwordx4 v254, s[20:23], s71 offen offset:128 nt lds\n"
"s_waitcnt vmcnt(15)\n"
"s_barrier\n"
"; step 2\n"
"s_add_u32 m0, s61, 57344\n"
"s_add_u32 s71, s77, 57344\n"
"buffer_load_dwordx4 v254, s[24:27], s71 offen nt lds\n"
"s_add_u32 m0, s61, 58240\n"
"s_nop 0\n"
"buffer_load_dwordx4 v254, s[24:27], s71 offen offset:128 nt lds\n"
"s_add_u32 m0, s61, 0\n"
"s_add_u32 s71, s77, 65536\n"
"buffer_load_dwordx4 v254, s[20:23], s71 offen nt lds\n"
"s_add_u32 m0, s61, 896\n"
"s_nop 0\n"
"buffer_load_dwordx4 v254, s[20:23], s71 offen offset:128 nt lds\n"
"s_and_b32 s71, s58, 7\n"
"s_lshl_b32 s71, s71, 17\n"
"s_mul_i32 s70, s56, 24576\n"
"s_add_u32 s71, s71, s70\n"
"s_add_u32 s71, s71, s66\n"
"v_lshrrev_b32 v196, 5, v253\n"
"v_lshlrev_b32 v196, 10, v196\n"
"v_and_b32 v197, 16, v253\n"
"v_lshl_add_u32 v196, v197, 3, v196\n"
"buffer_load_dword v199, v196, s[40:43], s71 offen\n"
"v_mul_f32 v0, s76, v0\n"
"v_mul_f32 v1, s76, v1\n"
"v_mul_f32 v2, s76, v2\n"
"v_mul_f32 v3, s76, v3\n"
"v_mul_f32 v12, s76, v12\n"
"v_mul_f32 v13, s76, v13\n"
"v_mul_f32 v14, s76, v14\n"
"v_mul_f32 v15, s76, v15\n"
"v_cvt_pk_bf16_f32 v96, v0, v1\n"
"v_cvt_pk_bf16_f32 v97, v2, v3\n"
"v_cvt_pk_bf16_f32 v98, v12, v13\n"
"v_cvt_pk_bf16_f32 v99, v14, v15\n"
"v_mul_f32 v4, s76, v4\n"
"v_mul_f32 v5, s76, v5\n"
"v_mul_f32 v6, s76, v6\n"
"v_mul_f32 v7, s76, v7\n"
"v_mul_f32 v16, s76, v16\n"
"v_mul_f32 v17, s76, v17\n"
"v_mul_f32 v18, s76, v18\n"
"v_mul_f32 v19, s76, v19\n"
"v_cvt_pk_bf16_f32 v112, v4, v5\n"
"v_cvt_pk_bf16_f32 v113, v6, v7\n"
"v_cvt_pk_bf16_f32 v114, v16, v17\n"
"v_cvt_pk_bf16_f32 v115, v18, v19\n"
"v_mul_f32 v8, s76, v8\n"
"v_mul_f32 v9, s76, v9\n"
"v_mul_f32 v10, s76, v10\n"
"v_mul_f32 v11, s76, v11\n"
"v_mul_f32 v20, s76, v20\n"
"v_mul_f32 v21, s76, v21\n"
"v_mul_f32 v22, s76, v22\n"
"v_mul_f32 v23, s76, v23\n"
"v_cvt_pk_bf16_f32 v128, v8, v9\n"
"v_cvt_pk_bf16_f32 v129, v10, v11\n"
"v_cvt_pk_bf16_f32 v130, v20, v21\n"
"v_cvt_pk_bf16_f32 v131, v22, v23\n"
"s_waitcnt vmcnt(16)\n"
"s_barrier\n"
"; step 3\n"
"s_add_u32 m0, s61, 65536\n"
"s_add_u32 s71, s77, 65536\n"
"buffer_load_dwordx4 v254, s[24:27], s71 offen nt lds\n"
"s_add_u32 m0, s61, 66432\n"
"s_nop 0\n"
"buffer_load_dwordx4 v254, s[24:27], s71 offen offset:128 nt lds\n"
"s_add_u32 m0, s61, 8192\n"
"s_add_u32 s71, s77, 73728\n"
"buffer_load_dwordx4 v254, s[20:23], s71 offen nt lds\n"
"s_add_u32 m0, s61, 9088\n"
"s_nop 0\n"
"buffer_load_dwordx4 v254, s[20:23], s71 offen offset:128 nt lds\n"
"v_mul_f32 v24, s76, v24\n"
"v_mul_f32 v25, s76, v25\n"
"v_mul_f32 v26, s76, v26\n"
"v_mul_f32 v27, s76, v27\n"
"v_mul_f32 v36, s76, v36\n"
"v_mul_f32 v37, s76, v37\n"
"v_mul_f32 v38, s76, v38\n"
"v_mul_f32 v39, s76, v39\n"
"v_cvt_pk_bf16_f32 v100, v24, v25\n"
"v_cvt_pk_bf16_f32 v101, v26, v27\n"
"v_cvt_pk_bf16_f32 v102, v36, v37\n"
"v_cvt_pk_bf16_f32 v103, v38, v39\n"
"v_mul_f32 v28, s76, v28\n"
"v_mul_f32 v29, s76, v29\n"
"v_mul_f32 v30, s76, v30\n"
"v_mul_f32 v31, s76, v31\n"
"v_mul_f32 v40, s76, v40\n"
"v_mul_f32 v41, s76, v41\n"
"v_mul_f32 v42, s76, v42\n"
"v_mul_f32 v43, s76, v43\n"
"v_cvt_pk_bf16_f32 v116, v28, v29\n"
"v_cvt_pk_bf16_f32 v117, v30, v31\n"
"v_cvt_pk_bf16_f32 v118, v40, v41\n"
"v_cvt_pk_bf16_f32 v119, v42, v43\n"
"v_mul_f32 v32, s76, v32\n"
"v_mul_f32 v33, s76, v33\n"
"v_mul_f32 v34, s76, v34\n"
"v_mul_f32 v35, s76, v35\n"
"v_mul_f32 v44, s76, v44\n"
"v_mul_f32 v45, s76, v45\n"
"v_mul_f32 v46, s76, v46\n"
"v_mul_f32 v47, s76, v47\n"
"v_cvt_pk_bf16_f32 v132, v32, v33\n"
"v_cvt_pk_bf16_f32 v133, v34, v35\n"
"v_cvt_pk_bf16_f32 v134, v44, v45\n"
"v_cvt_pk_bf16_f32 v135, v46, v47\n"
"s_waitcnt vmcnt(14)\n"
"s_barrier\n"
"; step 4\n"
"s_add_u32 m0, s61, 73728\n"
"s_add_u32 s71, s77, 73728\n"
"buffer_load_dwordx4 v254, s[24:27], s71 offen nt lds\n"
"s_add_u32 m0, s61, 74624\n"
"s_nop 0\n"
"buffer_load_dwordx4 v254, s[24:27], s71 offen offset:128 nt lds\n"
"s_add_u32 m0, s61, 16384\n"
"s_add_u32 s71, s77, 81920\n"
"buffer_load_dwordx4 v254, s[20:23], s71 offen nt lds\n"
"s_add_u32 m0, s61, 17280\n"
"s_nop 0\n"
"buffer_load_dwordx4 v254, s[20:23], s71 offen offset:128 nt lds\n"
"s_and_b32 s71, s58, 7\n"
"s_lshl_b32 s71, s71, 17\n"
"s_mul_i32 s70, s56, 24576\n"
"s_add_u32 s71, s71, s70\n"
"s_add_u32 s71, s71, s66\n"
"v_lshrrev_b32 v196, 5, v253\n"
"v_lshlrev_b32 v196, 10, v196\n"
"v_and_b32 v197, 16, v253\n"
"v_lshl_add_u32 v196, v197, 3, v196\n"
"buffer_load_dword v199, v196, s[40:43], s71 offen\n"
"v_mul_f32 v48, s76, v48\n"
"v_mul_f32 v49, s76, v49\n"
"v_mul_f32 v50, s76, v50\n"
"v_mul_f32 v51, s76, v51\n"
"v_mul_f32 v60, s76, v60\n"
"v_mul_f32 v61, s76, v61\n"
"v_mul_f32 v62, s76, v62\n"
"v_mul_f32 v63, s76, v63\n"
"v_cvt_pk_bf16_f32 v104, v48, v49\n"
"v_cvt_pk_bf16_f32 v105, v50, v51\n"
"v_cvt_pk_bf16_f32 v106, v60, v61\n"
"v_cvt_pk_bf16_f32 v107, v62, v63\n"
"v_mul_f32 v52, s76, v52\n"
"v_mul_f32 v53, s76, v53\n"
"v_mul_f32 v54, s76, v54\n"
"v_mul_f32 v55, s76, v55\n"
"v_mul_f32 v64, s76, v64\n"
"v_mul_f32 v65, s76, v65\n"
"v_mul_f32 v66, s76, v66\n"
"v_mul_f32 v67, s76, v67\n"
"v_cvt_pk_bf16_f32 v120, v52, v53\n"
"v_cvt_pk_bf16_f32 v121, v54, v55\n"
"v_cvt_pk_bf16_f32 v122, v64, v65\n"
"v_cvt_pk_bf16_f32 v123, v66, v67\n"
"v_mul_f32 v56, s76, v56\n"
"v_mul_f32 v57, s76, v57\n"
"v_mul_f32 v58, s76, v58\n"
"v_mul_f32 v59, s76, v59\n"
"v_mul_f32 v68, s76, v68\n"
"v_mul_f32 v69, s76, v69\n"
"v_mul_f32 v70, s76, v70\n"
"v_mul_f32 v71, s76, v71\n"
"v_cvt_pk_bf16_f32 v136, v56, v57\n"
"v_cvt_pk_bf16_f32 v137, v58, v59\n"
"v_cvt_pk_bf16_f32 v138, v68, v69\n"
"v_cvt_pk_bf16_f32 v139, v70, v71\n"
"s_waitcnt vmcnt(14)\n"
"s_barrier\n"
"; step 5\n"
"s_add_u32 m0, s61, 81920\n"
"s_add_u32 s71, s77, 81920\n"
"buffer_load_dwordx4 v254, s[24:27], s71 offen nt lds\n"
"s_add_u32 m0, s61, 82816\n"
"s_nop 0\n"
"buffer_load_dwordx4 v254, s[24:27], s71 offen offset:128 nt lds\n"
"s_add_u32 m0, s61, 24576\n"
"s_add_u32 s71, s77, 90112\n"
"buffer_load_dwordx4 v254, s[20:23], s71 offen nt lds\n"
"s_add_u32 m0, s61, 25472\n"
"s_nop 0\n"
"buffer_load_dwordx4 v254, s[20:23], s71 offen offset:128 nt lds\n"
"v_mul_f32 v72, s76, v72\n"
"v_mul_f32 v73, s76, v73\n"
"v_mul_f32 v74, s76, v74\n"
"v_mul_f32 v75, s76, v75\n"
"v_mul_f32 v84, s76, v84\n"
"v_mul_f32 v85, s76, v85\n"
"v_mul_f32 v86, s76, v86\n"
"v_mul_f32 v87, s76, v87\n"
"v_cvt_pk_bf16_f32 v108, v72, v73\n"
"v_cvt_pk_bf16_f32 v109, v74, v75\n"
"v_cvt_pk_bf16_f32 v110, v84, v85\n"
"v_cvt_pk_bf16_f32 v111, v86, v87\n"
"v_mul_f32 v76, s76, v76\n"
"v_mul_f32 v77, s76, v77\n"
"v_mul_f32 v78, s76, v78\n"
"v_mul_f32 v79, s76, v79\n"
"v_mul_f32 v88, s76, v88\n"
"v_mul_f32 v89, s76, v89\n"
"v_mul_f32 v90, s76, v90\n"
"v_mul_f32 v91, s76, v91\n"
"v_cvt_pk_bf16_f32 v124, v76, v77\n"
"v_cvt_pk_bf16_f32 v125, v78, v79\n"
"v_cvt_pk_bf16_f32 v126, v88, v89\n"
"v_cvt_pk_bf16_f32 v127, v90, v91\n"
"v_mul_f32 v80, s76, v80\n"
"v_mul_f32 v81, s76, v81\n"
"v_mul_f32 v82, s76, v82\n"
"v_mul_f32 v83, s76, v83\n"
"v_mul_f32 v92, s76, v92\n"
"v_mul_f32 v93, s76, v93\n"
"v_mul_f32 v94, s76, v94\n"
"v_mul_f32 v95, s76, v95\n"
"v_cvt_pk_bf16_f32 v140, v80, v81\n"
"v_cvt_pk_bf16_f32 v141, v82, v83\n"
"v_cvt_pk_bf16_f32 v142, v92, v93\n"
"v_cvt_pk_bf16_f32 v143, v94, v95\n"
"s_waitcnt vmcnt(14)\n"
"s_barrier\n"
"; step 6\n"
"s_add_u32 m0, s61, 90112\n"
"s_add_u32 s71, s77, 90112\n"
"buffer_load_dwordx4 v254, s[24:27], s71 offen nt lds\n"
"s_add_u32 m0, s61, 91008\n"
"s_nop 0\n"
"buffer_load_dwordx4 v254, s[24:27], s71 offen offset:128 nt lds\n"
"s_add_u32 m0, s61, 32768\n"
"s_add_u32 s71, s77, 98304\n"
"buffer_load_dwordx4 v254, s[20:23], s71 offen nt lds\n"
"s_add_u32 m0, s61, 33664\n"
"s_nop 0\n"
"buffer_load_dwordx4 v254, s[20:23], s71 offen offset:128 nt lds\n"
"s_and_b32 s71, s58, 7\n"
"s_lshl_b32 s71, s71, 17\n"
"s_mul_i32 s70, s56, 24576\n"
"s_add_u32 s71, s71, s70\n"
"s_add_u32 s71, s71, s66\n"
"v_lshrrev_b32 v196, 5, v253\n"
"v_lshlrev_b32 v196, 10, v196\n"
"v_and_b32 v197, 16, v253\n"
"v_lshl_add_u32 v196, v197, 3, v196\n"
"buffer_load_dword v199, v196, s[40:43], s71 offen\n"
"s_waitcnt vmcnt(14)\n"
"s_barrier\n"
"; step 7\n"
"s_add_u32 m0, s61, 49152\n"
"s_add_u32 s71, s77, 98304\n"
"buffer_load_dwordx4 v254, s[24:27], s71 offen nt lds\n"
"s_add_u32 m0, s61, 50048\n"
"s_nop 0\n"
"buffer_load_dwordx4 v254, s[24:27], s71 offen offset:128 nt lds\n"
"s_add_u32 m0, s61, 40960\n"
"s_add_u32 s71, s77, 106496\n"
"buffer_load_dwordx4 v254, s[20:23], s71 offen nt lds\n"
"s_add_u32 m0, s61, 41856\n"
"s_nop 0\n"
"buffer_load_dwordx4 v254, s[20:23], s71 offen offset:128 nt lds\n"
"s_waitcnt vmcnt(14)\n"
"s_barrier\n"
"; step 8\n"
"s_add_u32 m0, s61, 57344\n"
"s_add_u32 s71, s77, 106496\n"
"buffer_load_dwordx4 v254, s[24:27], s71 offen nt lds\n"
"s_add_u32 m0, s61, 58240\n"
"s_nop 0\n"
"buffer_load_dwordx4 v254, s[24:27], s71 offen offset:128 nt lds\n"
"s_add_u32 m0, s61, 0\n"
"s_add_u32 s71, s77, 114688\n"
"buffer_load_dwordx4 v254, s[20:23], s71 offen nt lds\n"
"s_add_u32 m0, s61, 896\n"
"s_nop 0\n"
"buffer_load_dwordx4 v254, s[20:23], s71 offen offset:128 nt lds\n"
"s_add_u32 s71, s67, 0\n"
"s_add_u32 m0, s61, 98304\n"
"s_and_b32 s71, s71, 1048575\n"
"s_add_u32 s71, s71, s33\n"
"buffer_load_dwordx4 v255, s[40:43], s71 offen lds\n"
"s_add_u32 m0, s61, 99328\n"
"s_xor_b32 s71, s71, 128\n"
"buffer_load_dwordx4 v255, s[40:43], s71 offen lds\n"
"s_add_u32 s71, s67, 32768\n"
"s_add_u32 m0, s61, 106496\n"
"s_and_b32 s71, s71, 1048575\n"
"s_add_u32 s71, s71, s33\n"
"buffer_load_dwordx4 v255, s[40:43], s71 offen lds\n"
"s_add_u32 m0, s61, 107520\n"
"s_xor_b32 s71, s71, 128\n"
"buffer_load_dwordx4 v255, s[40:43], s71 offen lds\n"
"s_waitcnt vmcnt(17)\n"
"s_barrier\n"
"; step 9\n"
"s_add_u32 m0, s61, 65536\n"
"s_add_u32 s71, s77, 114688\n"
"buffer_load_dwordx4 v254, s[24:27], s71 offen nt lds\n"
"s_add_u32 m0, s61, 66432\n"
"s_nop 0\n"
"buffer_load_dwordx4 v254, s[24:27], s71 offen offset:128 nt lds\n"
"s_add_u32 m0, s61, 8192\n"
"s_add_u32 s71, s77, 122880\n"
"buffer_load_dwordx4 v254, s[20:23], s71 offen nt lds\n"
"s_add_u32 m0, s61, 9088\n"
"s_nop 0\n"
"buffer_load_dwordx4 v254, s[20:23], s71 offen offset:128 nt lds\n"
"s_add_u32 s71, s67, 65536\n"
"s_add_u32 m0, s61, 114688\n"
"s_and_b32 s71, s71, 1048575\n"
"s_add_u32 s71, s71, s33\n"
"buffer_load_dwordx4 v255, s[40:43], s71 offen lds\n"
"s_add_u32 m0, s61, 115712\n"
"s_xor_b32 s71, s71, 128\n"
"buffer_load_dwordx4 v255, s[40:43], s71 offen lds\n"
"s_add_u32 s71, s67, 98304\n"
"s_add_u32 m0, s61, 122880\n"
"s_and_b32 s71, s71, 1048575\n"
"s_add_u32 s71, s71, s33\n"
"buffer_load_dwordx4 v255, s[40:43], s71 offen lds\n"
"s_add_u32 m0, s61, 123904\n"
"s_xor_b32 s71, s71, 128\n"
"buffer_load_dwordx4 v255, s[40:43], s71 offen lds\n"
"s_waitcnt vmcnt(21)\n"
"s_barrier\n"
"; step 10\n"
"s_add_u32 m0, s61, 73728\n"
"s_add_u32 s71, s77, 122880\n"
"buffer_load_dwordx4 v254, s[24:27], s71 offen nt lds\n"
"s_add_u32 m0, s61, 74624\n"
"s_nop 0\n"
"buffer_load_dwordx4 v254, s[24:27], s71 offen offset:128 nt lds\n"
"s_add_u32 m0, s61, 16384\n"
"s_add_u32 s71, s77, 131072\n"
"buffer_load_dwordx4 v254, s[20:23], s71 offen nt lds\n"
"s_add_u32 m0, s61, 17280\n"
"s_nop 0\n"
"buffer_load_dwordx4 v254, s[20:23], s71 offen offset:128 nt lds\n"
"s_cmp_le_u32 s60, 0\n"
"s_cselect_b32 s71, 262144, 0\n"
"s_add_u32 s71, s71, 0\n"
"s_add_u32 m0, s61, 131072\n"
"s_add_u32 s71, s71, s33\n"
"buffer_load_dwordx4 v255, s[40:43], s71 offen lds\n"
"s_add_u32 m0, s61, 132096\n"
"s_xor_b32 s71, s71, 128\n"
"buffer_load_dwordx4 v255, s[40:43], s71 offen lds\n"
"s_cmp_le_u32 s60, 0\n"
"s_cselect_b32 s71, 262144, 0\n"
"s_add_u32 s71, s71, 32768\n"
"s_add_u32 m0, s61, 139264\n"
"s_add_u32 s71, s71, s33\n"
"buffer_load_dwordx4 v255, s[40:43], s71 offen lds\n"
"s_add_u32 m0, s61, 140288\n"
"s_xor_b32 s71, s71, 128\n"
"buffer_load_dwordx4 v255, s[40:43], s71 offen lds\n"
"s_waitcnt vmcnt(24)\n"
"s_barrier\n"
"; step 11\n"
"s_add_u32 m0, s61, 81920\n"
"s_add_u32 s71, s77, 131072\n"
"buffer_load_dwordx4 v254, s[24:27], s71 offen nt lds\n"
"s_add_u32 m0, s61, 82816\n"
"s_nop 0\n"
"buffer_load_dwordx4 v254, s[24:27], s71 offen offset:128 nt lds\n"
"s_add_u32 m0, s61, 24576\n"
"s_add_u32 s71, s77, 139264\n"
"buffer_load_dwordx4 v254, s[20:23], s71 offen nt lds\n"
"s_add_u32 m0, s61, 25472\n"
"s_nop 0\n"
"buffer_load_dwordx4 v254, s[20:23], s71 offen offset:128 nt lds\n"
"s_cmp_le_u32 s60, 0\n"
"s_cselect_b32 s71, 262144, 0\n"
"s_add_u32 s71, s71, 65536\n"
"s_add_u32 m0, s61, 147456\n"
"s_add_u32 s71, s71, s33\n"
"buffer_load_dwordx4 v255, s[40:43], s71 offen lds\n"
"s_add_u32 m0, s61, 148480\n"
"s_xor_b32 s71, s71, 128\n"
"buffer_load_dwordx4 v255, s[40:43], s71 offen lds\n"
"s_cmp_le_u32 s60, 0\n"
"s_cselect_b32 s71, 262144, 0\n"
"s_add_u32 s71, s71, 98304\n"
"s_add_u32 m0, s61, 155648\n"
"s_add_u32 s71, s71, s33\n"
"buffer_load_dwordx4 v255, s[40:43], s71 offen lds\n"
"s_add_u32 m0, s61, 156672\n"
"s_xor_b32 s71, s71, 128\n"
"buffer_load_dwordx4 v255, s[40:43], s71 offen lds\n"
"s_waitcnt vmcnt(28)\n"
"s_barrier\n"
"; step 12\n"
"s_add_u32 m0, s61, 90112\n"
"s_add_u32 s71, s77, 139264\n"
"buffer_load_dwordx4 v254, s[24:27], s71 offen nt lds\n"
"s_add_u32 m0, s61, 91008\n"
"s_nop 0\n"
"buffer_load_dwordx4 v254, s[24:27], s71 offen offset:128 nt lds\n"
"s_add_u32 m0, s61, 32768\n"
"s_add_u32 s71, s77, 147456\n"
"buffer_load_dwordx4 v254, s[20:23], s71 offen nt lds\n"
"s_add_u32 m0, s61, 33664\n"
"s_nop 0\n"
"buffer_load_dwordx4 v254, s[20:23], s71 offen offset:128 nt lds\n"
"v_readfirstlane_b32 s80, v244\n"
"v_readfirstlane_b32 s81, v245\n"
"s_add_u32 s80, s80, 1\n"
"s_addc_u32 s81, s81, 0\n"
"s_waitcnt vmcnt(24)\n"
"s_barrier\n"
"; step 13\n"
"s_add_u32 m0, s61, 49152\n"
"s_add_u32 s71, s77, 147456\n"
"buffer_load_dwordx4 v254, s[24:27], s71 offen nt lds\n"
"s_add_u32 m0, s61, 50048\n"
"s_nop 0\n"
"buffer_load_dwordx4 v254, s[24:27], s71 offen offset:128 nt lds\n"
"s_add_u32 m0, s61, 40960\n"
"s_add_u32 s71, s77, 155648\n"
"buffer_load_dwordx4 v254, s[20:23], s71 offen nt lds\n"
"s_add_u32 m0, s61, 41856\n"
"s_nop 0\n"
"buffer_load_dwordx4 v254, s[20:23], s71 offen offset:128 nt lds\n"
"s_waitcnt vmcnt(20)\n"
"s_barrier\n"
"; step 14\n"
"s_add_u32 m0, s61, 57344\n"
"s_add_u32 s71, s77, 155648\n"
"buffer_load_dwordx4 v254, s[24:27], s71 offen nt lds\n"
"s_add_u32 m0, s61, 58240\n"
"s_nop 0\n"
"buffer_load_dwordx4 v254, s[24:27], s71 offen offset:128 nt lds\n"
"s_add_u32 m0, s61, 0\n"
"s_add_u32 s71, s77, 163840\n"
"buffer_load_dwordx4 v254, s[20:23], s71 offen nt lds\n"
"s_add_u32 m0, s61, 896\n"
"s_nop 0\n"
"buffer_load_dwordx4 v254, s[20:23], s71 offen offset:128 nt lds\n"
"v_add_u32 v248, 32768, v248\n"
"v_add_u32 v249, 81920, v249\n"
"v_add_u32 v250, 81920, v250\n"
"s_mov_b32 s68, 1\n"
"s_mov_b32 s75, 8192\n"
"s_mov_b32 s69, 16\n"
"v_add_u32 v242, -16384, v248\n"
"ds_read_b128 v[204:207], v242 offset:0\n"
"ds_read_b128 v[208:211], v242 offset:4096\n"
"ds_read_b128 v[212:215], v242 offset:512\n"
"ds_read_b128 v[216:219], v242 offset:4608\n"
"ds_read_b128 v[220:223], v242 offset:1024\n"
"ds_read_b128 v[224:227], v242 offset:5120\n"
"ds_read_b128 v[228:231], v242 offset:1536\n"
"ds_read_b128 v[232:235], v242 offset:5632\n"
"s_waitcnt vmcnt(16)\n"
"s_barrier\n"
"; step 15\n"
"v_add_u32 v242, -8192, v248\n"
"s_waitcnt lgkmcnt(6)\n"
"v_mfma_f32_16x16x32_bf16 v[144:147], v[204:207], v[96:99], 0\n"
"v_mfma_f32_16x16x32_bf16 v[148:151], v[204:207], v[112:115], 0\n"
"v_mfma_f32_16x16x32_bf16 v[152:155], v[204:207], v[128:131], 0\n"
"ds_read_b128 v[204:207], v242 offset:0\n"
"v_mfma_f32_16x16x32_bf16 v[156:159], v[208:211], v[96:99], 0\n"
"v_mfma_f32_16x16x32_bf16 v[160:163], v[208:211], v[112:115], 0\n"
"s_add_u32 m0, s61, 65536\n"
"s_add_u32 s71, s77, 163840\n"
"buffer_load_dwordx4 v254, s[24:27], s71 offen nt lds\n"
"s_add_u32 m0, s61, 66432\n"
"s_nop 0\n"
"buffer_load_dwordx4 v254, s[24:27], s71 offen offset:128 nt lds\n"
"v_mfma_f32_16x16x32_bf16 v[164:167], v[208:211], v[128:131], 0\n"
"ds_read_b128 v[208:211], v242 offset:4096\n"
"s_waitcnt lgkmcnt(6)\n"
"v_mfma_f32_16x16x32_bf16 v[144:147], v[212:215], v[100:103], v[144:147]\n"
"v_mfma_f32_16x16x32_bf16 v[148:151], v[212:215], v[116:119], v[148:151]\n"
"v_mfma_f32_16x16x32_bf16 v[152:155], v[212:215], v[132:135], v[152:155]\n"
"ds_read_b128 v[212:215], v242 offset:512\n"
"v_mfma_f32_16x16x32_bf16 v[156:159], v[216:219], v[100:103], v[156:159]\n"
"v_mfma_f32_16x16x32_bf16 v[160:163], v[216:219], v[116:119], v[160:163]\n"
"v_mfma_f32_16x16x32_bf16 v[164:167], v[216:219], v[132:135], v[164:167]\n"
"ds_read_b128 v[216:219], v242 offset:4608\n"
"s_waitcnt lgkmcnt(6)\n"
"v_mfma_f32_16x16x32_bf16 v[144:147], v[220:223], v[104:107], v[144:147]\n"
"v_mfma_f32_16x16x32_bf16 v[148:151], v[220:223], v[120:123], v[148:151]\n"
"v_mfma_f32_16x16x32_bf16 v[152:155], v[220:223], v[136:139], v[152:155]\n"
"ds_read_b128 v[220:223], v242 offset:1024\n"
"v_mfma_f32_16x16x32_bf16 v[156:159], v[224:227], v[104:107], v[156:159]\n"
"v_mfma_f32_16x16x32_bf16 v[160:163], v[224:227], v[120:123], v[160:163]\n"
"s_add_u32 m0, s61, 8192\n"
"s_add_u32 s71, s77, 172032\n"
"buffer_load_dwordx4 v254, s[20:23], s71 offen nt lds\n"
"s_add_u32 m0, s61, 9088\n"
"s_nop 0\n"
"buffer_load_dwordx4 v254, s[20:23], s71 offen offset:128 nt lds\n"
"v_mfma_f32_16x16x32_bf16 v[164:167], v[224:227], v[136:139], v[164:167]\n"
"ds_read_b128 v[224:227], v242 offset:5120\n"
"s_waitcnt lgkmcnt(6)\n"
"v_mfma_f32_16x16x32_bf16 v[144:147], v[228:231], v[108:111], v[144:147]\n"
"v_mfma_f32_16x16x32_bf16 v[148:151], v[228:231], v[124:127], v[148:151]\n"
"v_mfma_f32_16x16x32_bf16 v[152:155], v[228:231], v[140:143], v[152:155]\n"
"ds_read_b128 v[228:231], v242 offset:1536\n"
"v_mfma_f32_16x16x32_bf16 v[156:159], v[232:235], v[108:111], v[156:159]\n"
"v_mfma_f32_16x16x32_bf16 v[160:163], v[232:235], v[124:127], v[160:163]\n"
"v_mfma_f32_16x16x32_bf16 v[164:167], v[232:235], v[140:143], v[164:167]\n"
"ds_read_b128 v[232:235], v242 offset:5632\n"
"v_mov_b32 v236, 0\n"
"v_mov_b32 v237, 0\n"
"v_mov_b32 v238, 0\n"
"v_max3_f32 v239, v144, v145, v146\n"
"s_nop 0\n"
"v_max3_f32 v242, v147, v156, v157\n"
"v_max3_f32 v239, v239, v158, v159\n"
"v_max3_f32 v240, v148, v149, v150\n"
"v_max3_f32 v243, v151, v160, v161\n"
"v_max3_f32 v240, v240, v162, v163\n"
"v_max3_f32 v241, v152, v153, v154\n"
"v_max3_f32 v244, v155, v164, v165\n"
"v_max3_f32 v241, v241, v166, v167\n"
"v_max_f32 v239, v239, v242\n"
"v_max_f32 v240, v240, v243\n"
"v_max_f32 v241, v241, v244\n"
"v_mov_b32 v242, v239\n"
"v_mov_b32 v243, v240\n"
"v_mov_b32 v244, v241\n"
"v_permlane32_swap_b32 v239, v242\n"
"v_permlane32_swap_b32 v240, v243\n"
"v_permlane32_swap_b32 v241, v244\n"
"v_max_f32 v239, v239, v242\n"
"v_max_f32 v240, v240, v243\n"
"v_max_f32 v241, v241, v244\n"
"v_mov_b32 v242, v239\n"
"v_mov_b32 v243, v240\n"
"v_mov_b32 v244, v241\n"
"v_permlane16_swap_b32 v239, v242\n"
"v_permlane16_swap_b32 v240, v243\n"
"v_permlane16_swap_b32 v241, v244\n"
"v_max_f32 v239, v239, v242\n"
"v_max_f32 v240, v240, v243\n"
"v_max_f32 v241, v241, v244\n"
"v_sub_f32 v144, v144, v239\n"
"v_sub_f32 v145, v145, v239\n"
"v_sub_f32 v146, v146, v239\n"
"v_sub_f32 v147, v147, v239\n"
"v_sub_f32 v156, v156, v239\n"
"v_sub_f32 v157, v157, v239\n"
"v_sub_f32 v158, v158, v239\n"
"v_sub_f32 v159, v159, v239\n"
"v_sub_f32 v192, 0, v239\n"
"v_sub_f32 v193, 0, v239\n"
"v_sub_f32 v194, 0, v239\n"
"v_sub_f32 v195, 0, v239\n"
"v_sub_f32 v148, v148, v240\n"
"v_sub_f32 v149, v149, v240\n"
"v_sub_f32 v150, v150, v240\n"
"v_sub_f32 v151, v151, v240\n"
"v_sub_f32 v160, v160, v240\n"
"v_sub_f32 v161, v161, v240\n"
"v_sub_f32 v162, v162, v240\n"
"v_sub_f32 v163, v163, v240\n"
"v_sub_f32 v196, 0, v240\n"
"v_sub_f32 v197, 0, v240\n"
"v_sub_f32 v198, 0, v240\n"
"v_sub_f32 v199, 0, v240\n"
"v_sub_f32 v152, v152, v241\n"
"v_sub_f32 v153, v153, v241\n"
"v_sub_f32 v154, v154, v241\n"
"v_sub_f32 v155, v155, v241\n"
"v_sub_f32 v164, v164, v241\n"
"v_sub_f32 v165, v165, v241\n"
"v_sub_f32 v166, v166, v241\n"
"v_sub_f32 v167, v167, v241\n"
"v_sub_f32 v200, 0, v241\n"
"v_sub_f32 v201, 0, v241\n"
"v_sub_f32 v202, 0, v241\n"
"v_sub_f32 v203, 0, v241\n"
"v_mov_b32 v244, 1065369472\n"
"v_mov_b32 v245, 1065369472\n"
"v_mov_b32 v246, 1065369472\n"
"v_mov_b32 v247, 1065369472\n"
"v_exp_f32 v144, v144\n"
"v_exp_f32 v145, v145\n"
"v_exp_f32 v146, v146\n"
"v_exp_f32 v147, v147\n"
"v_exp_f32 v156, v156\n"
"v_exp_f32 v157, v157\n"
"v_exp_f32 v158, v158\n"
"v_exp_f32 v159, v159\n"
"v_exp_f32 v148, v148\n"
"v_exp_f32 v149, v149\n"
"v_exp_f32 v150, v150\n"
"v_exp_f32 v151, v151\n"
"v_exp_f32 v160, v160\n"
"v_exp_f32 v161, v161\n"
"v_exp_f32 v162, v162\n"
"v_exp_f32 v163, v163\n"
"s_setprio 1\n"
"s_waitcnt vmcnt(12)\n"
"s_barrier\n"
"; step 16\n"
"s_sleep 3\n"
"s_waitcnt lgkmcnt(6)\n"
"v_mfma_f32_16x16x32_bf16 v[168:171], v[204:207], v[96:99], v[192:195]\n"
"v_cvt_pk_bf16_f32 v144, v144, v145\n"
"v_cvt_pk_bf16_f32 v145, v146, v147\n"
"v_mfma_f32_16x16x32_bf16 v[172:175], v[204:207], v[112:115], v[196:199]\n"
"v_cvt_pk_bf16_f32 v146, v156, v157\n"
"v_cvt_pk_bf16_f32 v147, v158, v159\n"
"v_mfma_f32_16x16x32_bf16 v[176:179], v[204:207], v[128:131], v[200:203]\n"
"ds_read_b64_tr_b16 v[204:205], v249 offset:0\n"
"ds_read_b64_tr_b16 v[206:207], v249 offset:4096\n"
"v_mfma_f32_16x16x32_bf16 v[180:183], v[208:211], v[96:99], v[192:195]\n"
"v_cvt_pk_bf16_f32 v148, v148, v149\n"
"v_cvt_pk_bf16_f32 v149, v150, v151\n"
"v_mfma_f32_16x16x32_bf16 v[184:187], v[208:211], v[112:115], v[196:199]\n"
"v_cvt_pk_bf16_f32 v150, v160, v161\n"
"v_cvt_pk_bf16_f32 v151, v162, v163\n"
"v_mfma_f32_16x16x32_bf16 v[188:191], v[208:211], v[128:131], v[200:203]\n"
"ds_read_b64_tr_b16 v[208:209], v250 offset:0\n"
"ds_read_b64_tr_b16 v[210:211], v250 offset:4096\n"
"s_waitcnt lgkmcnt(8)\n"
"v_mfma_f32_16x16x32_bf16 v[168:171], v[212:215], v[100:103], v[168:171]\n"
"v_exp_f32 v152, v152\n"
"v_mfma_f32_16x16x32_bf16 v[172:175], v[212:215], v[116:119], v[172:175]\n"
"v_exp_f32 v153, v153\n"
"v_mfma_f32_16x16x32_bf16 v[176:179], v[212:215], v[132:135], v[176:179]\n"
"ds_read_b64_tr_b16 v[212:213], v249 offset:512\n"
"ds_read_b64_tr_b16 v[214:215], v249 offset:4608\n"
"v_mfma_f32_16x16x32_bf16 v[180:183], v[216:219], v[100:103], v[180:183]\n"
"v_exp_f32 v154, v154\n"
"v_mfma_f32_16x16x32_bf16 v[184:187], v[216:219], v[116:119], v[184:187]\n"
"v_exp_f32 v155, v155\n"
"v_mfma_f32_16x16x32_bf16 v[188:191], v[216:219], v[132:135], v[188:191]\n"
"ds_read_b64_tr_b16 v[216:217], v250 offset:512\n"
"ds_read_b64_tr_b16 v[218:219], v250 offset:4608\n"
"s_waitcnt lgkmcnt(10)\n"
"v_mfma_f32_16x16x32_bf16 v[168:171], v[220:223], v[104:107], v[168:171]\n"
"v_exp_f32 v164, v164\n"
"v_mfma_f32_16x16x32_bf16 v[172:175], v[220:223], v[120:123], v[172:175]\n"
"v_exp_f32 v165, v165\n"
"v_mfma_f32_16x16x32_bf16 v[176:179], v[220:223], v[136:139], v[176:179]\n"
"ds_read_b64_tr_b16 v[220:221], v249 offset:1024\n"
"ds_read_b64_tr_b16 v[222:223], v249 offset:5120\n"
"v_mfma_f32_16x16x32_bf16 v[180:183], v[224:227], v[104:107], v[180:183]\n"
"v_exp_f32 v166, v166\n"
"v_mfma_f32_16x16x32_bf16 v[184:187], v[224:227], v[120:123], v[184:187]\n"
"v_exp_f32 v167, v167\n"
"v_mfma_f32_16x16x32_bf16 v[188:191], v[224:227], v[136:139], v[188:191]\n"
"ds_read_b64_tr_b16 v[224:225], v250 offset:1024\n"
"ds_read_b64_tr_b16 v[226:227], v250 offset:5120\n"
"v_cvt_pk_bf16_f32 v152, v152, v153\n"
"v_cvt_pk_bf16_f32 v153, v154, v155\n"
"v_cvt_pk_bf16_f32 v154, v164, v165\n"
"v_cvt_pk_bf16_f32 v155, v166, v167\n"
"v_mfma_f32_16x16x32_bf16 v[156:159], v[244:247], v[144:147], 0\n"
"v_mfma_f32_16x16x32_bf16 v[160:163], v[244:247], v[148:151], 0\n"
"v_mfma_f32_16x16x32_bf16 v[164:167], v[244:247], v[152:155], 0\n"
"s_waitcnt lgkmcnt(12)\n"
"v_mfma_f32_16x16x32_bf16 v[168:171], v[228:231], v[108:111], v[168:171]\n"
"v_mfma_f32_16x16x32_bf16 v[172:175], v[228:231], v[124:127], v[172:175]\n"
"v_mfma_f32_16x16x32_bf16 v[176:179], v[228:231], v[140:143], v[176:179]\n"
"ds_read_b64_tr_b16 v[228:229], v249 offset:1536\n"
"ds_read_b64_tr_b16 v[230:231], v249 offset:5632\n"
"v_mfma_f32_16x16x32_bf16 v[180:183], v[232:235], v[108:111], v[180:183]\n"
"v_mfma_f32_16x16x32_bf16 v[184:187], v[232:235], v[124:127], v[184:187]\n"
"v_mfma_f32_16x16x32_bf16 v[188:191], v[232:235], v[140:143], v[188:191]\n"
"ds_read_b64_tr_b16 v[232:233], v250 offset:1536\n"
"ds_read_b64_tr_b16 v[234:235], v250 offset:5632\n"
"s_waitcnt lgkmcnt(12)\n"
"v_mfma_f32_16x16x32_bf16 v[0:3], v[204:207], v[144:147], 0\n"
"v_exp_f32 v168, v168\n"
"v_mfma_f32_16x16x32_bf16 v[4:7], v[204:207], v[148:151], 0\n"
"v_exp_f32 v169, v169\n"
"v_mfma_f32_16x16x32_bf16 v[8:11], v[204:207], v[152:155], 0\n"
"ds_read_b128 v[204:207], v248 offset:0\n"
"v_add_f32 v236, v236, v156\n"
"v_mfma_f32_16x16x32_bf16 v[12:15], v[208:211], v[144:147], 0\n"
"v_exp_f32 v170, v170\n"
"v_mfma_f32_16x16x32_bf16 v[16:19], v[208:211], v[148:151], 0\n"
"v_exp_f32 v171, v171\n"
"v_mfma_f32_16x16x32_bf16 v[20:23], v[208:211], v[152:155], 0\n"
"ds_read_b128 v[208:211], v248 offset:4096\n"
"v_add_f32 v237, v237, v160\n"
"s_waitcnt lgkmcnt(10)\n"
"v_mfma_f32_16x16x32_bf16 v[24:27], v[212:215], v[144:147], 0\n"
"v_exp_f32 v180, v180\n"
"v_mfma_f32_16x16x32_bf16 v[28:31], v[212:215], v[148:151], 0\n"
"v_exp_f32 v181, v181\n"
"v_mfma_f32_16x16x32_bf16 v[32:35], v[212:215], v[152:155], 0\n"
"ds_read_b128 v[212:215], v248 offset:512\n"
"v_add_f32 v238, v238, v164\n"
"v_mfma_f32_16x16x32_bf16 v[36:39], v[216:219], v[144:147], 0\n"
"v_exp_f32 v182, v182\n"
"v_mfma_f32_16x16x32_bf16 v[40:43], v[216:219], v[148:151], 0\n"
"v_exp_f32 v183, v183\n"
"v_mfma_f32_16x16x32_bf16 v[44:47], v[216:219], v[152:155], 0\n"
"ds_read_b128 v[216:219], v248 offset:4608\n"
"v_cvt_pk_bf16_f32 v168, v168, v169\n"
"s_waitcnt lgkmcnt(8)\n"
"v_mfma_f32_16x16x32_bf16 v[48:51], v[220:223], v[144:147], 0\n"
"v_exp_f32 v172, v172\n"
"v_mfma_f32_16x16x32_bf16 v[52:55], v[220:223], v[148:151], 0\n"
"v_exp_f32 v173, v173\n"
"v_mfma_f32_16x16x32_bf16 v[56:59], v[220:223], v[152:155], 0\n"
"ds_read_b128 v[220:223], v248 offset:1024\n"
"v_cvt_pk_bf16_f32 v169, v170, v171\n"
"v_mfma_f32_16x16x32_bf16 v[60:63], v[224:227], v[144:147], 0\n"
"v_exp_f32 v174, v174\n"
"v_mfma_f32_16x16x32_bf16 v[64:67], v[224:227], v[148:151], 0\n"
"v_exp_f32 v175, v175\n"
"v_mfma_f32_16x16x32_bf16 v[68:71], v[224:227], v[152:155], 0\n"
"ds_read_b128 v[224:227], v248 offset:5120\n"
"v_cvt_pk_bf16_f32 v170, v180, v181\n"
"s_waitcnt lgkmcnt(6)\n"
"v_mfma_f32_16x16x32_bf16 v[72:75], v[228:231], v[144:147], 0\n"
"s_add_u32 s69, s69, 1\n"
"s_sub_u32 s68, s68, 1\n"
"s_cselect_b32 s70, -40960, s75\n"
"s_cselect_b32 s68, 5, s68\n"
"v_exp_f32 v184, v184\n"
"v_mfma_f32_16x16x32_bf16 v[76:79], v[228:231], v[148:151], 0\n"
"v_exp_f32 v185, v185\n"
"v_mfma_f32_16x16x32_bf16 v[80:83], v[228:231], v[152:155], 0\n"
"ds_read_b128 v[228:231], v248 offset:1536\n"
"v_cvt_pk_bf16_f32 v171, v182, v183\n"
"v_mfma_f32_16x16x32_bf16 v[84:87], v[232:235], v[144:147], 0\n"
"v_exp_f32 v186, v186\n"
"v_mfma_f32_16x16x32_bf16 v[88:91], v[232:235], v[148:151], 0\n"
"v_exp_f32 v187, v187\n"
"v_mfma_f32_16x16x32_bf16 v[92:95], v[232:235], v[152:155], 0\n"
"ds_read_b128 v[232:235], v248 offset:5632\n"
"v_add_u32 v249, s70, v249\n"
"v_add_u32 v250, s70, v250\n"
"v_add_u32 v248, s70, v248\n"
"s_waitcnt vmcnt(8)\n"
"s_barrier\n"
"; step 17\n"
"s_sleep 3\n"
"s_waitcnt lgkmcnt(6)\n"
"v_mfma_f32_16x16x32_bf16 v[144:147], v[204:207], v[96:99], v[192:195]\n"
"v_cvt_pk_bf16_f32 v172, v172, v173\n"
"v_cvt_pk_bf16_f32 v173, v174, v175\n"
"v_mfma_f32_16x16x32_bf16 v[148:151], v[204:207], v[112:115], v[196:199]\n"
"v_cvt_pk_bf16_f32 v174, v184, v185\n"
"v_cvt_pk_bf16_f32 v175, v186, v187\n"
"v_mfma_f32_16x16x32_bf16 v[152:155], v[204:207], v[128:131], v[200:203]\n"
"ds_read_b64_tr_b16 v[204:205], v249 offset:0\n"
"ds_read_b64_tr_b16 v[206:207], v249 offset:4096\n"
"v_mfma_f32_16x16x32_bf16 v[156:159], v[208:211], v[96:99], v[192:195]\n"
"v_exp_f32 v176, v176\n"
"v_mfma_f32_16x16x32_bf16 v[160:163], v[208:211], v[112:115], v[196:199]\n"
"v_exp_f32 v177, v177\n"
"v_mfma_f32_16x16x32_bf16 v[164:167], v[208:211], v[128:131], v[200:203]\n"
"ds_read_b64_tr_b16 v[208:209], v250 offset:0\n"
"ds_read_b64_tr_b16 v[210:211], v250 offset:4096\n"
"s_waitcnt lgkmcnt(8)\n"
"v_mfma_f32_16x16x32_bf16 v[144:147], v[212:215], v[100:103], v[144:147]\n"
"v_exp_f32 v178, v178\n"
"v_mfma_f32_16x16x32_bf16 v[148:151], v[212:215], v[116:119], v[148:151]\n"
"v_exp_f32 v179, v179\n"
"v_mfma_f32_16x16x32_bf16 v[152:155], v[212:215], v[132:135], v[152:155]\n"
"ds_read_b64_tr_b16 v[212:213], v249 offset:512\n"
"ds_read_b64_tr_b16 v[214:215], v249 offset:4608\n"
"v_mfma_f32_16x16x32_bf16 v[156:159], v[216:219], v[100:103], v[156:159]\n"
"v_exp_f32 v188, v188\n"
"v_mfma_f32_16x16x32_bf16 v[160:163], v[216:219], v[116:119], v[160:163]\n"
"v_exp_f32 v189, v189\n"
"v_mfma_f32_16x16x32_bf16 v[164:167], v[216:219], v[132:135], v[164:167]\n"
"ds_read_b64_tr_b16 v[216:217], v250 offset:512\n"
"ds_read_b64_tr_b16 v[218:219], v250 offset:4608\n"
"s_waitcnt lgkmcnt(10)\n"
"v_mfma_f32_16x16x32_bf16 v[144:147], v[220:223], v[104:107], v[144:147]\n"
"v_exp_f32 v190, v190\n"
"v_mfma_f32_16x16x32_bf16 v[148:151], v[220:223], v[120:123], v[148:151]\n"
"v_exp_f32 v191, v191\n"
"v_mfma_f32_16x16x32_bf16 v[152:155], v[220:223], v[136:139], v[152:155]\n"
"ds_read_b64_tr_b16 v[220:221], v249 offset:1024\n"
"ds_read_b64_tr_b16 v[222:223], v249 offset:5120\n"
"v_mfma_f32_16x16x32_bf16 v[156:159], v[224:227], v[104:107], v[156:159]\n"
"v_cvt_pk_bf16_f32 v176, v176, v177\n"
"v_cvt_pk_bf16_f32 v177, v178, v179\n"
"v_mfma_f32_16x16x32_bf16 v[160:163], v[224:227], v[120:123], v[160:163]\n"
"v_cvt_pk_bf16_f32 v178, v188, v189\n"
"v_cvt_pk_bf16_f32 v179, v190, v191\n"
"v_mfma_f32_16x16x32_bf16 v[164:167], v[224:227], v[136:139], v[164:167]\n"
"ds_read_b64_tr_b16 v[224:225], v250 offset:1024\n"
"ds_read_b64_tr_b16 v[226:227], v250 offset:5120\n"
"v_mfma_f32_16x16x32_bf16 v[180:183], v[244:247], v[168:171], 0\n"
"v_mfma_f32_16x16x32_bf16 v[184:187], v[244:247], v[172:175], 0\n"
"v_mfma_f32_16x16x32_bf16 v[188:191], v[244:247], v[176:179], 0\n"
"s_waitcnt lgkmcnt(12)\n"
"v_mfma_f32_16x16x32_bf16 v[144:147], v[228:231], v[108:111], v[144:147]\n"
"v_mfma_f32_16x16x32_bf16 v[148:151], v[228:231], v[124:127], v[148:151]\n"
"v_mfma_f32_16x16x32_bf16 v[152:155], v[228:231], v[140:143], v[152:155]\n"
"ds_read_b64_tr_b16 v[228:229], v249 offset:1536\n"
"ds_read_b64_tr_b16 v[230:231], v249 offset:5632\n"
"v_mfma_f32_16x16x32_bf16 v[156:159], v[232:235], v[108:111], v[156:159]\n"
"s_nop 0\n"
"v_max3_f32 v242, v180, v184, v188\n"
"v_mfma_f32_16x16x32_bf16 v[160:163], v[232:235], v[124:127], v[160:163]\n"
"v_cmp_nge_f32 vcc, 0x46000000, v242\n"
"v_mfma_f32_16x16x32_bf16 v[164:167], v[232:235], v[140:143], v[164:167]\n"
"ds_read_b64_tr_b16 v[232:233], v250 offset:1536\n"
"ds_read_b64_tr_b16 v[234:235], v250 offset:5632\n"
"s_cbranch_vccnz .LrAy36\n"
".LrAby36:\n"
"s_waitcnt lgkmcnt(12)\n"
"v_mfma_f32_16x16x32_bf16 v[0:3], v[204:207], v[168:171], v[0:3]\n"
"v_exp_f32 v144, v144\n"
"v_mfma_f32_16x16x32_bf16 v[4:7], v[204:207], v[172:175], v[4:7]\n"
"v_exp_f32 v145, v145\n"
"v_mfma_f32_16x16x32_bf16 v[8:11], v[204:207], v[176:179], v[8:11]\n"
"ds_read_b128 v[204:207], v248 offset:0\n"
"v_add_f32 v236, v236, v180\n"
"v_mfma_f32_16x16x32_bf16 v[12:15], v[208:211], v[168:171], v[12:15]\n"
"v_exp_f32 v146, v146\n"
"v_mfma_f32_16x16x32_bf16 v[16:19], v[208:211], v[172:175], v[16:19]\n"
"v_exp_f32 v147, v147\n"
"v_mfma_f32_16x16x32_bf16 v[20:23], v[208:211], v[176:179], v[20:23]\n"
"ds_read_b128 v[208:211], v248 offset:4096\n"
"v_add_f32 v237, v237, v184\n"
"s_waitcnt lgkmcnt(10)\n"
"v_mfma_f32_16x16x32_bf16 v[24:27], v[212:215], v[168:171], v[24:27]\n"
"v_exp_f32 v156, v156\n"
"v_mfma_f32_16x16x32_bf16 v[28:31], v[212:215], v[172:175], v[28:31]\n"
"v_exp_f32 v157, v157\n"
"v_mfma_f32_16x16x32_bf16 v[32:35], v[212:215], v[176:179], v[32:35]\n"
"ds_read_b128 v[212:215], v248 offset:512\n"
"v_add_f32 v238, v238, v188\n"
"v_mfma_f32_16x16x32_bf16 v[36:39], v[216:219], v[168:171], v[36:39]\n"
"v_exp_f32 v158, v158\n"
"v_mfma_f32_16x16x32_bf16 v[40:43], v[216:219], v[172:175], v[40:43]\n"
"v_exp_f32 v159, v159\n"
"v_mfma_f32_16x16x32_bf16 v[44:47], v[216:219], v[176:179], v[44:47]\n"
"ds_read_b128 v[216:219], v248 offset:4608\n"
"v_cvt_pk_bf16_f32 v144, v144, v145\n"
"s_waitcnt lgkmcnt(8)\n"
"v_mfma_f32_16x16x32_bf16 v[48:51], v[220:223], v[168:171], v[48:51]\n"
"v_exp_f32 v148, v148\n"
"v_mfma_f32_16x16x32_bf16 v[52:55], v[220:223], v[172:175], v[52:55]\n"
"v_exp_f32 v149, v149\n"
"v_mfma_f32_16x16x32_bf16 v[56:59], v[220:223], v[176:179], v[56:59]\n"
"ds_read_b128 v[220:223], v248 offset:1024\n"
"v_cvt_pk_bf16_f32 v145, v146, v147\n"
"v_mfma_f32_16x16x32_bf16 v[60:63], v[224:227], v[168:171], v[60:63]\n"
"v_exp_f32 v150, v150\n"
"v_mfma_f32_16x16x32_bf16 v[64:67], v[224:227], v[172:175], v[64:67]\n"
"v_exp_f32 v151, v151\n"
"v_mfma_f32_16x16x32_bf16 v[68:71], v[224:227], v[176:179], v[68:71]\n"
"ds_read_b128 v[224:227], v248 offset:5120\n"
"v_cvt_pk_bf16_f32 v146, v156, v157\n"
"s_waitcnt lgkmcnt(6)\n"
"v_mfma_f32_16x16x32_bf16 v[72:75], v[228:231], v[168:171], v[72:75]\n"
"s_add_u32 s69, s69, 1\n"
"s_sub_u32 s68, s68, 1\n"
"s_cselect_b32 s70, -40960, s75\n"
"s_cselect_b32 s68, 5, s68\n"
"v_exp_f32 v160, v160\n"
"v_mfma_f32_16x16x32_bf16 v[76:79], v[228:231], v[172:175], v[76:79]\n"
"v_exp_f32 v161, v161\n"
"v_mfma_f32_16x16x32_bf16 v[80:83], v[228:231], v[176:179], v[80:83]\n"
"ds_read_b128 v[228:231], v248 offset:1536\n"
"v_cvt_pk_bf16_f32 v147, v158, v159\n"
"v_mfma_f32_16x16x32_bf16 v[84:87], v[232:235], v[168:171], v[84:87]\n"
"v_exp_f32 v162, v162\n"
"v_mfma_f32_16x16x32_bf16 v[88:91], v[232:235], v[172:175], v[88:91]\n"
"v_exp_f32 v163, v163\n"
"v_mfma_f32_16x16x32_bf16 v[92:95], v[232:235], v[176:179], v[92:95]\n"
"ds_read_b128 v[232:235], v248 offset:5632\n"
"v_add_u32 v249, s70, v249\n"
"v_add_u32 v250, s70, v250\n"
"v_add_u32 v248, s70, v248\n"
"s_waitcnt vmcnt(4)\n"
"s_barrier\n"
"; step 18\n"
"s_sleep 3\n"
"s_waitcnt lgkmcnt(6)\n"
"v_mfma_f32_16x16x32_bf16 v[168:171], v[204:207], v[96:99], v[192:195]\n"
"v_cvt_pk_bf16_f32 v148, v148, v149\n"
"v_cvt_pk_bf16_f32 v149, v150, v151\n"
"v_mfma_f32_16x16x32_bf16 v[172:175], v[204:207], v[112:115], v[196:199]\n"
"v_cvt_pk_bf16_f32 v150, v160, v161\n"
"v_cvt_pk_bf16_f32 v151, v162, v163\n"
"v_mfma_f32_16x16x32_bf16 v[176:179], v[204:207], v[128:131], v[200:203]\n"
"ds_read_b64_tr_b16 v[204:205], v249 offset:0\n"
"ds_read_b64_tr_b16 v[206:207], v249 offset:4096\n"
"v_mfma_f32_16x16x32_bf16 v[180:183], v[208:211], v[96:99], v[192:195]\n"
"v_exp_f32 v152, v152\n"
"v_mfma_f32_16x16x32_bf16 v[184:187], v[208:211], v[112:115], v[196:199]\n"
"v_exp_f32 v153, v153\n"
"v_mfma_f32_16x16x32_bf16 v[188:191], v[208:211], v[128:131], v[200:203]\n"
"ds_read_b64_tr_b16 v[208:209], v250 offset:0\n"
"ds_read_b64_tr_b16 v[210:211], v250 offset:4096\n"
"s_waitcnt lgkmcnt(8)\n"
"v_mfma_f32_16x16x32_bf16 v[168:171], v[212:215], v[100:103], v[168:171]\n"
"v_exp_f32 v154, v154\n"
"v_mfma_f32_16x16x32_bf16 v[172:175], v[212:215], v[116:119], v[172:175]\n"
"v_exp_f32 v155, v155\n"
"v_mfma_f32_16x16x32_bf16 v[176:179], v[212:215], v[132:135], v[176:179]\n"
"ds_read_b64_tr_b16 v[212:213], v249 offset:512\n"
"ds_read_b64_tr_b16 v[214:215], v249 offset:4608\n"
"v_mfma_f32_16x16x32_bf16 v[180:183], v[216:219], v[100:103], v[180:183]\n"
"v_exp_f32 v164, v164\n"
"v_mfma_f32_16x16x32_bf16 v[184:187], v[216:219], v[116:119], v[184:187]\n"
"v_exp_f32 v165, v165\n"
"v_mfma_f32_16x16x32_bf16 v[188:191], v[216:219], v[132:135], v[188:191]\n"
"ds_read_b64_tr_b16 v[216:217], v250 offset:512\n"
"ds_read_b64_tr_b16 v[218:219], v250 offset:4608\n"
"s_waitcnt lgkmcnt(10)\n"
"v_mfma_f32_16x16x32_bf16 v[168:171], v[220:223], v[104:107], v[168:171]\n"
"v_exp_f32 v166, v166\n"
"v_mfma_f32_16x16x32_bf16 v[172:175], v[220:223], v[120:123], v[172:175]\n"
"v_exp_f32 v167, v167\n"
"v_mfma_f32_16x16x32_bf16 v[176:179], v[220:223], v[136:139], v[176:179]\n"
"ds_read_b64_tr_b16 v[220:221], v249 offset:1024\n"
"ds_read_b64_tr_b16 v[222:223], v249 offset:5120\n"
"v_mfma_f32_16x16x32_bf16 v[180:183], v[224:227], v[104:107], v[180:183]\n"
"v_cvt_pk_bf16_f32 v152, v152, v153\n"
"v_cvt_pk_bf16_f32 v153, v154, v155\n"
"v_mfma_f32_16x16x32_bf16 v[184:187], v[224:227], v[120:123], v[184:187]\n"
"v_cvt_pk_bf16_f32 v154, v164, v165\n"
"v_cvt_pk_bf16_f32 v155, v166, v167\n"
"v_mfma_f32_16x16x32_bf16 v[188:191], v[224:227], v[136:139], v[188:191]\n"
"ds_read_b64_tr_b16 v[224:225], v250 offset:1024\n"
"ds_read_b64_tr_b16 v[226:227], v250 offset:5120\n"
"v_mfma_f32_16x16x32_bf16 v[156:159], v[244:247], v[144:147], 0\n"
"v_mfma_f32_16x16x32_bf16 v[160:163], v[244:247], v[148:151], 0\n"
"v_mfma_f32_16x16x32_bf16 v[164:167], v[244:247], v[152:155], 0\n"
"s_waitcnt lgkmcnt(12)\n"
"v_mfma_f32_16x16x32_bf16 v[168:171], v[228:231], v[108:111], v[168:171]\n"
"v_mfma_f32_16x16x32_bf16 v[172:175], v[228:231], v[124:127], v[172:175]\n"
"v_mfma_f32_16x16x32_bf16 v[176:179], v[228:231], v[140:143], v[176:179]\n"
"ds_read_b64_tr_b16 v[228:229], v249 offset:1536\n"
"ds_read_b64_tr_b16 v[230:231], v249 offset:5632\n"
"v_mfma_f32_16x16x32_bf16 v[180:183], v[232:235], v[108:111], v[180:183]\n"
"s_nop 0\n"
"v_max3_f32 v242, v156, v160, v164\n"
"v_mfma_f32_16x16x32_bf16 v[184:187], v[232:235], v[124:127], v[184:187]\n"
"v_cmp_nge_f32 vcc, 0x46000000, v242\n"
"v_mfma_f32_16x16x32_bf16 v[188:191], v[232:235], v[140:143], v[188:191]\n"
"ds_read_b64_tr_b16 v[232:233], v250 offset:1536\n"
"ds_read_b64_tr_b16 v[234:235], v250 offset:5632\n"
"s_cbranch_vccnz .LrAy37\n"
".LrAby37:\n"
"s_waitcnt lgkmcnt(12)\n"
"v_mfma_f32_16x16x32_bf16 v[0:3], v[204:207], v[144:147], v[0:3]\n"
"v_exp_f32 v168, v168\n"
"v_mfma_f32_16x16x32_bf16 v[4:7], v[204:207], v[148:151], v[4:7]\n"
"v_exp_f32 v169, v169\n"
"v_mfma_f32_16x16x32_bf16 v[8:11], v[204:207], v[152:155], v[8:11]\n"
"ds_read_b128 v[204:207], v248 offset:0\n"
"v_add_f32 v236, v236, v156\n"
"v_mfma_f32_16x16x32_bf16 v[12:15], v[208:211], v[144:147], v[12:15]\n"
"v_exp_f32 v170, v170\n"
"v_mfma_f32_16x16x32_bf16 v[16:19], v[208:211], v[148:151], v[16:19]\n"
"v_exp_f32 v171, v171\n"
"v_mfma_f32_16x16x32_bf16 v[20:23], v[208:211], v[152:155], v[20:23]\n"
"ds_read_b128 v[208:211], v248 offset:4096\n"
"v_add_f32 v237, v237, v160\n"
"s_waitcnt lgkmcnt(10)\n"
"v_mfma_f32_16x16x32_bf16 v[24:27], v[212:215], v[144:147], v[24:27]\n"
"v_exp_f32 v180, v180\n"
"v_mfma_f32_16x16x32_bf16 v[28:31], v[212:215], v[148:151], v[28:31]\n"
"v_exp_f32 v181, v181\n"
"v_mfma_f32_16x16x32_bf16 v[32:35], v[212:215], v[152:155], v[32:35]\n"
"ds_read_b128 v[212:215], v248 offset:512\n"
"v_add_f32 v238, v238, v164\n"
"v_mfma_f32_16x16x32_bf16 v[36:39], v[216:219], v[144:147], v[36:39]\n"
"v_exp_f32 v182, v182\n"
"v_mfma_f32_16x16x32_bf16 v[40:43], v[216:219], v[148:151], v[40:43]\n"
"v_exp_f32 v183, v183\n"
"v_mfma_f32_16x16x32_bf16 v[44:47], v[216:219], v[152:155], v[44:47]\n"
"ds_read_b128 v[216:219], v248 offset:4608\n"
"v_cvt_pk_bf16_f32 v168, v168, v169\n"
"s_waitcnt lgkmcnt(8)\n"
"v_mfma_f32_16x16x32_bf16 v[48:51], v[220:223], v[144:147], v[48:51]\n"
"v_exp_f32 v172, v172\n"
"v_mfma_f32_16x16x32_bf16 v[52:55], v[220:223], v[148:151], v[52:55]\n"
"v_exp_f32 v173, v173\n"
"v_mfma_f32_16x16x32_bf16 v[56:59], v[220:223], v[152:155], v[56:59]\n"
"ds_read_b128 v[220:223], v248 offset:1024\n"
"v_cvt_pk_bf16_f32 v169, v170, v171\n"
"v_mfma_f32_16x16x32_bf16 v[60:63], v[224:227], v[144:147], v[60:63]\n"
"v_exp_f32 v174, v174\n"
"v_mfma_f32_16x16x32_bf16 v[64:67], v[224:227], v[148:151], v[64:67]\n"
"v_exp_f32 v175, v175\n"
"v_mfma_f32_16x16x32_bf16 v[68:71], v[224:227], v[152:155], v[68:71]\n"
"ds_read_b128 v[224:227], v248 offset:5120\n"
"v_cvt_pk_bf16_f32 v170, v180, v181\n"
"s_waitcnt lgkmcnt(6)\n"
"v_mfma_f32_16x16x32_bf16 v[72:75], v[228:231], v[144:147], v[72:75]\n"
"s_add_u32 s69, s69, 1\n"
"s_sub_u32 s68, s68, 1\n"
"s_cselect_b32 s70, -40960, s75\n"
"s_cselect_b32 s68, 5, s68\n"
"v_exp_f32 v184, v184\n"
"v_mfma_f32_16x16x32_bf16 v[76:79], v[228:231], v[148:151], v[76:79]\n"
"v_exp_f32 v185, v185\n"
"v_mfma_f32_16x16x32_bf16 v[80:83], v[228:231], v[152:155], v[80:83]\n"
"ds_read_b128 v[228:231], v248 offset:1536\n"
"v_cvt_pk_bf16_f32 v171, v182, v183\n"
"v_mfma_f32_16x16x32_bf16 v[84:87], v[232:235], v[144:147], v[84:87]\n"
"v_exp_f32 v186, v186\n"
"v_mfma_f32_16x16x32_bf16 v[88:91], v[232:235], v[148:151], v[88:91]\n"
"v_exp_f32 v187, v187\n"
"v_mfma_f32_16x16x32_bf16 v[92:95], v[232:235], v[152:155], v[92:95]\n"
"ds_read_b128 v[232:235], v248 offset:5632\n"
"v_add_u32 v249, s70, v249\n"
"v_add_u32 v250, s70, v250\n"
"v_add_u32 v248, s70, v248\n"
"s_waitcnt vmcnt(0)\n"
"s_barrier\n"
"s_mov_b32 s72, 5\n"
".Lyloop38:\n"
"; step 19\n"
"s_waitcnt lgkmcnt(6)\n"
"v_mfma_f32_16x16x32_bf16 v[144:147], v[204:207], v[96:99], v[192:195]\n"
"v_cvt_pk_bf16_f32 v172, v172, v173\n"
"v_cvt_pk_bf16_f32 v173, v174, v175\n"
"v_mfma_f32_16x16x32_bf16 v[148:151], v[204:207], v[112:115], v[196:199]\n"
"v_cvt_pk_bf16_f32 v174, v184, v185\n"
"v_cvt_pk_bf16_f32 v175, v186, v187\n"
"v_mfma_f32_16x16x32_bf16 v[152:155], v[204:207], v[128:131], v[200:203]\n"
"ds_read_b64_tr_b16 v[204:205], v249 offset:0\n"
"ds_read_b64_tr_b16 v[206:207], v249 offset:4096\n"
"v_mfma_f32_16x16x32_bf16 v[156:159], v[208:211], v[96:99], v[192:195]\n"
"v_exp_f32 v176, v176\n"
"v_mfma_f32_16x16x32_bf16 v[160:163], v[208:211], v[112:115], v[196:199]\n"
"v_exp_f32 v177, v177\n"
"v_mfma_f32_16x16x32_bf16 v[164:167], v[208:211], v[128:131], v[200:203]\n"
"ds_read_b64_tr_b16 v[208:209], v250 offset:0\n"
"ds_read_b64_tr_b16 v[210:211], v250 offset:4096\n"
"s_waitcnt lgkmcnt(8)\n"
"v_mfma_f32_16x16x32_bf16 v[144:147], v[212:215], v[100:103], v[144:147]\n"
"v_exp_f32 v178, v178\n"
"v_mfma_f32_16x16x32_bf16 v[148:151], v[212:215], v[116:119], v[148:151]\n"
"v_exp_f32 v179, v179\n"
"v_mfma_f32_16x16x32_bf16 v[152:155], v[212:215], v[132:135], v[152:155]\n"
"ds_read_b64_tr_b16 v[212:213], v249 offset:512\n"
"ds_read_b64_tr_b16 v[214:215], v249 offset:4608\n"
"v_mfma_f32_16x16x32_bf16 v[156:159], v[216:219], v[100:103], v[156:159]\n"
"v_exp_f32 v188, v188\n"
"v_mfma_f32_16x16x32_bf16 v[160:163], v[216:219], v[116:119], v[160:163]\n"
"v_exp_f32 v189, v189\n"
"v_mfma_f32_16x16x32_bf16 v[164:167], v[216:219], v[132:135], v[164:167]\n"
"ds_read_b64_tr_b16 v[216:217], v250 offset:512\n"
"ds_read_b64_tr_b16 v[218:219], v250 offset:4608\n"
"s_waitcnt lgkmcnt(10)\n"
"v_mfma_f32_16x16x32_bf16 v[144:147], v[220:223], v[104:107], v[144:147]\n"
"v_exp_f32 v190, v190\n"
"v_mfma_f32_16x16x32_bf16 v[148:151], v[220:223], v[120:123], v[148:151]\n"
"v_exp_f32 v191, v191\n"
"v_mfma_f32_16x16x32_bf16 v[152:155], v[220:223], v[136:139], v[152:155]\n"
"ds_read_b64_tr_b16 v[220:221], v249 offset:1024\n"
"ds_read_b64_tr_b16 v[222:223], v249 offset:5120\n"
"v_mfma_f32_16x16x32_bf16 v[156:159], v[224:227], v[104:107], v[156:159]\n"
"v_cvt_pk_bf16_f32 v176, v176, v177\n"
"v_cvt_pk_bf16_f32 v177, v178, v179\n"
"v_mfma_f32_16x16x32_bf16 v[160:163], v[224:227], v[120:123], v[160:163]\n"
"v_cvt_pk_bf16_f32 v178, v188, v189\n"
"v_cvt_pk_bf16_f32 v179, v190, v191\n"
"v_mfma_f32_16x16x32_bf16 v[164:167], v[224:227], v[136:139], v[164:167]\n"
"ds_read_b64_tr_b16 v[224:225], v250 offset:1024\n"
"ds_read_b64_tr_b16 v[226:227], v250 offset:5120\n"
"v_mfma_f32_16x16x32_bf16 v[180:183], v[244:247], v[168:171], 0\n"
"v_mfma_f32_16x16x32_bf16 v[184:187], v[244:247], v[172:175], 0\n"
"v_mfma_f32_16x16x32_bf16 v[188:191], v[244:247], v[176:179], 0\n"
"s_waitcnt lgkmcnt(12)\n"
"v_mfma_f32_16x16x32_bf16 v[144:147], v[228:231], v[108:111], v[144:147]\n"
"v_mfma_f32_16x16x32_bf16 v[148:151], v[228:231], v[124:127], v[148:151]\n"
"v_mfma_f32_16x16x32_bf16 v[152:155], v[228:231], v[140:143], v[152:155]\n"
"ds_read_b64_tr_b16 v[228:229], v249 offset:1536\n"
"ds_read_b64_tr_b16 v[230:231], v249 offset:5632\n"
"v_mfma_f32_16x16x32_bf16 v[156:159], v[232:235], v[108:111], v[156:159]\n"
"s_nop 0\n"
"v_max3_f32 v242, v180, v184, v188\n"
"v_mfma_f32_16x16x32_bf16 v[160:163], v[232:235], v[124:127], v[160:163]\n"
"v_cmp_nge_f32 vcc, 0x46000000, v242\n"
"v_mfma_f32_16x16x32_bf16 v[164:167], v[232:235], v[140:143], v[164:167]\n"
"ds_read_b64_tr_b16 v[232:233], v250 offset:1536\n"
"ds_read_b64_tr_b16 v[234:235], v250 offset:5632\n"
"s_cbranch_vccnz .LrAy38\n"
".LrAby38:\n"
"s_waitcnt lgkmcnt(12)\n"
"v_mfma_f32_16x16x32_bf16 v[0:3], v[204:207], v[168:171], v[0:3]\n"
"v_exp_f32 v144, v144\n"
"v_mfma_f32_16x16x32_bf16 v[4:7], v[204:207], v[172:175], v[4:7]\n"
"v_exp_f32 v145, v145\n"
"v_mfma_f32_16x16x32_bf16 v[8:11], v[204:207], v[176:179], v[8:11]\n"
"ds_read_b128 v[204:207], v248 offset:0\n"
"v_add_f32 v236, v236, v180\n"
"v_mfma_f32_16x16x32_bf16 v[12:15], v[208:211], v[168:171], v[12:15]\n"
"v_exp_f32 v146, v146\n"
"v_mfma_f32_16x16x32_bf16 v[16:19], v[208:211], v[172:175], v[16:19]\n"
"v_exp_f32 v147, v147\n"
"v_mfma_f32_16x16x32_bf16 v[20:23], v[208:211], v[176:179], v[20:23]\n"
"ds_read_b128 v[208:211], v248 offset:4096\n"
"v_add_f32 v237, v237, v184\n"
"s_waitcnt lgkmcnt(10)\n"
"v_mfma_f32_16x16x32_bf16 v[24:27], v[212:215], v[168:171], v[24:27]\n"
"v_exp_f32 v156, v156\n"
"v_mfma_f32_16x16x32_bf16 v[28:31], v[212:215], v[172:175], v[28:31]\n"
"v_exp_f32 v157, v157\n"
"v_mfma_f32_16x16x32_bf16 v[32:35], v[212:215], v[176:179], v[32:35]\n"
"ds_read_b128 v[212:215], v248 offset:512\n"
"v_add_f32 v238, v238, v188\n"
"v_mfma_f32_16x16x32_bf16 v[36:39], v[216:219], v[168:171], v[36:39]\n"
"v_exp_f32 v158, v158\n"
"v_mfma_f32_16x16x32_bf16 v[40:43], v[216:219], v[172:175], v[40:43]\n"
"v_exp_f32 v159, v159\n"
"v_mfma_f32_16x16x32_bf16 v[44:47], v[216:219], v[176:179], v[44:47]\n"
"ds_read_b128 v[216:219], v248 offset:4608\n"
"v_cvt_pk_bf16_f32 v144, v144, v145\n"
"s_waitcnt lgkmcnt(8)\n"
"v_mfma_f32_16x16x32_bf16 v[48:51], v[220:223], v[168:171], v[48:51]\n"
"v_exp_f32 v148, v148\n"
"v_mfma_f32_16x16x32_bf16 v[52:55], v[220:223], v[172:175], v[52:55]\n"
"v_exp_f32 v149, v149\n"
"v_mfma_f32_16x16x32_bf16 v[56:59], v[220:223], v[176:179], v[56:59]\n"
"ds_read_b128 v[220:223], v248 offset:1024\n"
"v_cvt_pk_bf16_f32 v145, v146, v147\n"
"v_mfma_f32_16x16x32_bf16 v[60:63], v[224:227], v[168:171], v[60:63]\n"
"v_exp_f32 v150, v150\n"
"v_mfma_f32_16x16x32_bf16 v[64:67], v[224:227], v[172:175], v[64:67]\n"
"v_exp_f32 v151, v151\n"
"v_mfma_f32_16x16x32_bf16 v[68:71], v[224:227], v[176:179], v[68:71]\n"
"ds_read_b128 v[224:227], v248 offset:5120\n"
"v_cvt_pk_bf16_f32 v146, v156, v157\n"
"s_waitcnt lgkmcnt(6)\n"
"v_mfma_f32_16x16x32_bf16 v[72:75], v[228:231], v[168:171], v[72:75]\n"
"s_add_u32 s69, s69, 1\n"
"s_sub_u32 s68, s68, 1\n"
"s_cselect_b32 s70, -40960, s75\n"
"s_cselect_b32 s68, 5, s68\n"
"v_exp_f32 v160, v160\n"
"v_mfma_f32_16x16x32_bf16 v[76:79], v[228:231], v[172:175], v[76:79]\n"
"v_exp_f32 v161, v161\n"
"v_mfma_f32_16x16x32_bf16 v[80:83], v[228:231], v[176:179], v[80:83]\n"
"ds_read_b128 v[228:231], v248 offset:1536\n"
"v_cvt_pk_bf16_f32 v147, v158, v159\n"
"v_mfma_f32_16x16x32_bf16 v[84:87], v[232:235], v[168:171], v[84:87]\n"
"v_exp_f32 v162, v162\n"
"v_mfma_f32_16x16x32_bf16 v[88:91], v[232:235], v[172:175], v[88:91]\n"
"v_exp_f32 v163, v163\n"
"v_mfma_f32_16x16x32_bf16 v[92:95], v[232:235], v[176:179], v[92:95]\n"
"ds_read_b128 v[232:235], v248 offset:5632\n"
"v_add_u32 v249, s70, v249\n"
"v_add_u32 v250, s70, v250\n"
"v_add_u32 v248, s70, v248\n"
"s_barrier\n"
"; step 20\n"
"s_waitcnt lgkmcnt(6)\n"
"v_mfma_f32_16x16x32_bf16 v[168:171], v[204:207], v[96:99], v[192:195]\n"
"v_cvt_pk_bf16_f32 v148, v148, v149\n"
"v_cvt_pk_bf16_f32 v149, v150, v151\n"
"v_mfma_f32_16x16x32_bf16 v[172:175], v[204:207], v[112:115], v[196:199]\n"
"v_cvt_pk_bf16_f32 v150, v160, v161\n"
"v_cvt_pk_bf16_f32 v151, v162, v163\n"
"v_mfma_f32_16x16x32_bf16 v[176:179], v[204:207], v[128:131], v[200:203]\n"
"ds_read_b64_tr_b16 v[204:205], v249 offset:0\n"
"ds_read_b64_tr_b16 v[206:207], v249 offset:4096\n"
"v_mfma_f32_16x16x32_bf16 v[180:183], v[208:211], v[96:99], v[192:195]\n"
"v_exp_f32 v152, v152\n"
"v_mfma_f32_16x16x32_bf16 v[184:187], v[208:211], v[112:115], v[196:199]\n"
"v_exp_f32 v153, v153\n"
"v_mfma_f32_16x16x32_bf16 v[188:191], v[208:211], v[128:131], v[200:203]\n"
"ds_read_b64_tr_b16 v[208:209], v250 offset:0\n"
"ds_read_b64_tr_b16 v[210:211], v250 offset:4096\n"
"s_waitcnt lgkmcnt(8)\n"
"v_mfma_f32_16x16x32_bf16 v[168:171], v[212:215], v[100:103], v[168:171]\n"
"v_exp_f32 v154, v154\n"
"v_mfma_f32_16x16x32_bf16 v[172:175], v[212:215], v[116:119], v[172:175]\n"
"v_exp_f32 v155, v155\n"
"v_mfma_f32_16x16x32_bf16 v[176:179], v[212:215], v[132:135], v[176:179]\n"
"ds_read_b64_tr_b16 v[212:213], v249 offset:512\n"
"ds_read_b64_tr_b16 v[214:215], v249 offset:4608\n"
"v_mfma_f32_16x16x32_bf16 v[180:183], v[216:219], v[100:103], v[180:183]\n"
"v_exp_f32 v164, v164\n"
"v_mfma_f32_16x16x32_bf16 v[184:187], v[216:219], v[116:119], v[184:187]\n"
"v_exp_f32 v165, v165\n"
"v_mfma_f32_16x16x32_bf16 v[188:191], v[216:219], v[132:135], v[188:191]\n"
"ds_read_b64_tr_b16 v[216:217], v250 offset:512\n"
"ds_read_b64_tr_b16 v[218:219], v250 offset:4608\n"
"s_waitcnt lgkmcnt(10)\n"
"v_mfma_f32_16x16x32_bf16 v[168:171], v[220:223], v[104:107], v[168:171]\n"
"v_exp_f32 v166, v166\n"
"v_mfma_f32_16x16x32_bf16 v[172:175], v[220:223], v[120:123], v[172:175]\n"
"v_exp_f32 v167, v167\n"
"v_mfma_f32_16x16x32_bf16 v[176:179], v[220:223], v[136:139], v[176:179]\n"
"ds_read_b64_tr_b16 v[220:221], v249 offset:1024\n"
"ds_read_b64_tr_b16 v[222:223], v249 offset:5120\n"
"v_mfma_f32_16x16x32_bf16 v[180:183], v[224:227], v[104:107], v[180:183]\n"
"v_cvt_pk_bf16_f32 v152, v152, v153\n"
"v_cvt_pk_bf16_f32 v153, v154, v155\n"
"v_mfma_f32_16x16x32_bf16 v[184:187], v[224:227], v[120:123], v[184:187]\n"
"v_cvt_pk_bf16_f32 v154, v164, v165\n"
"v_cvt_pk_bf16_f32 v155, v166, v167\n"
"v_mfma_f32_16x16x32_bf16 v[188:191], v[224:227], v[136:139], v[188:191]\n"
"ds_read_b64_tr_b16 v[224:225], v250 offset:1024\n"
"ds_read_b64_tr_b16 v[226:227], v250 offset:5120\n"
"v_mfma_f32_16x16x32_bf16 v[156:159], v[244:247], v[144:147], 0\n"
"v_mfma_f32_16x16x32_bf16 v[160:163], v[244:247], v[148:151], 0\n"
"v_mfma_f32_16x16x32_bf16 v[164:167], v[244:247], v[152:155], 0\n"
"s_waitcnt lgkmcnt(12)\n"
"v_mfma_f32_16x16x32_bf16 v[168:171], v[228:231], v[108:111], v[168:171]\n"
"v_mfma_f32_16x16x32_bf16 v[172:175], v[228:231], v[124:127], v[172:175]\n"
"v_mfma_f32_16x16x32_bf16 v[176:179], v[228:231], v[140:143], v[176:179]\n"
"ds_read_b64_tr_b16 v[228:229], v249 offset:1536\n"
"ds_read_b64_tr_b16 v[230:231], v249 offset:5632\n"
"v_mfma_f32_16x16x32_bf16 v[180:183], v[232:235], v[108:111], v[180:183]\n"
"s_nop 0\n"
"v_max3_f32 v242, v156, v160, v164\n"
"v_mfma_f32_16x16x32_bf16 v[184:187], v[232:235], v[124:127], v[184:187]\n"
"v_cmp_nge_f32 vcc, 0x46000000, v242\n"
"v_mfma_f32_16x16x32_bf16 v[188:191], v[232:235], v[140:143], v[188:191]\n"
"ds_read_b64_tr_b16 v[232:233], v250 offset:1536\n"
"ds_read_b64_tr_b16 v[234:235], v250 offset:5632\n"
"s_cbranch_vccnz .LrAy39\n"
".LrAby39:\n"
"s_waitcnt lgkmcnt(12)\n"
"v_mfma_f32_16x16x32_bf16 v[0:3], v[204:207], v[144:147], v[0:3]\n"
"v_exp_f32 v168, v168\n"
"v_mfma_f32_16x16x32_bf16 v[4:7], v[204:207], v[148:151], v[4:7]\n"
"v_exp_f32 v169, v169\n"
"v_mfma_f32_16x16x32_bf16 v[8:11], v[204:207], v[152:155], v[8:11]\n"
"ds_read_b128 v[204:207], v248 offset:0\n"
"v_add_f32 v236, v236, v156\n"
"v_mfma_f32_16x16x32_bf16 v[12:15], v[208:211], v[144:147], v[12:15]\n"
"v_exp_f32 v170, v170\n"
"v_mfma_f32_16x16x32_bf16 v[16:19], v[208:211], v[148:151], v[16:19]\n"
"v_exp_f32 v171, v171\n"
"v_mfma_f32_16x16x32_bf16 v[20:23], v[208:211], v[152:155], v[20:23]\n"
"ds_read_b128 v[208:211], v248 offset:4096\n"
"v_add_f32 v237, v237, v160\n"
"s_waitcnt lgkmcnt(10)\n"
"v_mfma_f32_16x16x32_bf16 v[24:27], v[212:215], v[144:147], v[24:27]\n"
"v_exp_f32 v180, v180\n"
"v_mfma_f32_16x16x32_bf16 v[28:31], v[212:215], v[148:151], v[28:31]\n"
"v_exp_f32 v181, v181\n"
"v_mfma_f32_16x16x32_bf16 v[32:35], v[212:215], v[152:155], v[32:35]\n"
"ds_read_b128 v[212:215], v248 offset:512\n"
"v_add_f32 v238, v238, v164\n"
"v_mfma_f32_16x16x32_bf16 v[36:39], v[216:219], v[144:147], v[36:39]\n"
"v_exp_f32 v182, v182\n"
"v_mfma_f32_16x16x32_bf16 v[40:43], v[216:219], v[148:151], v[40:43]\n"
"v_exp_f32 v183, v183\n"
"v_mfma_f32_16x16x32_bf16 v[44:47], v[216:219], v[152:155], v[44:47]\n"
"ds_read_b128 v[216:219], v248 offset:4608\n"
"v_cvt_pk_bf16_f32 v168, v168, v169\n"
"s_waitcnt lgkmcnt(8)\n"
"v_mfma_f32_16x16x32_bf16 v[48:51], v[220:223], v[144:147], v[48:51]\n"
"v_exp_f32 v172, v172\n"
"v_mfma_f32_16x16x32_bf16 v[52:55], v[220:223], v[148:151], v[52:55]\n"
"v_exp_f32 v173, v173\n"
"v_mfma_f32_16x16x32_bf16 v[56:59], v[220:223], v[152:155], v[56:59]\n"
"ds_read_b128 v[220:223], v248 offset:1024\n"
"v_cvt_pk_bf16_f32 v169, v170, v171\n"
"v_mfma_f32_16x16x32_bf16 v[60:63], v[224:227], v[144:147], v[60:63]\n"
"v_exp_f32 v174, v174\n"
"v_mfma_f32_16x16x32_bf16 v[64:67], v[224:227], v[148:151], v[64:67]\n"
"v_exp_f32 v175, v175\n"
"v_mfma_f32_16x16x32_bf16 v[68:71], v[224:227], v[152:155], v[68:71]\n"
"ds_read_b128 v[224:227], v248 offset:5120\n"
"v_cvt_pk_bf16_f32 v170, v180, v181\n"
"s_waitcnt lgkmcnt(6)\n"
"v_mfma_f32_16x16x32_bf16 v[72:75], v[228:231], v[144:147], v[72:75]\n"
"s_add_u32 s69, s69, 1\n"
"s_sub_u32 s68, s68, 1\n"
"s_cselect_b32 s70, -40960, s75\n"
"s_cselect_b32 s68, 5, s68\n"
"v_exp_f32 v184, v184\n"
"v_mfma_f32_16x16x32_bf16 v[76:79], v[228:231], v[148:151], v[76:79]\n"
"v_exp_f32 v185, v185\n"
"v_mfma_f32_16x16x32_bf16 v[80:83], v[228:231], v[152:155], v[80:83]\n"
"ds_read_b128 v[228:231], v248 offset:1536\n"
"v_cvt_pk_bf16_f32 v171, v182, v183\n"
"v_mfma_f32_16x16x32_bf16 v[84:87], v[232:235], v[144:147], v[84:87]\n"
"v_exp_f32 v186, v186\n"
"v_mfma_f32_16x16x32_bf16 v[88:91], v[232:235], v[148:151], v[88:91]\n"
"v_exp_f32 v187, v187\n"
"v_mfma_f32_16x16x32_bf16 v[92:95], v[232:235], v[152:155], v[92:95]\n"
"ds_read_b128 v[232:235], v248 offset:5632\n"
"v_add_u32 v249, s70, v249\n"
"v_add_u32 v250, s70, v250\n"
"v_add_u32 v248, s70, v248\n"
"s_barrier\n"
"s_sub_u32 s72, s72, 1\n"
"s_cmp_lg_u32 s72, 0\n"
"s_cbranch_scc1 .Lyloop38\n"
"; step 29\n"
"s_waitcnt lgkmcnt(6)\n"
"v_mfma_f32_16x16x32_bf16 v[144:147], v[204:207], v[96:99], v[192:195]\n"
"v_cvt_pk_bf16_f32 v172, v172, v173\n"
"v_cvt_pk_bf16_f32 v173, v174, v175\n"
"v_mfma_f32_16x16x32_bf16 v[148:151], v[204:207], v[112:115], v[196:199]\n"
"v_cvt_pk_bf16_f32 v174, v184, v185\n"
"v_cvt_pk_bf16_f32 v175, v186, v187\n"
"v_mfma_f32_16x16x32_bf16 v[152:155], v[204:207], v[128:131], v[200:203]\n"
"ds_read_b64_tr_b16 v[204:205], v249 offset:0\n"
"ds_read_b64_tr_b16 v[206:207], v249 offset:4096\n"
"v_mfma_f32_16x16x32_bf16 v[156:159], v[208:211], v[96:99], v[192:195]\n"
"v_exp_f32 v176, v176\n"
"v_mfma_f32_16x16x32_bf16 v[160:163], v[208:211], v[112:115], v[196:199]\n"
"v_exp_f32 v177, v177\n"
"v_mfma_f32_16x16x32_bf16 v[164:167], v[208:211], v[128:131], v[200:203]\n"
"ds_read_b64_tr_b16 v[208:209], v250 offset:0\n"
"ds_read_b64_tr_b16 v[210:211], v250 offset:4096\n"
"s_waitcnt lgkmcnt(8)\n"
"v_mfma_f32_16x16x32_bf16 v[144:147], v[212:215], v[100:103], v[144:147]\n"
"v_exp_f32 v178, v178\n"
"v_mfma_f32_16x16x32_bf16 v[148:151], v[212:215], v[116:119], v[148:151]\n"
"v_exp_f32 v179, v179\n"
"v_mfma_f32_16x16x32_bf16 v[152:155], v[212:215], v[132:135], v[152:155]\n"
"ds_read_b64_tr_b16 v[212:213], v249 offset:512\n"
"ds_read_b64_tr_b16 v[214:215], v249 offset:4608\n"
"v_mfma_f32_16x16x32_bf16 v[156:159], v[216:219], v[100:103], v[156:159]\n"
"v_exp_f32 v188, v188\n"
"v_mfma_f32_16x16x32_bf16 v[160:163], v[216:219], v[116:119], v[160:163]\n"
"v_exp_f32 v189, v189\n"
"v_mfma_f32_16x16x32_bf16 v[164:167], v[216:219], v[132:135], v[164:167]\n"
"ds_read_b64_tr_b16 v[216:217], v250 offset:512\n"
"ds_read_b64_tr_b16 v[218:219], v250 offset:4608\n"
"s_waitcnt lgkmcnt(10)\n"
"v_mfma_f32_16x16x32_bf16 v[144:147], v[220:223], v[104:107], v[144:147]\n"
"v_exp_f32 v190, v190\n"
"v_mfma_f32_16x16x32_bf16 v[148:151], v[220:223], v[120:123], v[148:151]\n"
"v_exp_f32 v191, v191\n"
"v_mfma_f32_16x16x32_bf16 v[152:155], v[220:223], v[136:139], v[152:155]\n"
"ds_read_b64_tr_b16 v[220:221], v249 offset:1024\n"
"ds_read_b64_tr_b16 v[222:223], v249 offset:5120\n"
"v_mfma_f32_16x16x32_bf16 v[156:159], v[224:227], v[104:107], v[156:159]\n"
"v_cvt_pk_bf16_f32 v176, v176, v177\n"
"v_cvt_pk_bf16_f32 v177, v178, v179\n"
"v_mfma_f32_16x16x32_bf16 v[160:163], v[224:227], v[120:123], v[160:163]\n"
"v_cvt_pk_bf16_f32 v178, v188, v189\n"
"v_cvt_pk_bf16_f32 v179, v190, v191\n"
"v_mfma_f32_16x16x32_bf16 v[164:167], v[224:227], v[136:139], v[164:167]\n"
"ds_read_b64_tr_b16 v[224:225], v250 offset:1024\n"
"ds_read_b64_tr_b16 v[226:227], v250 offset:5120\n"
"v_mfma_f32_16x16x32_bf16 v[180:183], v[244:247], v[168:171], 0\n"
"v_mfma_f32_16x16x32_bf16 v[184:187], v[244:247], v[172:175], 0\n"
"v_mfma_f32_16x16x32_bf16 v[188:191], v[244:247], v[176:179], 0\n"
"s_waitcnt lgkmcnt(12)\n"
"v_mfma_f32_16x16x32_bf16 v[144:147], v[228:231], v[108:111], v[144:147]\n"
"v_mfma_f32_16x16x32_bf16 v[148:151], v[228:231], v[124:127], v[148:151]\n"
"v_mfma_f32_16x16x32_bf16 v[152:155], v[228:231], v[140:143], v[152:155]\n"
"ds_read_b64_tr_b16 v[228:229], v249 offset:1536\n"
"ds_read_b64_tr_b16 v[230:231], v249 offset:5632\n"
"v_mfma_f32_16x16x32_bf16 v[156:159], v[232:235], v[108:111], v[156:159]\n"
"s_nop 0\n"
"v_max3_f32 v242, v180, v184, v188\n"
"v_mfma_f32_16x16x32_bf16 v[160:163], v[232:235], v[124:127], v[160:163]\n"
"v_cmp_nge_f32 vcc, 0x46000000, v242\n"
"v_mfma_f32_16x16x32_bf16 v[164:167], v[232:235], v[140:143], v[164:167]\n"
"ds_read_b64_tr_b16 v[232:233], v250 offset:1536\n"
"ds_read_b64_tr_b16 v[234:235], v250 offset:5632\n"
"s_cbranch_vccnz .LrAy40\n"
".LrAby40:\n"
"s_waitcnt lgkmcnt(12)\n"
"v_mfma_f32_16x16x32_bf16 v[0:3], v[204:207], v[168:171], v[0:3]\n"
"v_exp_f32 v144, v144\n"
"v_mfma_f32_16x16x32_bf16 v[4:7], v[204:207], v[172:175], v[4:7]\n"
"v_exp_f32 v145, v145\n"
"v_mfma_f32_16x16x32_bf16 v[8:11], v[204:207], v[176:179], v[8:11]\n"
"ds_read_b128 v[204:207], v248 offset:0\n"
"v_add_f32 v236, v236, v180\n"
"v_mfma_f32_16x16x32_bf16 v[12:15], v[208:211], v[168:171], v[12:15]\n"
"v_exp_f32 v146, v146\n"
"v_mfma_f32_16x16x32_bf16 v[16:19], v[208:211], v[172:175], v[16:19]\n"
"v_exp_f32 v147, v147\n"
"v_mfma_f32_16x16x32_bf16 v[20:23], v[208:211], v[176:179], v[20:23]\n"
"ds_read_b128 v[208:211], v248 offset:4096\n"
"v_add_f32 v237, v237, v184\n"
"s_waitcnt lgkmcnt(10)\n"
"v_mfma_f32_16x16x32_bf16 v[24:27], v[212:215], v[168:171], v[24:27]\n"
"v_exp_f32 v156, v156\n"
"v_mfma_f32_16x16x32_bf16 v[28:31], v[212:215], v[172:175], v[28:31]\n"
"v_exp_f32 v157, v157\n"
"v_mfma_f32_16x16x32_bf16 v[32:35], v[212:215], v[176:179], v[32:35]\n"
"ds_read_b128 v[212:215], v248 offset:512\n"
"v_add_f32 v238, v238, v188\n"
"v_mfma_f32_16x16x32_bf16 v[36:39], v[216:219], v[168:171], v[36:39]\n"
"v_exp_f32 v158, v158\n"
"v_mfma_f32_16x16x32_bf16 v[40:43], v[216:219], v[172:175], v[40:43]\n"
"v_exp_f32 v159, v159\n"
"v_mfma_f32_16x16x32_bf16 v[44:47], v[216:219], v[176:179], v[44:47]\n"
"ds_read_b128 v[216:219], v248 offset:4608\n"
"v_cvt_pk_bf16_f32 v144, v144, v145\n"
"s_waitcnt lgkmcnt(8)\n"
"v_mfma_f32_16x16x32_bf16 v[48:51], v[220:223], v[168:171], v[48:51]\n"
"v_exp_f32 v148, v148\n"
"v_mfma_f32_16x16x32_bf16 v[52:55], v[220:223], v[172:175], v[52:55]\n"
"v_exp_f32 v149, v149\n"
"v_mfma_f32_16x16x32_bf16 v[56:59], v[220:223], v[176:179], v[56:59]\n"
"ds_read_b128 v[220:223], v248 offset:1024\n"
"v_cvt_pk_bf16_f32 v145, v146, v147\n"
"v_mfma_f32_16x16x32_bf16 v[60:63], v[224:227], v[168:171], v[60:63]\n"
"v_exp_f32 v150, v150\n"
"v_mfma_f32_16x16x32_bf16 v[64:67], v[224:227], v[172:175], v[64:67]\n"
"v_exp_f32 v151, v151\n"
"v_mfma_f32_16x16x32_bf16 v[68:71], v[224:227], v[176:179], v[68:71]\n"
"ds_read_b128 v[224:227], v248 offset:5120\n"
"v_cvt_pk_bf16_f32 v146, v156, v157\n"
"s_waitcnt lgkmcnt(6)\n"
"v_mfma_f32_16x16x32_bf16 v[72:75], v[228:231], v[168:171], v[72:75]\n"
"s_add_u32 s69, s69, 1\n"
"s_sub_u32 s68, s68, 1\n"
"s_cselect_b32 s70, -40960, s75\n"
"s_cselect_b32 s68, 5, s68\n"
"v_exp_f32 v160, v160\n"
"v_mfma_f32_16x16x32_bf16 v[76:79], v[228:231], v[172:175], v[76:79]\n"
"v_exp_f32 v161, v161\n"
"v_mfma_f32_16x16x32_bf16 v[80:83], v[228:231], v[176:179], v[80:83]\n"
"ds_read_b128 v[228:231], v248 offset:1536\n"
"v_cvt_pk_bf16_f32 v147, v158, v159\n"
"v_mfma_f32_16x16x32_bf16 v[84:87], v[232:235], v[168:171], v[84:87]\n"
"v_exp_f32 v162, v162\n"
"v_mfma_f32_16x16x32_bf16 v[88:91], v[232:235], v[172:175], v[88:91]\n"
"v_exp_f32 v163, v163\n"
"v_mfma_f32_16x16x32_bf16 v[92:95], v[232:235], v[176:179], v[92:95]\n"
"ds_read_b128 v[232:235], v248 offset:5632\n"
"v_add_u32 v249, s70, v249\n"
"v_add_u32 v250, s70, v250\n"
"v_add_u32 v248, s70, v248\n"
"s_barrier\n"
"; step 30\n"
"s_waitcnt lgkmcnt(6)\n"
"v_mfma_f32_16x16x32_bf16 v[168:171], v[204:207], v[96:99], v[192:195]\n"
"v_cvt_pk_bf16_f32 v148, v148, v149\n"
"v_cvt_pk_bf16_f32 v149, v150, v151\n"
"v_mfma_f32_16x16x32_bf16 v[172:175], v[204:207], v[112:115], v[196:199]\n"
"v_cvt_pk_bf16_f32 v150, v160, v161\n"
"v_cvt_pk_bf16_f32 v151, v162, v163\n"
"v_mfma_f32_16x16x32_bf16 v[176:179], v[204:207], v[128:131], v[200:203]\n"
"ds_read_b64_tr_b16 v[204:205], v249 offset:0\n"
"ds_read_b64_tr_b16 v[206:207], v249 offset:4096\n"
"v_mfma_f32_16x16x32_bf16 v[180:183], v[208:211], v[96:99], v[192:195]\n"
"v_exp_f32 v152, v152\n"
"v_mfma_f32_16x16x32_bf16 v[184:187], v[208:211], v[112:115], v[196:199]\n"
"v_exp_f32 v153, v153\n"
"v_mfma_f32_16x16x32_bf16 v[188:191], v[208:211], v[128:131], v[200:203]\n"
"ds_read_b64_tr_b16 v[208:209], v250 offset:0\n"
"ds_read_b64_tr_b16 v[210:211], v250 offset:4096\n"
"s_waitcnt lgkmcnt(8)\n"
"v_mfma_f32_16x16x32_bf16 v[168:171], v[212:215], v[100:103], v[168:171]\n"
"v_exp_f32 v154, v154\n"
"v_mfma_f32_16x16x32_bf16 v[172:175], v[212:215], v[116:119], v[172:175]\n"
"v_exp_f32 v155, v155\n"
"v_mfma_f32_16x16x32_bf16 v[176:179], v[212:215], v[132:135], v[176:179]\n"
"ds_read_b64_tr_b16 v[212:213], v249 offset:512\n"
"ds_read_b64_tr_b16 v[214:215], v249 offset:4608\n"
"v_mfma_f32_16x16x32_bf16 v[180:183], v[216:219], v[100:103], v[180:183]\n"
"v_exp_f32 v164, v164\n"
"v_mfma_f32_16x16x32_bf16 v[184:187], v[216:219], v[116:119], v[184:187]\n"
"v_exp_f32 v165, v165\n"
"v_mfma_f32_16x16x32_bf16 v[188:191], v[216:219], v[132:135], v[188:191]\n"
"ds_read_b64_tr_b16 v[216:217], v250 offset:512\n"
"ds_read_b64_tr_b16 v[218:219], v250 offset:4608\n"
"s_waitcnt lgkmcnt(10)\n"
"v_mfma_f32_16x16x32_bf16 v[168:171], v[220:223], v[104:107], v[168:171]\n"
"v_exp_f32 v166, v166\n"
"v_mfma_f32_16x16x32_bf16 v[172:175], v[220:223], v[120:123], v[172:175]\n"
"v_exp_f32 v167, v167\n"
"v_mfma_f32_16x16x32_bf16 v[176:179], v[220:223], v[136:139], v[176:179]\n"
"ds_read_b64_tr_b16 v[220:221], v249 offset:1024\n"
"ds_read_b64_tr_b16 v[222:223], v249 offset:5120\n"
"v_mfma_f32_16x16x32_bf16 v[180:183], v[224:227], v[104:107], v[180:183]\n"
"v_cvt_pk_bf16_f32 v152, v152, v153\n"
"v_cvt_pk_bf16_f32 v153, v154, v155\n"
"v_mfma_f32_16x16x32_bf16 v[184:187], v[224:227], v[120:123], v[184:187]\n"
"v_cvt_pk_bf16_f32 v154, v164, v165\n"
"v_cvt_pk_bf16_f32 v155, v166, v167\n"
"v_mfma_f32_16x16x32_bf16 v[188:191], v[224:227], v[136:139], v[188:191]\n"
"ds_read_b64_tr_b16 v[224:225], v250 offset:1024\n"
"ds_read_b64_tr_b16 v[226:227], v250 offset:5120\n"
"v_mfma_f32_16x16x32_bf16 v[156:159], v[244:247], v[144:147], 0\n"
"v_mfma_f32_16x16x32_bf16 v[160:163], v[244:247], v[148:151], 0\n"
"v_mfma_f32_16x16x32_bf16 v[164:167], v[244:247], v[152:155], 0\n"
"s_waitcnt lgkmcnt(12)\n"
"v_mfma_f32_16x16x32_bf16 v[168:171], v[228:231], v[108:111], v[168:171]\n"
"v_mfma_f32_16x16x32_bf16 v[172:175], v[228:231], v[124:127], v[172:175]\n"
"v_mfma_f32_16x16x32_bf16 v[176:179], v[228:231], v[140:143], v[176:179]\n"
"ds_read_b64_tr_b16 v[228:229], v249 offset:1536\n"
"ds_read_b64_tr_b16 v[230:231], v249 offset:5632\n"
"v_mfma_f32_16x16x32_bf16 v[180:183], v[232:235], v[108:111], v[180:183]\n"
"s_nop 0\n"
"v_max3_f32 v242, v156, v160, v164\n"
"v_mfma_f32_16x16x32_bf16 v[184:187], v[232:235], v[124:127], v[184:187]\n"
"v_cmp_nge_f32 vcc, 0x46000000, v242\n"
"v_mfma_f32_16x16x32_bf16 v[188:191], v[232:235], v[140:143], v[188:191]\n"
"ds_read_b64_tr_b16 v[232:233], v250 offset:1536\n"
"ds_read_b64_tr_b16 v[234:235], v250 offset:5632\n"
"s_cbranch_vccnz .LrAy41\n"
".LrAby41:\n"
"s_waitcnt lgkmcnt(12)\n"
"v_mfma_f32_16x16x32_bf16 v[0:3], v[204:207], v[144:147], v[0:3]\n"
"v_exp_f32 v168, v168\n"
"v_mfma_f32_16x16x32_bf16 v[4:7], v[204:207], v[148:151], v[4:7]\n"
"v_exp_f32 v169, v169\n"
"v_mfma_f32_16x16x32_bf16 v[8:11], v[204:207], v[152:155], v[8:11]\n"
"v_add_f32 v236, v236, v156\n"
"v_mfma_f32_16x16x32_bf16 v[12:15], v[208:211], v[144:147], v[12:15]\n"
"v_exp_f32 v170, v170\n"
"v_mfma_f32_16x16x32_bf16 v[16:19], v[208:211], v[148:151], v[16:19]\n"
"v_exp_f32 v171, v171\n"
"v_mfma_f32_16x16x32_bf16 v[20:23], v[208:211], v[152:155], v[20:23]\n"
"v_add_f32 v237, v237, v160\n"
"s_waitcnt lgkmcnt(8)\n"
"v_mfma_f32_16x16x32_bf16 v[24:27], v[212:215], v[144:147], v[24:27]\n"
"v_exp_f32 v180, v180\n"
"v_mfma_f32_16x16x32_bf16 v[28:31], v[212:215], v[148:151], v[28:31]\n"
"v_exp_f32 v181, v181\n"
"v_mfma_f32_16x16x32_bf16 v[32:35], v[212:215], v[152:155], v[32:35]\n"
"v_add_f32 v238, v238, v164\n"
"v_mfma_f32_16x16x32_bf16 v[36:39], v[216:219], v[144:147], v[36:39]\n"
"v_exp_f32 v182, v182\n"
"v_mfma_f32_16x16x32_bf16 v[40:43], v[216:219], v[148:151], v[40:43]\n"
"v_exp_f32 v183, v183\n"
"v_mfma_f32_16x16x32_bf16 v[44:47], v[216:219], v[152:155], v[44:47]\n"
"v_cvt_pk_bf16_f32 v168, v168, v169\n"
"s_waitcnt lgkmcnt(4)\n"
"v_mfma_f32_16x16x32_bf16 v[48:51], v[220:223], v[144:147], v[48:51]\n"
"v_exp_f32 v172, v172\n"
"v_mfma_f32_16x16x32_bf16 v[52:55], v[220:223], v[148:151], v[52:55]\n"
"v_exp_f32 v173, v173\n"
"v_mfma_f32_16x16x32_bf16 v[56:59], v[220:223], v[152:155], v[56:59]\n"
"v_cvt_pk_bf16_f32 v169, v170, v171\n"
"v_mfma_f32_16x16x32_bf16 v[60:63], v[224:227], v[144:147], v[60:63]\n"
"v_exp_f32 v174, v174\n"
"v_mfma_f32_16x16x32_bf16 v[64:67], v[224:227], v[148:151], v[64:67]\n"
"v_exp_f32 v175, v175\n"
"v_mfma_f32_16x16x32_bf16 v[68:71], v[224:227], v[152:155], v[68:71]\n"
"v_cvt_pk_bf16_f32 v170, v180, v181\n"
"s_waitcnt lgkmcnt(0)\n"
"v_mfma_f32_16x16x32_bf16 v[72:75], v[228:231], v[144:147], v[72:75]\n"
"s_add_u32 s69, s69, 1\n"
"s_sub_u32 s68, s68, 1\n"
"s_cselect_b32 s70, -40960, s75\n"
"s_cselect_b32 s68, 5, s68\n"
"v_exp_f32 v184, v184\n"
"v_mfma_f32_16x16x32_bf16 v[76:79], v[228:231], v[148:151], v[76:79]\n"
"v_exp_f32 v185, v185\n"
"v_mfma_f32_16x16x32_bf16 v[80:83], v[228:231], v[152:155], v[80:83]\n"
"v_cvt_pk_bf16_f32 v171, v182, v183\n"
"v_mfma_f32_16x16x32_bf16 v[84:87], v[232:235], v[144:147], v[84:87]\n"
"v_exp_f32 v186, v186\n"
"v_mfma_f32_16x16x32_bf16 v[88:91], v[232:235], v[148:151], v[88:91]\n"
"v_exp_f32 v187, v187\n"
"v_mfma_f32_16x16x32_bf16 v[92:95], v[232:235], v[152:155], v[92:95]\n"
"v_add_u32 v249, s70, v249\n"
"v_add_u32 v250, s70, v250\n"
"v_add_u32 v248, s70, v248\n"
"s_barrier\n"
"; step 31\n"
"ds_read_b64_tr_b16 v[204:205], v249 offset:0\n"
"ds_read_b64_tr_b16 v[206:207], v249 offset:4096\n"
"ds_read_b64_tr_b16 v[208:209], v250 offset:0\n"
"ds_read_b64_tr_b16 v[210:211], v250 offset:4096\n"
"ds_read_b64_tr_b16 v[212:213], v249 offset:512\n"
"ds_read_b64_tr_b16 v[214:215], v249 offset:4608\n"
"ds_read_b64_tr_b16 v[216:217], v250 offset:512\n"
"ds_read_b64_tr_b16 v[218:219], v250 offset:4608\n"
"ds_read_b64_tr_b16 v[220:221], v249 offset:1024\n"
"ds_read_b64_tr_b16 v[222:223], v249 offset:5120\n"
"ds_read_b64_tr_b16 v[224:225], v250 offset:1024\n"
"ds_read_b64_tr_b16 v[226:227], v250 offset:5120\n"
"ds_read_b64_tr_b16 v[228:229], v249 offset:1536\n"
"ds_read_b64_tr_b16 v[230:231], v249 offset:5632\n"
"ds_read_b64_tr_b16 v[232:233], v250 offset:1536\n"
"ds_read_b64_tr_b16 v[234:235], v250 offset:5632\n"
"v_exp_f32 v176, v176\n"
"v_cvt_pk_bf16_f32 v172, v172, v173\n"
"v_exp_f32 v177, v177\n"
"v_cvt_pk_bf16_f32 v173, v174, v175\n"
"v_exp_f32 v178, v178\n"
"v_cvt_pk_bf16_f32 v174, v184, v185\n"
"v_exp_f32 v179, v179\n"
"v_cvt_pk_bf16_f32 v175, v186, v187\n"
"v_exp_f32 v188, v188\n"
"v_exp_f32 v189, v189\n"
"v_exp_f32 v190, v190\n"
"v_exp_f32 v191, v191\n"
"v_cvt_pk_bf16_f32 v176, v176, v177\n"
"v_cvt_pk_bf16_f32 v177, v178, v179\n"
"v_cvt_pk_bf16_f32 v178, v188, v189\n"
"v_cvt_pk_bf16_f32 v179, v190, v191\n"
"v_mfma_f32_16x16x32_bf16 v[180:183], v[244:247], v[168:171], 0\n"
"v_mfma_f32_16x16x32_bf16 v[184:187], v[244:247], v[172:175], 0\n"
"v_mfma_f32_16x16x32_bf16 v[188:191], v[244:247], v[176:179], 0\n"
"s_nop 7\n"
"v_max3_f32 v242, v180, v184, v188\n"
"v_cmp_nge_f32 vcc, 0x46000000, v242\n"
"s_cbranch_vccnz .LrAy42\n"
".LrAby42:\n"
"v_add_f32 v236, v236, v180\n"
"v_add_f32 v237, v237, v184\n"
"v_add_f32 v238, v238, v188\n"
"s_waitcnt lgkmcnt(14)\n"
"v_mfma_f32_16x16x32_bf16 v[0:3], v[204:207], v[168:171], v[0:3]\n"
"s_waitcnt lgkmcnt(12)\n"
"v_mfma_f32_16x16x32_bf16 v[12:15], v[208:211], v[168:171], v[12:15]\n"
"v_rcp_f32 v239, v236\n"
"s_waitcnt lgkmcnt(10)\n"
"v_mfma_f32_16x16x32_bf16 v[24:27], v[212:215], v[168:171], v[24:27]\n"
"s_waitcnt lgkmcnt(8)\n"
"v_mfma_f32_16x16x32_bf16 v[36:39], v[216:219], v[168:171], v[36:39]\n"
"v_rcp_f32 v240, v237\n"
"s_waitcnt lgkmcnt(6)\n"
"v_mfma_f32_16x16x32_bf16 v[48:51], v[220:223], v[168:171], v[48:51]\n"
"s_waitcnt lgkmcnt(4)\n"
"v_mfma_f32_16x16x32_bf16 v[60:63], v[224:227], v[168:171], v[60:63]\n"
"s_waitcnt lgkmcnt(2)\n"
"v_mfma_f32_16x16x32_bf16 v[72:75], v[228:231], v[168:171], v[72:75]\n"
"v_rcp_f32 v241, v238\n"
"s_waitcnt lgkmcnt(0)\n"
"v_mfma_f32_16x16x32_bf16 v[84:87], v[232:235], v[168:171], v[84:87]\n"
"v_mfma_f32_16x16x32_bf16 v[4:7], v[204:207], v[172:175], v[4:7]\n"
"v_mfma_f32_16x16x32_bf16 v[16:19], v[208:211], v[172:175], v[16:19]\n"
"s_add_u32 s74, s73, 0\n"
"v_mul_f32 v0, v239, v0\n"
"v_mul_f32 v1, v239, v1\n"
"v_mfma_f32_16x16x32_bf16 v[28:31], v[212:215], v[172:175], v[28:31]\n"
"v_mul_f32 v2, v239, v2\n"
"v_mul_f32 v3, v239, v3\n"
"v_mfma_f32_16x16x32_bf16 v[40:43], v[216:219], v[172:175], v[40:43]\n"
"v_mul_f32 v12, v239, v12\n"
"v_mul_f32 v13, v239, v13\n"
"v_mul_f32 v14, v239, v14\n"
"v_mfma_f32_16x16x32_bf16 v[52:55], v[220:223], v[172:175], v[52:55]\n"
"v_mul_f32 v15, v239, v15\n"
"v_cvt_pk_bf16_f32 v96, v0, v1\n"
"v_mfma_f32_16x16x32_bf16 v[64:67], v[224:227], v[172:175], v[64:67]\n"
"v_cvt_pk_bf16_f32 v97, v2, v3\n"
"v_cvt_pk_bf16_f32 v98, v12, v13\n"
"v_mfma_f32_16x16x32_bf16 v[76:79], v[228:231], v[172:175], v[76:79]\n"
"v_cvt_pk_bf16_f32 v99, v14, v15\n"
"buffer_store_dwordx4 v[96:99], v253, s[44:47], s74 offen sc1\n"
"v_mfma_f32_16x16x32_bf16 v[88:91], v[232:235], v[172:175], v[88:91]\n"
"v_mul_f32 v24, v239, v24\n"
"v_mul_f32 v25, v239, v25\n"
"v_mfma_f32_16x16x32_bf16 v[8:11], v[204:207], v[176:179], v[8:11]\n"
"v_mul_f32 v26, v239, v26\n"
"v_mul_f32 v27, v239, v27\n"
"v_mfma_f32_16x16x32_bf16 v[20:23], v[208:211], v[176:179], v[20:23]\n"
"v_mul_f32 v36, v239, v36\n"
"v_mul_f32 v37, v239, v37\n"
"v_mfma_f32_16x16x32_bf16 v[32:35], v[212:215], v[176:179], v[32:35]\n"
"v_mul_f32 v38, v239, v38\n"
"v_mul_f32 v39, v239, v39\n"
"v_mfma_f32_16x16x32_bf16 v[44:47], v[216:219], v[176:179], v[44:47]\n"
"v_cvt_pk_bf16_f32 v100, v24, v25\n"
"v_cvt_pk_bf16_f32 v101, v26, v27\n"
"v_cvt_pk_bf16_f32 v102, v36, v37\n"
"v_mfma_f32_16x16x32_bf16 v[56:59], v[220:223], v[176:179], v[56:59]\n"
"v_cvt_pk_bf16_f32 v103, v38, v39\n"
"v_mfma_f32_16x16x32_bf16 v[68:71], v[224:227], v[176:179], v[68:71]\n"
"buffer_store_dwordx4 v[100:103], v253, s[44:47], s74 offen offset:1024 sc1\n"
"v_mul_f32 v48, v239, v48\n"
"v_mfma_f32_16x16x32_bf16 v[80:83], v[228:231], v[176:179], v[80:83]\n"
"v_mul_f32 v49, v239, v49\n"
"v_mul_f32 v50, v239, v50\n"
"v_mfma_f32_16x16x32_bf16 v[92:95], v[232:235], v[176:179], v[92:95]\n"
"v_mul_f32 v51, v239, v51\n"
"v_mul_f32 v60, v239, v60\n"
"s_barrier\n"
"; step 32\n"
"s_setprio 1\n"
"v_mul_f32 v61, v239, v61\n"
"v_mul_f32 v62, v239, v62\n"
"v_mul_f32 v63, v239, v63\n"
"v_cvt_pk_bf16_f32 v104, v48, v49\n"
"v_cvt_pk_bf16_f32 v105, v50, v51\n"
"v_cvt_pk_bf16_f32 v106, v60, v61\n"
"v_cvt_pk_bf16_f32 v107, v62, v63\n"
"buffer_store_dwordx4 v[104:107], v253, s[44:47], s74 offen offset:2048 sc1\n"
"v_mul_f32 v72, v239, v72\n"
"v_mul_f32 v73, v239, v73\n"
"v_mul_f32 v74, v239, v74\n"
"v_mul_f32 v75, v239, v75\n"
"v_mul_f32 v84, v239, v84\n"
"v_mul_f32 v85, v239, v85\n"
"v_mul_f32 v86, v239, v86\n"
"v_mul_f32 v87, v239, v87\n"
"v_cvt_pk_bf16_f32 v108, v72, v73\n"
"v_cvt_pk_bf16_f32 v109, v74, v75\n"
"v_cvt_pk_bf16_f32 v110, v84, v85\n"
"v_cvt_pk_bf16_f32 v111, v86, v87\n"
"buffer_store_dwordx4 v[108:111], v253, s[44:47], s74 offen offset:3072 sc1\n"
"s_add_u32 s74, s73, 4096\n"
"v_mul_f32 v4, v240, v4\n"
"v_mul_f32 v5, v240, v5\n"
"v_mul_f32 v6, v240, v6\n"
"v_mul_f32 v7, v240, v7\n"
"v_mul_f32 v16, v240, v16\n"
"v_mul_f32 v17, v240, v17\n"
"v_mul_f32 v18, v240, v18\n"
"v_mul_f32 v19, v240, v19\n"
"v_cvt_pk_bf16_f32 v112, v4, v5\n"
"v_cvt_pk_bf16_f32 v113, v6, v7\n"
"v_cvt_pk_bf16_f32 v114, v16, v17\n"
"v_cvt_pk_bf16_f32 v115, v18, v19\n"
"buffer_store_dwordx4 v[112:115], v253, s[44:47], s74 offen sc1\n"
"v_mul_f32 v28, v240, v28\n"
"v_mul_f32 v29, v240, v29\n"
"v_mul_f32 v30, v240, v30\n"
"v_mul_f32 v31, v240, v31\n"
"v_mul_f32 v40, v240, v40\n"
"v_mul_f32 v41, v240, v41\n"
"v_mul_f32 v42, v240, v42\n"
"v_mul_f32 v43, v240, v43\n"
"v_cvt_pk_bf16_f32 v116, v28, v29\n"
"v_cvt_pk_bf16_f32 v117, v30, v31\n"
"v_cvt_pk_bf16_f32 v118, v40, v41\n"
"v_cvt_pk_bf16_f32 v119, v42, v43\n"
"buffer_store_dwordx4 v[116:119], v253, s[44:47], s74 offen offset:1024 sc1\n"
"v_mul_f32 v52, v240, v52\n"
"v_mul_f32 v53, v240, v53\n"
"v_mul_f32 v54, v240, v54\n"
"v_mul_f32 v55, v240, v55\n"
"v_mul_f32 v64, v240, v64\n"
"v_mul_f32 v65, v240, v65\n"
"v_mul_f32 v66, v240, v66\n"
"v_mul_f32 v67, v240, v67\n"
"v_cvt_pk_bf16_f32 v120, v52, v53\n"
"v_cvt_pk_bf16_f32 v121, v54, v55\n"
"v_cvt_pk_bf16_f32 v122, v64, v65\n"
"v_cvt_pk_bf16_f32 v123, v66, v67\n"
"buffer_store_dwordx4 v[120:123], v253, s[44:47], s74 offen offset:2048 sc1\n"
"v_mul_f32 v76, v240, v76\n"
"v_mul_f32 v77, v240, v77\n"
"v_mul_f32 v78, v240, v78\n"
"v_mul_f32 v79, v240, v79\n"
"v_mul_f32 v88, v240, v88\n"
"v_mul_f32 v89, v240, v89\n"
"v_mul_f32 v90, v240, v90\n"
"v_mul_f32 v91, v240, v91\n"
"v_cvt_pk_bf16_f32 v124, v76, v77\n"
"v_cvt_pk_bf16_f32 v125, v78, v79\n"
"v_cvt_pk_bf16_f32 v126, v88, v89\n"
"v_cvt_pk_bf16_f32 v127, v90, v91\n"
"buffer_store_dwordx4 v[124:127], v253, s[44:47], s74 offen offset:3072 sc1\n"
"s_add_u32 s74, s73, 8192\n"
"v_mul_f32 v8, v241, v8\n"
"v_mul_f32 v9, v241, v9\n"
"v_mul_f32 v10, v241, v10\n"
"v_mul_f32 v11, v241, v11\n"
"v_mul_f32 v20, v241, v20\n"
"v_mul_f32 v21, v241, v21\n"
"v_mul_f32 v22, v241, v22\n"
"v_mul_f32 v23, v241, v23\n"
"v_cvt_pk_bf16_f32 v128, v8, v9\n"
"v_cvt_pk_bf16_f32 v129, v10, v11\n"
"v_cvt_pk_bf16_f32 v130, v20, v21\n"
"v_cvt_pk_bf16_f32 v131, v22, v23\n"
"buffer_store_dwordx4 v[128:131], v253, s[44:47], s74 offen sc1\n"
"v_mul_f32 v32, v241, v32\n"
"v_mul_f32 v33, v241, v33\n"
"v_mul_f32 v34, v241, v34\n"
"v_mul_f32 v35, v241, v35\n"
"v_mul_f32 v44, v241, v44\n"
"v_mul_f32 v45, v241, v45\n"
"v_mul_f32 v46, v241, v46\n"
"v_mul_f32 v47, v241, v47\n"
"v_cvt_pk_bf16_f32 v132, v32, v33\n"
"v_cvt_pk_bf16_f32 v133, v34, v35\n"
"v_cvt_pk_bf16_f32 v134, v44, v45\n"
"v_cvt_pk_bf16_f32 v135, v46, v47\n"
"buffer_store_dwordx4 v[132:135], v253, s[44:47], s74 offen offset:1024 sc1\n"
"v_mul_f32 v56, v241, v56\n"
"v_mul_f32 v57, v241, v57\n"
"v_mul_f32 v58, v241, v58\n"
"v_mul_f32 v59, v241, v59\n"
"v_mul_f32 v68, v241, v68\n"
"v_mul_f32 v69, v241, v69\n"
"v_mul_f32 v70, v241, v70\n"
"v_mul_f32 v71, v241, v71\n"
"v_cvt_pk_bf16_f32 v136, v56, v57\n"
"v_cvt_pk_bf16_f32 v137, v58, v59\n"
"v_cvt_pk_bf16_f32 v138, v68, v69\n"
"v_cvt_pk_bf16_f32 v139, v70, v71\n"
"buffer_store_dwordx4 v[136:139], v253, s[44:47], s74 offen offset:2048 sc1\n"
"v_mul_f32 v80, v241, v80\n"
"v_mul_f32 v81, v241, v81\n"
"v_mul_f32 v82, v241, v82\n"
"v_mul_f32 v83, v241, v83\n"
"v_mul_f32 v92, v241, v92\n"
"v_mul_f32 v93, v241, v93\n"
"v_mul_f32 v94, v241, v94\n"
"v_mul_f32 v95, v241, v95\n"
"v_cvt_pk_bf16_f32 v140, v80, v81\n"
"v_cvt_pk_bf16_f32 v141, v82, v83\n"
"v_cvt_pk_bf16_f32 v142, v92, v93\n"
"v_cvt_pk_bf16_f32 v143, v94, v95\n"
"buffer_store_dwordx4 v[140:143], v253, s[44:47], s74 offen offset:3072 sc1\n"
"s_lshl_b32 s71, s58, 2\n"
"s_add_u32 s71, s71, s59\n"
"s_lshl_b32 s71, s71, 3\n"
"s_add_u32 s71, s71, s56\n"
"s_lshl_b32 s71, s71, 6\n"
"s_add_u32 s71, s71, 4352\n"
"s_lshl_b32 s70, s60, 3\n"
"s_add_u32 s71, s71, s70\n"
"v_mov_b32 v244, s80\n"
"v_mov_b32 v245, s81\n"
"v_cmp_eq_u32 vcc, 0, v253\n"
"v_mov_b32 v243, 2147483647\n"
"s_nop 0\n"
"v_cndmask_b32 v242, v243, v253, vcc\n"
"s_waitcnt vmcnt(0)\n"
"buffer_store_dwordx2 v[244:245], v242, s[48:51], s71 offen sc1\n"
"s_add_u32 s71, s67, 229376\n"
"s_add_u32 m0, s61, 40960\n"
"s_and_b32 s71, s71, 1048575\n"
"s_add_u32 s71, s71, s33\n"
"buffer_load_dwordx4 v255, s[40:43], s71 offen lds\n"
"s_add_u32 m0, s61, 41984\n"
"s_xor_b32 s71, s71, 128\n"
"buffer_load_dwordx4 v255, s[40:43], s71 offen lds\n"
"s_sleep 4\n"
"s_cmp_le_u32 s60, 0\n"
"s_cselect_b32 s71, 262144, 0\n"
"s_add_u32 s71, s71, 131072\n"
"s_add_u32 m0, s61, 49152\n"
"s_add_u32 s71, s71, s33\n"
"buffer_load_dwordx4 v255, s[40:43], s71 offen lds\n"
"s_add_u32 m0, s61, 50176\n"
"s_xor_b32 s71, s71, 128\n"
"buffer_load_dwordx4 v255, s[40:43], s71 offen lds\n"
"s_sleep 4\n"
"s_cmp_le_u32 s60, 0\n"
"s_cselect_b32 s71, 262144, 0\n"
"s_add_u32 s71, s71, 163840\n"
"s_add_u32 m0, s61, 114688\n"
"s_add_u32 s71, s71, s33\n"
"buffer_load_dwordx4 v255, s[40:43], s71 offen lds\n"
"s_add_u32 m0, s61, 115712\n"
"s_xor_b32 s71, s71, 128\n"
"buffer_load_dwordx4 v255, s[40:43], s71 offen lds\n"
"s_sleep 4\n"
"s_cmp_le_u32 s60, 0\n"
"s_cselect_b32 s71, 262144, 0\n"
"s_add_u32 s71, s71, 196608\n"
"s_add_u32 m0, s61, 57344\n"
"s_add_u32 s71, s71, s33\n"
"buffer_load_dwordx4 v255, s[40:43], s71 offen lds\n"
"s_add_u32 m0, s61, 58368\n"
"s_xor_b32 s71, s71, 128\n"
"buffer_load_dwordx4 v255, s[40:43], s71 offen lds\n"
"s_sleep 4\n"
"s_cmp_le_u32 s60, 0\n"
"s_cselect_b32 s71, 262144, 0\n"
"s_add_u32 s71, s71, 229376\n"
"s_add_u32 m0, s61, 122880\n"
"s_add_u32 s71, s71, s33\n"
"buffer_load_dwordx4 v255, s[40:43], s71 offen lds\n"
"s_add_u32 m0, s61, 123904\n"
"s_xor_b32 s71, s71, 128\n"
"buffer_load_dwordx4 v255, s[40:43], s71 offen lds\n"
"s_sleep 4\n"
"s_barrier\n"
"; step 33\n"
"s_cmp_le_u32 s60, 1\n"
"s_cselect_b32 s71, 262144, 0\n"
"s_add_u32 s71, s71, 393216\n"
"s_add_u32 m0, s61, 0\n"
"s_add_u32 s71, s71, s33\n"
"buffer_load_dwordx4 v255, s[40:43], s71 offen lds\n"
"s_add_u32 m0, s61, 1024\n"
"s_xor_b32 s71, s71, 128\n"
"buffer_load_dwordx4 v255, s[40:43], s71 offen lds\n"
"s_sleep 4\n"
"s_cmp_le_u32 s60, 1\n"
"s_cselect_b32 s71, 262144, 0\n"
"s_add_u32 s71, s71, 425984\n"
"s_add_u32 m0, s61, 8192\n"
"s_add_u32 s71, s71, s33\n"
"buffer_load_dwordx4 v255, s[40:43], s71 offen lds\n"
"s_add_u32 m0, s61, 9216\n"
"s_xor_b32 s71, s71, 128\n"
"buffer_load_dwordx4 v255, s[40:43], s71 offen lds\n"
"s_sleep 4\n"
"s_cmp_le_u32 s60, 1\n"
"s_cselect_b32 s71, 262144, 0\n"
"s_add_u32 s71, s71, 458752\n"
"s_add_u32 m0, s61, 16384\n"
"s_add_u32 s71, s71, s33\n"
"buffer_load_dwordx4 v255, s[40:43], s71 offen lds\n"
"s_add_u32 m0, s61, 17408\n"
"s_xor_b32 s71, s71, 128\n"
"buffer_load_dwordx4 v255, s[40:43], s71 offen lds\n"
"s_sleep 4\n"
"s_cmp_le_u32 s60, 1\n"
"s_cselect_b32 s71, 262144, 0\n"
"s_add_u32 s71, s71, 491520\n"
"s_add_u32 m0, s61, 65536\n"
"s_add_u32 s71, s71, s33\n"
"buffer_load_dwordx4 v255, s[40:43], s71 offen lds\n"
"s_add_u32 m0, s61, 66560\n"
"s_xor_b32 s71, s71, 128\n"
"buffer_load_dwordx4 v255, s[40:43], s71 offen lds\n"
"s_sleep 4\n"
"s_cmp_le_u32 s60, 2\n"
"s_cselect_b32 s71, 262144, 0\n"
"s_add_u32 s71, s71, 655360\n"
"s_add_u32 m0, s61, 131072\n"
"s_add_u32 s71, s71, s33\n"
"buffer_load_dwordx4 v255, s[40:43], s71 offen lds\n"
"s_add_u32 m0, s61, 132096\n"
"s_xor_b32 s71, s71, 128\n"
"buffer_load_dwordx4 v255, s[40:43], s71 offen lds\n"
"s_sleep 4\n"
"s_waitcnt vmcnt(16)\n"
"s_barrier\n"
"; step 34\n"
"s_cmp_le_u32 s60, 2\n"
"s_cselect_b32 s71, 262144, 0\n"
"s_add_u32 s71, s71, 688128\n"
"s_add_u32 m0, s61, 139264\n"
"s_add_u32 s71, s71, s33\n"
"buffer_load_dwordx4 v255, s[40:43], s71 offen lds\n"
"s_add_u32 m0, s61, 140288\n"
"s_xor_b32 s71, s71, 128\n"
"buffer_load_dwordx4 v255, s[40:43], s71 offen lds\n"
"s_sleep 4\n"
"s_cmp_le_u32 s60, 2\n"
"s_cselect_b32 s71, 262144, 0\n"
"s_add_u32 s71, s71, 720896\n"
"s_add_u32 m0, s61, 147456\n"
"s_add_u32 s71, s71, s33\n"
"buffer_load_dwordx4 v255, s[40:43], s71 offen lds\n"
"s_add_u32 m0, s61, 148480\n"
"s_xor_b32 s71, s71, 128\n"
"buffer_load_dwordx4 v255, s[40:43], s71 offen lds\n"
"s_sleep 4\n"
"s_cmp_le_u32 s60, 2\n"
"s_cselect_b32 s71, 262144, 0\n"
"s_add_u32 s71, s71, 753664\n"
"s_add_u32 m0, s61, 155648\n"
"s_add_u32 s71, s71, s33\n"
"buffer_load_dwordx4 v255, s[40:43], s71 offen lds\n"
"s_add_u32 m0, s61, 156672\n"
"s_xor_b32 s71, s71, 128\n"
"buffer_load_dwordx4 v255, s[40:43], s71 offen lds\n"
"s_sleep 4\n"
"s_waitcnt vmcnt(14)\n"
"s_barrier\n"
"; step 35\n"
"s_waitcnt vmcnt(6)\n"
"s_barrier\n"
"; step 36\n"
"s_waitcnt vmcnt(0)\n"
"s_barrier\n"
"s_branch .Lend\n"
".LrAx20:\n"
"s_nop 7\n"
"s_nop 7\n"
"v_bfe_u32 v243, v253, 4, 3\n"
"v_lshlrev_b32 v243, 8, v243\n"
"v_bfe_u32 v242, v253, 7, 1\n"
"v_lshl_add_u32 v243, v242, 11, v243\n"
"v_lshrrev_b32 v242, 8, v253\n"
"v_lshl_add_u32 v243, v242, 4, v243\n"
"s_and_b32 s70, s69, 15\n"
"s_lshr_b32 s71, s70, 0\n"
"s_lshl_b32 s71, s71, 2\n"
"s_and_b32 s70, s70, 0\n"
"s_or_b32 s71, s71, s70\n"
"s_lshl_b32 s71, s71, 11\n"
"s_lshr_b32 s70, s69, 4\n"
"s_lshl_b32 s70, s70, 17\n"
"s_add_u32 s71, s71, s70\n"
"s_add_u32 s70, s71, 0\n"
"buffer_load_dwordx4 v[244:247], v243, s[20:23], s70 offen offset:0\n"
"s_waitcnt vmcnt(0)\n"
"v_mfma_f32_16x16x32_bf16 v[168:171], v[244:247], v[96:99], v[192:195]\n"
"v_mfma_f32_16x16x32_bf16 v[172:175], v[244:247], v[112:115], v[196:199]\n"
"v_mfma_f32_16x16x32_bf16 v[176:179], v[244:247], v[128:131], v[200:203]\n"
"s_nop 7\n"
"buffer_load_dwordx4 v[244:247], v243, s[20:23], s70 offen offset:64\n"
"s_waitcnt vmcnt(0)\n"
"v_mfma_f32_16x16x32_bf16 v[168:171], v[244:247], v[100:103], v[168:171]\n"
"v_mfma_f32_16x16x32_bf16 v[172:175], v[244:247], v[116:119], v[172:175]\n"
"v_mfma_f32_16x16x32_bf16 v[176:179], v[244:247], v[132:135], v[176:179]\n"
"s_nop 7\n"
"buffer_load_dwordx4 v[244:247], v243, s[20:23], s70 offen offset:128\n"
"s_waitcnt vmcnt(0)\n"
"v_mfma_f32_16x16x32_bf16 v[168:171], v[244:247], v[104:107], v[168:171]\n"
"v_mfma_f32_16x16x32_bf16 v[172:175], v[244:247], v[120:123], v[172:175]\n"
"v_mfma_f32_16x16x32_bf16 v[176:179], v[244:247], v[136:139], v[176:179]\n"
"s_nop 7\n"
"buffer_load_dwordx4 v[244:247], v243, s[20:23], s70 offen offset:192\n"
"s_waitcnt vmcnt(0)\n"
"v_mfma_f32_16x16x32_bf16 v[168:171], v[244:247], v[108:111], v[168:171]\n"
"v_mfma_f32_16x16x32_bf16 v[172:175], v[244:247], v[124:127], v[172:175]\n"
"v_mfma_f32_16x16x32_bf16 v[176:179], v[244:247], v[140:143], v[176:179]\n"
"s_nop 7\n"
"s_add_u32 s70, s71, 4096\n"
"buffer_load_dwordx4 v[244:247], v243, s[20:23], s70 offen offset:0\n"
"s_waitcnt vmcnt(0)\n"
"v_mfma_f32_16x16x32_bf16 v[180:183], v[244:247], v[96:99], v[192:195]\n"
"v_mfma_f32_16x16x32_bf16 v[184:187], v[244:247], v[112:115], v[196:199]\n"
"v_mfma_f32_16x16x32_bf16 v[188:191], v[244:247], v[128:131], v[200:203]\n"
"s_nop 7\n"
"buffer_load_dwordx4 v[244:247], v243, s[20:23], s70 offen offset:64\n"
"s_waitcnt vmcnt(0)\n"
"v_mfma_f32_16x16x32_bf16 v[180:183], v[244:247], v[100:103], v[180:183]\n"
"v_mfma_f32_16x16x32_bf16 v[184:187], v[244:247], v[116:119], v[184:187]\n"
"v_mfma_f32_16x16x32_bf16 v[188:191], v[244:247], v[132:135], v[188:191]\n"
"s_nop 7\n"
"buffer_load_dwordx4 v[244:247], v243, s[20:23], s70 offen offset:128\n"
"s_waitcnt vmcnt(0)\n"
"v_mfma_f32_16x16x32_bf16 v[180:183], v[244:247], v[104:107], v[180:183]\n"
"v_mfma_f32_16x16x32_bf16 v[184:187], v[244:247], v[120:123], v[184:187]\n"
"v_mfma_f32_16x16x32_bf16 v[188:191], v[244:247], v[136:139], v[188:191]\n"
"s_nop 7\n"
"buffer_load_dwordx4 v[244:247], v243, s[20:23], s70 offen offset:192\n"
"s_waitcnt vmcnt(0)\n"
"v_mfma_f32_16x16x32_bf16 v[180:183], v[244:247], v[108:111], v[180:183]\n"
"v_mfma_f32_16x16x32_bf16 v[184:187], v[244:247], v[124:127], v[184:187]\n"
"v_mfma_f32_16x16x32_bf16 v[188:191], v[244:247], v[140:143], v[188:191]\n"
"s_nop 7\n"
"s_nop 7\n"
"s_nop 7\n"
"v_max3_f32 v242, v168, v169, v170\n"
"v_max3_f32 v242, v242, v171, v180\n"
"v_max3_f32 v242, v242, v181, v182\n"
"v_max_f32 v242, v242, v183\n"
"v_mov_b32 v243, v242\n"
"s_nop 1\n"
"v_permlane32_swap_b32 v242, v243\n"
"v_max_f32 v242, v242, v243\n"
"v_mov_b32 v243, v242\n"
"s_nop 1\n"
"v_permlane16_swap_b32 v242, v243\n"
"v_max_f32 v242, v242, v243\n"
"v_max_f32 v242, 0, v242\n"
"v_sub_f32 v168, v168, v242\n"
"v_sub_f32 v169, v169, v242\n"
"v_sub_f32 v170, v170, v242\n"
"v_sub_f32 v171, v171, v242\n"
"v_sub_f32 v180, v180, v242\n"
"v_sub_f32 v181, v181, v242\n"
"v_sub_f32 v182, v182, v242\n"
"v_sub_f32 v183, v183, v242\n"
"v_sub_f32 v192, v192, v242\n"
"v_sub_f32 v193, v193, v242\n"
"v_sub_f32 v194, v194, v242\n"
"v_sub_f32 v195, v195, v242\n"
"v_sub_f32 v144, v144, v242\n"
"v_sub_f32 v145, v145, v242\n"
"v_sub_f32 v146, v146, v242\n"
"v_sub_f32 v147, v147, v242\n"
"v_sub_f32 v156, v156, v242\n"
"v_sub_f32 v157, v157, v242\n"
"v_sub_f32 v158, v158, v242\n"
"v_sub_f32 v159, v159, v242\n"
"v_exp_f32 v243, -v242\n"
"s_nop 1\n"
"v_mul_f32 v236, v236, v243\n"
"v_mul_f32 v0, v0, v243\n"
"v_mul_f32 v1, v1, v243\n"
"v_mul_f32 v2, v2, v243\n"
"v_mul_f32 v3, v3, v243\n"
"v_mul_f32 v12, v12, v243\n"
"v_mul_f32 v13, v13, v243\n"
"v_mul_f32 v14, v14, v243\n"
"v_mul_f32 v15, v15, v243\n"
"v_mul_f32 v24, v24, v243\n"
"v_mul_f32 v25, v25, v243\n"
"v_mul_f32 v26, v26, v243\n"
"v_mul_f32 v27, v27, v243\n"
"v_mul_f32 v36, v36, v243\n"
"v_mul_f32 v37, v37, v243\n"
"v_mul_f32 v38, v38, v243\n"
"v_mul_f32 v39, v39, v243\n"
"v_mul_f32 v48, v48, v243\n"
"v_mul_f32 v49, v49, v243\n"
"v_mul_f32 v50, v50, v243\n"
"v_mul_f32 v51, v51, v243\n"
"v_mul_f32 v60, v60, v243\n"
"v_mul_f32 v61, v61, v243\n"
"v_mul_f32 v62, v62, v243\n"
"v_mul_f32 v63, v63, v243\n"
"v_mul_f32 v72, v72, v243\n"
"v_mul_f32 v73, v73, v243\n"
"v_mul_f32 v74, v74, v243\n"
"v_mul_f32 v75, v75, v243\n"
"v_mul_f32 v84, v84, v243\n"
"v_mul_f32 v85, v85, v243\n"
"v_mul_f32 v86, v86, v243\n"
"v_mul_f32 v87, v87, v243\n"
"v_exp_f32 v168, v168\n"
"v_exp_f32 v169, v169\n"
"v_exp_f32 v170, v170\n"
"v_exp_f32 v171, v171\n"
"v_exp_f32 v180, v180\n"
"v_exp_f32 v181, v181\n"
"v_exp_f32 v182, v182\n"
"v_exp_f32 v183, v183\n"
"s_nop 1\n"
"v_cvt_pk_bf16_f32 v168, v168, v169\n"
"v_cvt_pk_bf16_f32 v169, v170, v171\n"
"v_cvt_pk_bf16_f32 v170, v180, v181\n"
"v_cvt_pk_bf16_f32 v171, v182, v183\n"
"v_max3_f32 v242, v172, v173, v174\n"
"v_max3_f32 v242, v242, v175, v184\n"
"v_max3_f32 v242, v242, v185, v186\n"
"v_max_f32 v242, v242, v187\n"
"v_mov_b32 v243, v242\n"
"s_nop 1\n"
"v_permlane32_swap_b32 v242, v243\n"
"v_max_f32 v242, v242, v243\n"
"v_mov_b32 v243, v242\n"
"s_nop 1\n"
"v_permlane16_swap_b32 v242, v243\n"
"v_max_f32 v242, v242, v243\n"
"v_max_f32 v242, 0, v242\n"
"v_sub_f32 v172, v172, v242\n"
"v_sub_f32 v173, v173, v242\n"
"v_sub_f32 v174, v174, v242\n"
"v_sub_f32 v175, v175, v242\n"
"v_sub_f32 v184, v184, v242\n"
"v_sub_f32 v185, v185, v242\n"
"v_sub_f32 v186, v186, v242\n"
"v_sub_f32 v187, v187, v242\n"
"v_sub_f32 v196, v196, v242\n"
"v_sub_f32 v197, v197, v242\n"
"v_sub_f32 v198, v198, v242\n"
"v_sub_f32 v199, v199, v242\n"
"v_sub_f32 v148, v148, v242\n"
"v_sub_f32 v149, v149, v242\n"
"v_sub_f32 v150, v150, v242\n"
"v_sub_f32 v151, v151, v242\n"
"v_sub_f32 v160, v160, v242\n"
"v_sub_f32 v161, v161, v242\n"
"v_sub_f32 v162, v162, v242\n"
"v_sub_f32 v163, v163, v242\n"
"v_exp_f32 v243, -v242\n"
"s_nop 1\n"
"v_mul_f32 v237, v237, v243\n"
"v_mul_f32 v4, v4, v243\n"
"v_mul_f32 v5, v5, v243\n"
"v_mul_f32 v6, v6, v243\n"
"v_mul_f32 v7, v7, v243\n"
"v_mul_f32 v16, v16, v243\n"
"v_mul_f32 v17, v17, v243\n"
"v_mul_f32 v18, v18, v243\n"
"v_mul_f32 v19, v19, v243\n"
"v_mul_f32 v28, v28, v243\n"
"v_mul_f32 v29, v29, v243\n"
"v_mul_f32 v30, v30, v243\n"
"v_mul_f32 v31, v31, v243\n"
"v_mul_f32 v40, v40, v243\n"
"v_mul_f32 v41, v41, v243\n"
"v_mul_f32 v42, v42, v243\n"
"v_mul_f32 v43, v43, v243\n"
"v_mul_f32 v52, v52, v243\n"
"v_mul_f32 v53, v53, v243\n"
"v_mul_f32 v54, v54, v243\n"
"v_mul_f32 v55, v55, v243\n"
"v_mul_f32 v64, v64, v243\n"
"v_mul_f32 v65, v65, v243\n"
"v_mul_f32 v66, v66, v243\n"
"v_mul_f32 v67, v67, v243\n"
"v_mul_f32 v76, v76, v243\n"
"v_mul_f32 v77, v77, v243\n"
"v_mul_f32 v78, v78, v243\n"
"v_mul_f32 v79, v79, v243\n"
"v_mul_f32 v88, v88, v243\n"
"v_mul_f32 v89, v89, v243\n"
"v_mul_f32 v90, v90, v243\n"
"v_mul_f32 v91, v91, v243\n"
"v_exp_f32 v172, v172\n"
"v_exp_f32 v173, v173\n"
"v_exp_f32 v174, v174\n"
"v_exp_f32 v175, v175\n"
"v_exp_f32 v184, v184\n"
"v_exp_f32 v185, v185\n"
"v_exp_f32 v186, v186\n"
"v_exp_f32 v187, v187\n"
"s_nop 1\n"
"v_cvt_pk_bf16_f32 v172, v172, v173\n"
"v_cvt_pk_bf16_f32 v173, v174, v175\n"
"v_cvt_pk_bf16_f32 v174, v184, v185\n"
"v_cvt_pk_bf16_f32 v175, v186, v187\n"
"v_max3_f32 v242, v176, v177, v178\n"
"v_max3_f32 v242, v242, v179, v188\n"
"v_max3_f32 v242, v242, v189, v190\n"
"v_max_f32 v242, v242, v191\n"
"v_mov_b32 v243, v242\n"
"s_nop 1\n"
"v_permlane32_swap_b32 v242, v243\n"
"v_max_f32 v242, v242, v243\n"
"v_mov_b32 v243, v242\n"
"s_nop 1\n"
"v_permlane16_swap_b32 v242, v243\n"
"v_max_f32 v242, v242, v243\n"
"v_max_f32 v242, 0, v242\n"
"v_sub_f32 v176, v176, v242\n"
"v_sub_f32 v177, v177, v242\n"
"v_sub_f32 v178, v178, v242\n"
"v_sub_f32 v179, v179, v242\n"
"v_sub_f32 v188, v188, v242\n"
"v_sub_f32 v189, v189, v242\n"
"v_sub_f32 v190, v190, v242\n"
"v_sub_f32 v191, v191, v242\n"
"v_sub_f32 v200, v200, v242\n"
"v_sub_f32 v201, v201, v242\n"
"v_sub_f32 v202, v202, v242\n"
"v_sub_f32 v203, v203, v242\n"
"v_sub_f32 v152, v152, v242\n"
"v_sub_f32 v153, v153, v242\n"
"v_sub_f32 v154, v154, v242\n"
"v_sub_f32 v155, v155, v242\n"
"v_sub_f32 v164, v164, v242\n"
"v_sub_f32 v165, v165, v242\n"
"v_sub_f32 v166, v166, v242\n"
"v_sub_f32 v167, v167, v242\n"
"v_exp_f32 v243, -v242\n"
"s_nop 1\n"
"v_mul_f32 v238, v238, v243\n"
"v_mul_f32 v8, v8, v243\n"
"v_mul_f32 v9, v9, v243\n"
"v_mul_f32 v10, v10, v243\n"
"v_mul_f32 v11, v11, v243\n"
"v_mul_f32 v20, v20, v243\n"
"v_mul_f32 v21, v21, v243\n"
"v_mul_f32 v22, v22, v243\n"
"v_mul_f32 v23, v23, v243\n"
"v_mul_f32 v32, v32, v243\n"
"v_mul_f32 v33, v33, v243\n"
"v_mul_f32 v34, v34, v243\n"
"v_mul_f32 v35, v35, v243\n"
"v_mul_f32 v44, v44, v243\n"
"v_mul_f32 v45, v45, v243\n"
"v_mul_f32 v46, v46, v243\n"
"v_mul_f32 v47, v47, v243\n"
"v_mul_f32 v56, v56, v243\n"
"v_mul_f32 v57, v57, v243\n"
"v_mul_f32 v58, v58, v243\n"
"v_mul_f32 v59, v59, v243\n"
"v_mul_f32 v68, v68, v243\n"
"v_mul_f32 v69, v69, v243\n"
"v_mul_f32 v70, v70, v243\n"
"v_mul_f32 v71, v71, v243\n"
"v_mul_f32 v80, v80, v243\n"
"v_mul_f32 v81, v81, v243\n"
"v_mul_f32 v82, v82, v243\n"
"v_mul_f32 v83, v83, v243\n"
"v_mul_f32 v92, v92, v243\n"
"v_mul_f32 v93, v93, v243\n"
"v_mul_f32 v94, v94, v243\n"
"v_mul_f32 v95, v95, v243\n"
"v_exp_f32 v176, v176\n"
"v_exp_f32 v177, v177\n"
"v_exp_f32 v178, v178\n"
"v_exp_f32 v179, v179\n"
"v_exp_f32 v188, v188\n"
"v_exp_f32 v189, v189\n"
"v_exp_f32 v190, v190\n"
"v_exp_f32 v191, v191\n"
"s_nop 1\n"
"v_cvt_pk_bf16_f32 v176, v176, v177\n"
"v_cvt_pk_bf16_f32 v177, v178, v179\n"
"v_cvt_pk_bf16_f32 v178, v188, v189\n"
"v_cvt_pk_bf16_f32 v179, v190, v191\n"
"v_mov_b32 v244, 0x3f803f80\n"
"v_mov_b32 v245, 0x3f803f80\n"
"v_mov_b32 v246, 0x3f803f80\n"
"v_mov_b32 v247, 0x3f803f80\n"
"s_nop 1\n"
"v_mfma_f32_16x16x32_bf16 v[180:183], v[244:247], v[168:171], 0\n"
"v_mfma_f32_16x16x32_bf16 v[184:187], v[244:247], v[172:175], 0\n"
"v_mfma_f32_16x16x32_bf16 v[188:191], v[244:247], v[176:179], 0\n"
"s_nop 7\n"
"s_nop 7\n"
"s_nop 3\n"
"s_branch .LrAbx20\n"
".LrAx21:\n"
"s_nop 7\n"
"s_nop 7\n"
"v_bfe_u32 v243, v253, 4, 3\n"
"v_lshlrev_b32 v243, 8, v243\n"
"v_bfe_u32 v242, v253, 7, 1\n"
"v_lshl_add_u32 v243, v242, 11, v243\n"
"v_lshrrev_b32 v242, 8, v253\n"
"v_lshl_add_u32 v243, v242, 4, v243\n"
"s_and_b32 s70, s69, 15\n"
"s_lshr_b32 s71, s70, 0\n"
"s_lshl_b32 s71, s71, 2\n"
"s_and_b32 s70, s70, 0\n"
"s_or_b32 s71, s71, s70\n"
"s_lshl_b32 s71, s71, 11\n"
"s_lshr_b32 s70, s69, 4\n"
"s_lshl_b32 s70, s70, 17\n"
"s_add_u32 s71, s71, s70\n"
"s_add_u32 s70, s71, 0\n"
"buffer_load_dwordx4 v[244:247], v243, s[20:23], s70 offen offset:0\n"
"s_waitcnt vmcnt(0)\n"
"v_mfma_f32_16x16x32_bf16 v[144:147], v[244:247], v[96:99], v[192:195]\n"
"v_mfma_f32_16x16x32_bf16 v[148:151], v[244:247], v[112:115], v[196:199]\n"
"v_mfma_f32_16x16x32_bf16 v[152:155], v[244:247], v[128:131], v[200:203]\n"
"s_nop 7\n"
"buffer_load_dwordx4 v[244:247], v243, s[20:23], s70 offen offset:64\n"
"s_waitcnt vmcnt(0)\n"
"v_mfma_f32_16x16x32_bf16 v[144:147], v[244:247], v[100:103], v[144:147]\n"
"v_mfma_f32_16x16x32_bf16 v[148:151], v[244:247], v[116:119], v[148:151]\n"
"v_mfma_f32_16x16x32_bf16 v[152:155], v[244:247], v[132:135], v[152:155]\n"
"s_nop 7\n"
"buffer_load_dwordx4 v[244:247], v243, s[20:23], s70 offen offset:128\n"
"s_waitcnt vmcnt(0)\n"
"v_mfma_f32_16x16x32_bf16 v[144:147], v[244:247], v[104:107], v[144:147]\n"
"v_mfma_f32_16x16x32_bf16 v[148:151], v[244:247], v[120:123], v[148:151]\n"
"v_mfma_f32_16x16x32_bf16 v[152:155], v[244:247], v[136:139], v[152:155]\n"
"s_nop 7\n"
"buffer_load_dwordx4 v[244:247], v243, s[20:23], s70 offen offset:192\n"
"s_waitcnt vmcnt(0)\n"
"v_mfma_f32_16x16x32_bf16 v[144:147], v[244:247], v[108:111], v[144:147]\n"
"v_mfma_f32_16x16x32_bf16 v[148:151], v[244:247], v[124:127], v[148:151]\n"
"v_mfma_f32_16x16x32_bf16 v[152:155], v[244:247], v[140:143], v[152:155]\n"
"s_nop 7\n"
"s_add_u32 s70, s71, 4096\n"
"buffer_load_dwordx4 v[244:247], v243, s[20:23], s70 offen offset:0\n"
"s_waitcnt vmcnt(0)\n"
"v_mfma_f32_16x16x32_bf16 v[156:159], v[244:247], v[96:99], v[192:195]\n"
"v_mfma_f32_16x16x32_bf16 v[160:163], v[244:247], v[112:115], v[196:199]\n"
"v_mfma_f32_16x16x32_bf16 v[164:167], v[244:247], v[128:131], v[200:203]\n"
"s_nop 7\n"
"buffer_load_dwordx4 v[244:247], v243, s[20:23], s70 offen offset:64\n"
"s_waitcnt vmcnt(0)\n"
"v_mfma_f32_16x16x32_bf16 v[156:159], v[244:247], v[100:103], v[156:159]\n"
"v_mfma_f32_16x16x32_bf16 v[160:163], v[244:247], v[116:119], v[160:163]\n"
"v_mfma_f32_16x16x32_bf16 v[164:167], v[244:247], v[132:135], v[164:167]\n"
"s_nop 7\n"
"buffer_load_dwordx4 v[244:247], v243, s[20:23], s70 offen offset:128\n"
"s_waitcnt vmcnt(0)\n"
"v_mfma_f32_16x16x32_bf16 v[156:159], v[244:247], v[104:107], v[156:159]\n"
"v_mfma_f32_16x16x32_bf16 v[160:163], v[244:247], v[120:123], v[160:163]\n"
"v_mfma_f32_16x16x32_bf16 v[164:167], v[244:247], v[136:139], v[164:167]\n"
"s_nop 7\n"
"buffer_load_dwordx4 v[244:247], v243, s[20:23], s70 offen offset:192\n"
"s_waitcnt vmcnt(0)\n"
"v_mfma_f32_16x16x32_bf16 v[156:159], v[244:247], v[108:111], v[156:159]\n"
"v_mfma_f32_16x16x32_bf16 v[160:163], v[244:247], v[124:127], v[160:163]\n"
"v_mfma_f32_16x16x32_bf16 v[164:167], v[244:247], v[140:143], v[164:167]\n"
"s_nop 7\n"
"s_nop 7\n"
"s_nop 7\n"
"v_max3_f32 v242, v144, v145, v146\n"
"v_max3_f32 v242, v242, v147, v156\n"
"v_max3_f32 v242, v242, v157, v158\n"
"v_max_f32 v242, v242, v159\n"
"v_mov_b32 v243, v242\n"
"s_nop 1\n"
"v_permlane32_swap_b32 v242, v243\n"
"v_max_f32 v242, v242, v243\n"
"v_mov_b32 v243, v242\n"
"s_nop 1\n"
"v_permlane16_swap_b32 v242, v243\n"
"v_max_f32 v242, v242, v243\n"
"v_max_f32 v242, 0, v242\n"
"v_sub_f32 v144, v144, v242\n"
"v_sub_f32 v145, v145, v242\n"
"v_sub_f32 v146, v146, v242\n"
"v_sub_f32 v147, v147, v242\n"
"v_sub_f32 v156, v156, v242\n"
"v_sub_f32 v157, v157, v242\n"
"v_sub_f32 v158, v158, v242\n"
"v_sub_f32 v159, v159, v242\n"
"v_sub_f32 v192, v192, v242\n"
"v_sub_f32 v193, v193, v242\n"
"v_sub_f32 v194, v194, v242\n"
"v_sub_f32 v195, v195, v242\n"
"v_sub_f32 v168, v168, v242\n"
"v_sub_f32 v169, v169, v242\n"
"v_sub_f32 v170, v170, v242\n"
"v_sub_f32 v171, v171, v242\n"
"v_sub_f32 v180, v180, v242\n"
"v_sub_f32 v181, v181, v242\n"
"v_sub_f32 v182, v182, v242\n"
"v_sub_f32 v183, v183, v242\n"
"v_exp_f32 v243, -v242\n"
"s_nop 1\n"
"v_mul_f32 v236, v236, v243\n"
"v_mul_f32 v0, v0, v243\n"
"v_mul_f32 v1, v1, v243\n"
"v_mul_f32 v2, v2, v243\n"
"v_mul_f32 v3, v3, v243\n"
"v_mul_f32 v12, v12, v243\n"
"v_mul_f32 v13, v13, v243\n"
"v_mul_f32 v14, v14, v243\n"
"v_mul_f32 v15, v15, v243\n"
"v_mul_f32 v24, v24, v243\n"
"v_mul_f32 v25, v25, v243\n"
"v_mul_f32 v26, v26, v243\n"
"v_mul_f32 v27, v27, v243\n"
"v_mul_f32 v36, v36, v243\n"
"v_mul_f32 v37, v37, v243\n"
"v_mul_f32 v38, v38, v243\n"
"v_mul_f32 v39, v39, v243\n"
"v_mul_f32 v48, v48, v243\n"
"v_mul_f32 v49, v49, v243\n"
"v_mul_f32 v50, v50, v243\n"
"v_mul_f32 v51, v51, v243\n"
"v_mul_f32 v60, v60, v243\n"
"v_mul_f32 v61, v61, v243\n"
"v_mul_f32 v62, v62, v243\n"
"v_mul_f32 v63, v63, v243\n"
"v_mul_f32 v72, v72, v243\n"
"v_mul_f32 v73, v73, v243\n"
"v_mul_f32 v74, v74, v243\n"
"v_mul_f32 v75, v75, v243\n"
"v_mul_f32 v84, v84, v243\n"
"v_mul_f32 v85, v85, v243\n"
"v_mul_f32 v86, v86, v243\n"
"v_mul_f32 v87, v87, v243\n"
"v_exp_f32 v144, v144\n"
"v_exp_f32 v145, v145\n"
"v_exp_f32 v146, v146\n"
"v_exp_f32 v147, v147\n"
"v_exp_f32 v156, v156\n"
"v_exp_f32 v157, v157\n"
"v_exp_f32 v158, v158\n"
"v_exp_f32 v159, v159\n"
"s_nop 1\n"
"v_cvt_pk_bf16_f32 v144, v144, v145\n"
"v_cvt_pk_bf16_f32 v145, v146, v147\n"
"v_cvt_pk_bf16_f32 v146, v156, v157\n"
"v_cvt_pk_bf16_f32 v147, v158, v159\n"
"v_max3_f32 v242, v148, v149, v150\n"
"v_max3_f32 v242, v242, v151, v160\n"
"v_max3_f32 v242, v242, v161, v162\n"
"v_max_f32 v242, v242, v163\n"
"v_mov_b32 v243, v242\n"
"s_nop 1\n"
"v_permlane32_swap_b32 v242, v243\n"
"v_max_f32 v242, v242, v243\n"
"v_mov_b32 v243, v242\n"
"s_nop 1\n"
"v_permlane16_swap_b32 v242, v243\n"
"v_max_f32 v242, v242, v243\n"
"v_max_f32 v242, 0, v242\n"
"v_sub_f32 v148, v148, v242\n"
"v_sub_f32 v149, v149, v242\n"
"v_sub_f32 v150, v150, v242\n"
"v_sub_f32 v151, v151, v242\n"
"v_sub_f32 v160, v160, v242\n"
"v_sub_f32 v161, v161, v242\n"
"v_sub_f32 v162, v162, v242\n"
"v_sub_f32 v163, v163, v242\n"
"v_sub_f32 v196, v196, v242\n"
"v_sub_f32 v197, v197, v242\n"
"v_sub_f32 v198, v198, v242\n"
"v_sub_f32 v199, v199, v242\n"
"v_sub_f32 v172, v172, v242\n"
"v_sub_f32 v173, v173, v242\n"
"v_sub_f32 v174, v174, v242\n"
"v_sub_f32 v175, v175, v242\n"
"v_sub_f32 v184, v184, v242\n"
"v_sub_f32 v185, v185, v242\n"
"v_sub_f32 v186, v186, v242\n"
"v_sub_f32 v187, v187, v242\n"
"v_exp_f32 v243, -v242\n"
"s_nop 1\n"
"v_mul_f32 v237, v237, v243\n"
"v_mul_f32 v4, v4, v243\n"
"v_mul_f32 v5, v5, v243\n"
"v_mul_f32 v6, v6, v243\n"
"v_mul_f32 v7, v7, v243\n"
"v_mul_f32 v16, v16, v243\n"
"v_mul_f32 v17, v17, v243\n"
"v_mul_f32 v18, v18, v243\n"
"v_mul_f32 v19, v19, v243\n"
"v_mul_f32 v28, v28, v243\n"
"v_mul_f32 v29, v29, v243\n"
"v_mul_f32 v30, v30, v243\n"
"v_mul_f32 v31, v31, v243\n"
"v_mul_f32 v40, v40, v243\n"
"v_mul_f32 v41, v41, v243\n"
"v_mul_f32 v42, v42, v243\n"
"v_mul_f32 v43, v43, v243\n"
"v_mul_f32 v52, v52, v243\n"
"v_mul_f32 v53, v53, v243\n"
"v_mul_f32 v54, v54, v243\n"
"v_mul_f32 v55, v55, v243\n"
"v_mul_f32 v64, v64, v243\n"
"v_mul_f32 v65, v65, v243\n"
"v_mul_f32 v66, v66, v243\n"
"v_mul_f32 v67, v67, v243\n"
"v_mul_f32 v76, v76, v243\n"
"v_mul_f32 v77, v77, v243\n"
"v_mul_f32 v78, v78, v243\n"
"v_mul_f32 v79, v79, v243\n"
"v_mul_f32 v88, v88, v243\n"
"v_mul_f32 v89, v89, v243\n"
"v_mul_f32 v90, v90, v243\n"
"v_mul_f32 v91, v91, v243\n"
"v_exp_f32 v148, v148\n"
"v_exp_f32 v149, v149\n"
"v_exp_f32 v150, v150\n"
"v_exp_f32 v151, v151\n"
"v_exp_f32 v160, v160\n"
"v_exp_f32 v161, v161\n"
"v_exp_f32 v162, v162\n"
"v_exp_f32 v163, v163\n"
"s_nop 1\n"
"v_cvt_pk_bf16_f32 v148, v148, v149\n"
"v_cvt_pk_bf16_f32 v149, v150, v151\n"
"v_cvt_pk_bf16_f32 v150, v160, v161\n"
"v_cvt_pk_bf16_f32 v151, v162, v163\n"
"v_max3_f32 v242, v152, v153, v154\n"
"v_max3_f32 v242, v242, v155, v164\n"
"v_max3_f32 v242, v242, v165, v166\n"
"v_max_f32 v242, v242, v167\n"
"v_mov_b32 v243, v242\n"
"s_nop 1\n"
"v_permlane32_swap_b32 v242, v243\n"
"v_max_f32 v242, v242, v243\n"
"v_mov_b32 v243, v242\n"
"s_nop 1\n"
"v_permlane16_swap_b32 v242, v243\n"
"v_max_f32 v242, v242, v243\n"
"v_max_f32 v242, 0, v242\n"
"v_sub_f32 v152, v152, v242\n"
"v_sub_f32 v153, v153, v242\n"
"v_sub_f32 v154, v154, v242\n"
"v_sub_f32 v155, v155, v242\n"
"v_sub_f32 v164, v164, v242\n"
"v_sub_f32 v165, v165, v242\n"
"v_sub_f32 v166, v166, v242\n"
"v_sub_f32 v167, v167, v242\n"
"v_sub_f32 v200, v200, v242\n"
"v_sub_f32 v201, v201, v242\n"
"v_sub_f32 v202, v202, v242\n"
"v_sub_f32 v203, v203, v242\n"
"v_sub_f32 v176, v176, v242\n"
"v_sub_f32 v177, v177, v242\n"
"v_sub_f32 v178, v178, v242\n"
"v_sub_f32 v179, v179, v242\n"
"v_sub_f32 v188, v188, v242\n"
"v_sub_f32 v189, v189, v242\n"
"v_sub_f32 v190, v190, v242\n"
"v_sub_f32 v191, v191, v242\n"
"v_exp_f32 v243, -v242\n"
"s_nop 1\n"
"v_mul_f32 v238, v238, v243\n"
"v_mul_f32 v8, v8, v243\n"
"v_mul_f32 v9, v9, v243\n"
"v_mul_f32 v10, v10, v243\n"
"v_mul_f32 v11, v11, v243\n"
"v_mul_f32 v20, v20, v243\n"
"v_mul_f32 v21, v21, v243\n"
"v_mul_f32 v22, v22, v243\n"
"v_mul_f32 v23, v23, v243\n"
"v_mul_f32 v32, v32, v243\n"
"v_mul_f32 v33, v33, v243\n"
"v_mul_f32 v34, v34, v243\n"
"v_mul_f32 v35, v35, v243\n"
"v_mul_f32 v44, v44, v243\n"
"v_mul_f32 v45, v45, v243\n"
"v_mul_f32 v46, v46, v243\n"
"v_mul_f32 v47, v47, v243\n"
"v_mul_f32 v56, v56, v243\n"
"v_mul_f32 v57, v57, v243\n"
"v_mul_f32 v58, v58, v243\n"
"v_mul_f32 v59, v59, v243\n"
"v_mul_f32 v68, v68, v243\n"
"v_mul_f32 v69, v69, v243\n"
"v_mul_f32 v70, v70, v243\n"
"v_mul_f32 v71, v71, v243\n"
"v_mul_f32 v80, v80, v243\n"
"v_mul_f32 v81, v81, v243\n"
"v_mul_f32 v82, v82, v243\n"
"v_mul_f32 v83, v83, v243\n"
"v_mul_f32 v92, v92, v243\n"
"v_mul_f32 v93, v93, v243\n"
"v_mul_f32 v94, v94, v243\n"
"v_mul_f32 v95, v95, v243\n"
"v_exp_f32 v152, v152\n"
"v_exp_f32 v153, v153\n"
"v_exp_f32 v154, v154\n"
"v_exp_f32 v155, v155\n"
"v_exp_f32 v164, v164\n"
"v_exp_f32 v165, v165\n"
"v_exp_f32 v166, v166\n"
"v_exp_f32 v167, v167\n"
"s_nop 1\n"
"v_cvt_pk_bf16_f32 v152, v152, v153\n"
"v_cvt_pk_bf16_f32 v153, v154, v155\n"
"v_cvt_pk_bf16_f32 v154, v164, v165\n"
"v_cvt_pk_bf16_f32 v155, v166, v167\n"
"v_mov_b32 v244, 0x3f803f80\n"
"v_mov_b32 v245, 0x3f803f80\n"
"v_mov_b32 v246, 0x3f803f80\n"
"v_mov_b32 v247, 0x3f803f80\n"
"s_nop 1\n"
"v_mfma_f32_16x16x32_bf16 v[156:159], v[244:247], v[144:147], 0\n"
"v_mfma_f32_16x16x32_bf16 v[160:163], v[244:247], v[148:151], 0\n"
"v_mfma_f32_16x16x32_bf16 v[164:167], v[244:247], v[152:155], 0\n"
"s_nop 7\n"
"s_nop 7\n"
"s_nop 3\n"
"s_branch .LrAbx21\n"
".LrAx22:\n"
"s_nop 7\n"
"s_nop 7\n"
"v_bfe_u32 v243, v253, 4, 3\n"
"v_lshlrev_b32 v243, 8, v243\n"
"v_bfe_u32 v242, v253, 7, 1\n"
"v_lshl_add_u32 v243, v242, 11, v243\n"
"v_lshrrev_b32 v242, 8, v253\n"
"v_lshl_add_u32 v243, v242, 4, v243\n"
"s_and_b32 s70, s69, 15\n"
"s_lshr_b32 s71, s70, 0\n"
"s_lshl_b32 s71, s71, 2\n"
"s_and_b32 s70, s70, 0\n"
"s_or_b32 s71, s71, s70\n"
"s_lshl_b32 s71, s71, 11\n"
"s_lshr_b32 s70, s69, 4\n"
"s_lshl_b32 s70, s70, 17\n"
"s_add_u32 s71, s71, s70\n"
"s_add_u32 s70, s71, 0\n"
"buffer_load_dwordx4 v[244:247], v243, s[20:23], s70 offen offset:0\n"
"s_waitcnt vmcnt(0)\n"
"v_mfma_f32_16x16x32_bf16 v[168:171], v[244:247], v[96:99], v[192:195]\n"
"v_mfma_f32_16x16x32_bf16 v[172:175], v[244:247], v[112:115], v[196:199]\n"
"v_mfma_f32_16x16x32_bf16 v[176:179], v[244:247], v[128:131], v[200:203]\n"
"s_nop 7\n"
"buffer_load_dwordx4 v[244:247], v243, s[20:23], s70 offen offset:64\n"
"s_waitcnt vmcnt(0)\n"
"v_mfma_f32_16x16x32_bf16 v[168:171], v[244:247], v[100:103], v[168:171]\n"
"v_mfma_f32_16x16x32_bf16 v[172:175], v[244:247], v[116:119], v[172:175]\n"
"v_mfma_f32_16x16x32_bf16 v[176:179], v[244:247], v[132:135], v[176:179]\n"
"s_nop 7\n"
"buffer_load_dwordx4 v[244:247], v243, s[20:23], s70 offen offset:128\n"
"s_waitcnt vmcnt(0)\n"
"v_mfma_f32_16x16x32_bf16 v[168:171], v[244:247], v[104:107], v[168:171]\n"
"v_mfma_f32_16x16x32_bf16 v[172:175], v[244:247], v[120:123], v[172:175]\n"
"v_mfma_f32_16x16x32_bf16 v[176:179], v[244:247], v[136:139], v[176:179]\n"
"s_nop 7\n"
"buffer_load_dwordx4 v[244:247], v243, s[20:23], s70 offen offset:192\n"
"s_waitcnt vmcnt(0)\n"
"v_mfma_f32_16x16x32_bf16 v[168:171], v[244:247], v[108:111], v[168:171]\n"
"v_mfma_f32_16x16x32_bf16 v[172:175], v[244:247], v[124:127], v[172:175]\n"
"v_mfma_f32_16x16x32_bf16 v[176:179], v[244:247], v[140:143], v[176:179]\n"
"s_nop 7\n"
"s_add_u32 s70, s71, 4096\n"
"buffer_load_dwordx4 v[244:247], v243, s[20:23], s70 offen offset:0\n"
"s_waitcnt vmcnt(0)\n"
"v_mfma_f32_16x16x32_bf16 v[180:183], v[244:247], v[96:99], v[192:195]\n"
"v_mfma_f32_16x16x32_bf16 v[184:187], v[244:247], v[112:115], v[196:199]\n"
"v_mfma_f32_16x16x32_bf16 v[188:191], v[244:247], v[128:131], v[200:203]\n"
"s_nop 7\n"
"buffer_load_dwordx4 v[244:247], v243, s[20:23], s70 offen offset:64\n"
"s_waitcnt vmcnt(0)\n"
"v_mfma_f32_16x16x32_bf16 v[180:183], v[244:247], v[100:103], v[180:183]\n"
"v_mfma_f32_16x16x32_bf16 v[184:187], v[244:247], v[116:119], v[184:187]\n"
"v_mfma_f32_16x16x32_bf16 v[188:191], v[244:247], v[132:135], v[188:191]\n"
"s_nop 7\n"
"buffer_load_dwordx4 v[244:247], v243, s[20:23], s70 offen offset:128\n"
"s_waitcnt vmcnt(0)\n"
"v_mfma_f32_16x16x32_bf16 v[180:183], v[244:247], v[104:107], v[180:183]\n"
"v_mfma_f32_16x16x32_bf16 v[184:187], v[244:247], v[120:123], v[184:187]\n"
"v_mfma_f32_16x16x32_bf16 v[188:191], v[244:247], v[136:139], v[188:191]\n"
"s_nop 7\n"
"buffer_load_dwordx4 v[244:247], v243, s[20:23], s70 offen offset:192\n"
"s_waitcnt vmcnt(0)\n"
"v_mfma_f32_16x16x32_bf16 v[180:183], v[244:247], v[108:111], v[180:183]\n"
"v_mfma_f32_16x16x32_bf16 v[184:187], v[244:247], v[124:127], v[184:187]\n"
"v_mfma_f32_16x16x32_bf16 v[188:191], v[244:247], v[140:143], v[188:191]\n"
"s_nop 7\n"
"s_nop 7\n"
"s_nop 7\n"
"v_max3_f32 v242, v168, v169, v170\n"
"v_max3_f32 v242, v242, v171, v180\n"
"v_max3_f32 v242, v242, v181, v182\n"
"v_max_f32 v242, v242, v183\n"
"v_mov_b32 v243, v242\n"
"s_nop 1\n"
"v_permlane32_swap_b32 v242, v243\n"
"v_max_f32 v242, v242, v243\n"
"v_mov_b32 v243, v242\n"
"s_nop 1\n"
"v_permlane16_swap_b32 v242, v243\n"
"v_max_f32 v242, v242, v243\n"
"v_max_f32 v242, 0, v242\n"
"v_sub_f32 v168, v168, v242\n"
"v_sub_f32 v169, v169, v242\n"
"v_sub_f32 v170, v170, v242\n"
"v_sub_f32 v171, v171, v242\n"
"v_sub_f32 v180, v180, v242\n"
"v_sub_f32 v181, v181, v242\n"
"v_sub_f32 v182, v182, v242\n"
"v_sub_f32 v183, v183, v242\n"
"v_sub_f32 v192, v192, v242\n"
"v_sub_f32 v193, v193, v242\n"
"v_sub_f32 v194, v194, v242\n"
"v_sub_f32 v195, v195, v242\n"
"v_sub_f32 v144, v144, v242\n"
"v_sub_f32 v145, v145, v242\n"
"v_sub_f32 v146, v146, v242\n"
"v_sub_f32 v147, v147, v242\n"
"v_sub_f32 v156, v156, v242\n"
"v_sub_f32 v157, v157, v242\n"
"v_sub_f32 v158, v158, v242\n"
"v_sub_f32 v159, v159, v242\n"
"v_exp_f32 v243, -v242\n"
"s_nop 1\n"
"v_mul_f32 v236, v236, v243\n"
"v_mul_f32 v0, v0, v243\n"
"v_mul_f32 v1, v1, v243\n"
"v_mul_f32 v2, v2, v243\n"
"v_mul_f32 v3, v3, v243\n"
"v_mul_f32 v12, v12, v243\n"
"v_mul_f32 v13, v13, v243\n"
"v_mul_f32 v14, v14, v243\n"
"v_mul_f32 v15, v15, v243\n"
"v_mul_f32 v24, v24, v243\n"
"v_mul_f32 v25, v25, v243\n"
"v_mul_f32 v26, v26, v243\n"
"v_mul_f32 v27, v27, v243\n"
"v_mul_f32 v36, v36, v243\n"
"v_mul_f32 v37, v37, v243\n"
"v_mul_f32 v38, v38, v243\n"
"v_mul_f32 v39, v39, v243\n"
"v_mul_f32 v48, v48, v243\n"
"v_mul_f32 v49, v49, v243\n"
"v_mul_f32 v50, v50, v243\n"
"v_mul_f32 v51, v51, v243\n"
"v_mul_f32 v60, v60, v243\n"
"v_mul_f32 v61, v61, v243\n"
"v_mul_f32 v62, v62, v243\n"
"v_mul_f32 v63, v63, v243\n"
"v_mul_f32 v72, v72, v243\n"
"v_mul_f32 v73, v73, v243\n"
"v_mul_f32 v74, v74, v243\n"
"v_mul_f32 v75, v75, v243\n"
"v_mul_f32 v84, v84, v243\n"
"v_mul_f32 v85, v85, v243\n"
"v_mul_f32 v86, v86, v243\n"
"v_mul_f32 v87, v87, v243\n"
"v_exp_f32 v168, v168\n"
"v_exp_f32 v169, v169\n"
"v_exp_f32 v170, v170\n"
"v_exp_f32 v171, v171\n"
"v_exp_f32 v180, v180\n"
"v_exp_f32 v181, v181\n"
"v_exp_f32 v182, v182\n"
"v_exp_f32 v183, v183\n"
"s_nop 1\n"
"v_cvt_pk_bf16_f32 v168, v168, v169\n"
"v_cvt_pk_bf16_f32 v169, v170, v171\n"
"v_cvt_pk_bf16_f32 v170, v180, v181\n"
"v_cvt_pk_bf16_f32 v171, v182, v183\n"
"v_max3_f32 v242, v172, v173, v174\n"
"v_max3_f32 v242, v242, v175, v184\n"
"v_max3_f32 v242, v242, v185, v186\n"
"v_max_f32 v242, v242, v187\n"
"v_mov_b32 v243, v242\n"
"s_nop 1\n"
"v_permlane32_swap_b32 v242, v243\n"
"v_max_f32 v242, v242, v243\n"
"v_mov_b32 v243, v242\n"
"s_nop 1\n"
"v_permlane16_swap_b32 v242, v243\n"
"v_max_f32 v242, v242, v243\n"
"v_max_f32 v242, 0, v242\n"
"v_sub_f32 v172, v172, v242\n"
"v_sub_f32 v173, v173, v242\n"
"v_sub_f32 v174, v174, v242\n"
"v_sub_f32 v175, v175, v242\n"
"v_sub_f32 v184, v184, v242\n"
"v_sub_f32 v185, v185, v242\n"
"v_sub_f32 v186, v186, v242\n"
"v_sub_f32 v187, v187, v242\n"
"v_sub_f32 v196, v196, v242\n"
"v_sub_f32 v197, v197, v242\n"
"v_sub_f32 v198, v198, v242\n"
"v_sub_f32 v199, v199, v242\n"
"v_sub_f32 v148, v148, v242\n"
"v_sub_f32 v149, v149, v242\n"
"v_sub_f32 v150, v150, v242\n"
"v_sub_f32 v151, v151, v242\n"
"v_sub_f32 v160, v160, v242\n"
"v_sub_f32 v161, v161, v242\n"
"v_sub_f32 v162, v162, v242\n"
"v_sub_f32 v163, v163, v242\n"
"v_exp_f32 v243, -v242\n"
"s_nop 1\n"
"v_mul_f32 v237, v237, v243\n"
"v_mul_f32 v4, v4, v243\n"
"v_mul_f32 v5, v5, v243\n"
"v_mul_f32 v6, v6, v243\n"
"v_mul_f32 v7, v7, v243\n"
"v_mul_f32 v16, v16, v243\n"
"v_mul_f32 v17, v17, v243\n"
"v_mul_f32 v18, v18, v243\n"
"v_mul_f32 v19, v19, v243\n"
"v_mul_f32 v28, v28, v243\n"
"v_mul_f32 v29, v29, v243\n"
"v_mul_f32 v30, v30, v243\n"
"v_mul_f32 v31, v31, v243\n"
"v_mul_f32 v40, v40, v243\n"
"v_mul_f32 v41, v41, v243\n"
"v_mul_f32 v42, v42, v243\n"
"v_mul_f32 v43, v43, v243\n"
"v_mul_f32 v52, v52, v243\n"
"v_mul_f32 v53, v53, v243\n"
"v_mul_f32 v54, v54, v243\n"
"v_mul_f32 v55, v55, v243\n"
"v_mul_f32 v64, v64, v243\n"
"v_mul_f32 v65, v65, v243\n"
"v_mul_f32 v66, v66, v243\n"
"v_mul_f32 v67, v67, v243\n"
"v_mul_f32 v76, v76, v243\n"
"v_mul_f32 v77, v77, v243\n"
"v_mul_f32 v78, v78, v243\n"
"v_mul_f32 v79, v79, v243\n"
"v_mul_f32 v88, v88, v243\n"
"v_mul_f32 v89, v89, v243\n"
"v_mul_f32 v90, v90, v243\n"
"v_mul_f32 v91, v91, v243\n"
"v_exp_f32 v172, v172\n"
"v_exp_f32 v173, v173\n"
"v_exp_f32 v174, v174\n"
"v_exp_f32 v175, v175\n"
"v_exp_f32 v184, v184\n"
"v_exp_f32 v185, v185\n"
"v_exp_f32 v186, v186\n"
"v_exp_f32 v187, v187\n"
"s_nop 1\n"
"v_cvt_pk_bf16_f32 v172, v172, v173\n"
"v_cvt_pk_bf16_f32 v173, v174, v175\n"
"v_cvt_pk_bf16_f32 v174, v184, v185\n"
"v_cvt_pk_bf16_f32 v175, v186, v187\n"
"v_max3_f32 v242, v176, v177, v178\n"
"v_max3_f32 v242, v242, v179, v188\n"
"v_max3_f32 v242, v242, v189, v190\n"
"v_max_f32 v242, v242, v191\n"
"v_mov_b32 v243, v242\n"
"s_nop 1\n"
"v_permlane32_swap_b32 v242, v243\n"
"v_max_f32 v242, v242, v243\n"
"v_mov_b32 v243, v242\n"
"s_nop 1\n"
"v_permlane16_swap_b32 v242, v243\n"
"v_max_f32 v242, v242, v243\n"
"v_max_f32 v242, 0, v242\n"
"v_sub_f32 v176, v176, v242\n"
"v_sub_f32 v177, v177, v242\n"
"v_sub_f32 v178, v178, v242\n"
"v_sub_f32 v179, v179, v242\n"
"v_sub_f32 v188, v188, v242\n"
"v_sub_f32 v189, v189, v242\n"
"v_sub_f32 v190, v190, v242\n"
"v_sub_f32 v191, v191, v242\n"
"v_sub_f32 v200, v200, v242\n"
"v_sub_f32 v201, v201, v242\n"
"v_sub_f32 v202, v202, v242\n"
"v_sub_f32 v203, v203, v242\n"
"v_sub_f32 v152, v152, v242\n"
"v_sub_f32 v153, v153, v242\n"
"v_sub_f32 v154, v154, v242\n"
"v_sub_f32 v155, v155, v242\n"
"v_sub_f32 v164, v164, v242\n"
"v_sub_f32 v165, v165, v242\n"
"v_sub_f32 v166, v166, v242\n"
"v_sub_f32 v167, v167, v242\n"
"v_exp_f32 v243, -v242\n"
"s_nop 1\n"
"v_mul_f32 v238, v238, v243\n"
"v_mul_f32 v8, v8, v243\n"
"v_mul_f32 v9, v9, v243\n"
"v_mul_f32 v10, v10, v243\n"
"v_mul_f32 v11, v11, v243\n"
"v_mul_f32 v20, v20, v243\n"
"v_mul_f32 v21, v21, v243\n"
"v_mul_f32 v22, v22, v243\n"
"v_mul_f32 v23, v23, v243\n"
"v_mul_f32 v32, v32, v243\n"
"v_mul_f32 v33, v33, v243\n"
"v_mul_f32 v34, v34, v243\n"
"v_mul_f32 v35, v35, v243\n"
"v_mul_f32 v44, v44, v243\n"
"v_mul_f32 v45, v45, v243\n"
"v_mul_f32 v46, v46, v243\n"
"v_mul_f32 v47, v47, v243\n"
"v_mul_f32 v56, v56, v243\n"
"v_mul_f32 v57, v57, v243\n"
"v_mul_f32 v58, v58, v243\n"
"v_mul_f32 v59, v59, v243\n"
"v_mul_f32 v68, v68, v243\n"
"v_mul_f32 v69, v69, v243\n"
"v_mul_f32 v70, v70, v243\n"
"v_mul_f32 v71, v71, v243\n"
"v_mul_f32 v80, v80, v243\n"
"v_mul_f32 v81, v81, v243\n"
"v_mul_f32 v82, v82, v243\n"
"v_mul_f32 v83, v83, v243\n"
"v_mul_f32 v92, v92, v243\n"
"v_mul_f32 v93, v93, v243\n"
"v_mul_f32 v94, v94, v243\n"
"v_mul_f32 v95, v95, v243\n"
"v_exp_f32 v176, v176\n"
"v_exp_f32 v177, v177\n"
"v_exp_f32 v178, v178\n"
"v_exp_f32 v179, v179\n"
"v_exp_f32 v188, v188\n"
"v_exp_f32 v189, v189\n"
"v_exp_f32 v190, v190\n"
"v_exp_f32 v191, v191\n"
"s_nop 1\n"
"v_cvt_pk_bf16_f32 v176, v176, v177\n"
"v_cvt_pk_bf16_f32 v177, v178, v179\n"
"v_cvt_pk_bf16_f32 v178, v188, v189\n"
"v_cvt_pk_bf16_f32 v179, v190, v191\n"
"v_mov_b32 v244, 0x3f803f80\n"
"v_mov_b32 v245, 0x3f803f80\n"
"v_mov_b32 v246, 0x3f803f80\n"
"v_mov_b32 v247, 0x3f803f80\n"
"s_nop 1\n"
"v_mfma_f32_16x16x32_bf16 v[180:183], v[244:247], v[168:171], 0\n"
"v_mfma_f32_16x16x32_bf16 v[184:187], v[244:247], v[172:175], 0\n"
"v_mfma_f32_16x16x32_bf16 v[188:191], v[244:247], v[176:179], 0\n"
"s_nop 7\n"
"s_nop 7\n"
"s_nop 3\n"
"s_branch .LrAbx22\n"
".LrAx23:\n"
"s_nop 7\n"
"s_nop 7\n"
"v_bfe_u32 v243, v253, 4, 3\n"
"v_lshlrev_b32 v243, 8, v243\n"
"v_bfe_u32 v242, v253, 7, 1\n"
"v_lshl_add_u32 v243, v242, 11, v243\n"
"v_lshrrev_b32 v242, 8, v253\n"
"v_lshl_add_u32 v243, v242, 4, v243\n"
"s_and_b32 s70, s69, 15\n"
"s_lshr_b32 s71, s70, 0\n"
"s_lshl_b32 s71, s71, 2\n"
"s_and_b32 s70, s70, 0\n"
"s_or_b32 s71, s71, s70\n"
"s_lshl_b32 s71, s71, 11\n"
"s_lshr_b32 s70, s69, 4\n"
"s_lshl_b32 s70, s70, 17\n"
"s_add_u32 s71, s71, s70\n"
"s_add_u32 s70, s71, 0\n"
"buffer_load_dwordx4 v[244:247], v243, s[20:23], s70 offen offset:0\n"
"s_waitcnt vmcnt(0)\n"
"v_mfma_f32_16x16x32_bf16 v[144:147], v[244:247], v[96:99], v[192:195]\n"
"v_mfma_f32_16x16x32_bf16 v[148:151], v[244:247], v[112:115], v[196:199]\n"
"v_mfma_f32_16x16x32_bf16 v[152:155], v[244:247], v[128:131], v[200:203]\n"
"s_nop 7\n"
"buffer_load_dwordx4 v[244:247], v243, s[20:23], s70 offen offset:64\n"
"s_waitcnt vmcnt(0)\n"
"v_mfma_f32_16x16x32_bf16 v[144:147], v[244:247], v[100:103], v[144:147]\n"
"v_mfma_f32_16x16x32_bf16 v[148:151], v[244:247], v[116:119], v[148:151]\n"
"v_mfma_f32_16x16x32_bf16 v[152:155], v[244:247], v[132:135], v[152:155]\n"
"s_nop 7\n"
"buffer_load_dwordx4 v[244:247], v243, s[20:23], s70 offen offset:128\n"
"s_waitcnt vmcnt(0)\n"
"v_mfma_f32_16x16x32_bf16 v[144:147], v[244:247], v[104:107], v[144:147]\n"
"v_mfma_f32_16x16x32_bf16 v[148:151], v[244:247], v[120:123], v[148:151]\n"
"v_mfma_f32_16x16x32_bf16 v[152:155], v[244:247], v[136:139], v[152:155]\n"
"s_nop 7\n"
"buffer_load_dwordx4 v[244:247], v243, s[20:23], s70 offen offset:192\n"
"s_waitcnt vmcnt(0)\n"
"v_mfma_f32_16x16x32_bf16 v[144:147], v[244:247], v[108:111], v[144:147]\n"
"v_mfma_f32_16x16x32_bf16 v[148:151], v[244:247], v[124:127], v[148:151]\n"
"v_mfma_f32_16x16x32_bf16 v[152:155], v[244:247], v[140:143], v[152:155]\n"
"s_nop 7\n"
"s_add_u32 s70, s71, 4096\n"
"buffer_load_dwordx4 v[244:247], v243, s[20:23], s70 offen offset:0\n"
"s_waitcnt vmcnt(0)\n"
"v_mfma_f32_16x16x32_bf16 v[156:159], v[244:247], v[96:99], v[192:195]\n"
"v_mfma_f32_16x16x32_bf16 v[160:163], v[244:247], v[112:115], v[196:199]\n"
"v_mfma_f32_16x16x32_bf16 v[164:167], v[244:247], v[128:131], v[200:203]\n"
"s_nop 7\n"
"buffer_load_dwordx4 v[244:247], v243, s[20:23], s70 offen offset:64\n"
"s_waitcnt vmcnt(0)\n"
"v_mfma_f32_16x16x32_bf16 v[156:159], v[244:247], v[100:103], v[156:159]\n"
"v_mfma_f32_16x16x32_bf16 v[160:163], v[244:247], v[116:119], v[160:163]\n"
"v_mfma_f32_16x16x32_bf16 v[164:167], v[244:247], v[132:135], v[164:167]\n"
"s_nop 7\n"
"buffer_load_dwordx4 v[244:247], v243, s[20:23], s70 offen offset:128\n"
"s_waitcnt vmcnt(0)\n"
"v_mfma_f32_16x16x32_bf16 v[156:159], v[244:247], v[104:107], v[156:159]\n"
"v_mfma_f32_16x16x32_bf16 v[160:163], v[244:247], v[120:123], v[160:163]\n"
"v_mfma_f32_16x16x32_bf16 v[164:167], v[244:247], v[136:139], v[164:167]\n"
"s_nop 7\n"
"buffer_load_dwordx4 v[244:247], v243, s[20:23], s70 offen offset:192\n"
"s_waitcnt vmcnt(0)\n"
"v_mfma_f32_16x16x32_bf16 v[156:159], v[244:247], v[108:111], v[156:159]\n"
"v_mfma_f32_16x16x32_bf16 v[160:163], v[244:247], v[124:127], v[160:163]\n"
"v_mfma_f32_16x16x32_bf16 v[164:167], v[244:247], v[140:143], v[164:167]\n"
"s_nop 7\n"
"s_nop 7\n"
"s_nop 7\n"
"v_max3_f32 v242, v144, v145, v146\n"
"v_max3_f32 v242, v242, v147, v156\n"
"v_max3_f32 v242, v242, v157, v158\n"
"v_max_f32 v242, v242, v159\n"
"v_mov_b32 v243, v242\n"
"s_nop 1\n"
"v_permlane32_swap_b32 v242, v243\n"
"v_max_f32 v242, v242, v243\n"
"v_mov_b32 v243, v242\n"
"s_nop 1\n"
"v_permlane16_swap_b32 v242, v243\n"
"v_max_f32 v242, v242, v243\n"
"v_max_f32 v242, 0, v242\n"
"v_sub_f32 v144, v144, v242\n"
"v_sub_f32 v145, v145, v242\n"
"v_sub_f32 v146, v146, v242\n"
"v_sub_f32 v147, v147, v242\n"
"v_sub_f32 v156, v156, v242\n"
"v_sub_f32 v157, v157, v242\n"
"v_sub_f32 v158, v158, v242\n"
"v_sub_f32 v159, v159, v242\n"
"v_sub_f32 v192, v192, v242\n"
"v_sub_f32 v193, v193, v242\n"
"v_sub_f32 v194, v194, v242\n"
"v_sub_f32 v195, v195, v242\n"
"v_sub_f32 v168, v168, v242\n"
"v_sub_f32 v169, v169, v242\n"
"v_sub_f32 v170, v170, v242\n"
"v_sub_f32 v171, v171, v242\n"
"v_sub_f32 v180, v180, v242\n"
"v_sub_f32 v181, v181, v242\n"
"v_sub_f32 v182, v182, v242\n"
"v_sub_f32 v183, v183, v242\n"
"v_exp_f32 v243, -v242\n"
"s_nop 1\n"
"v_mul_f32 v236, v236, v243\n"
"v_mul_f32 v0, v0, v243\n"
"v_mul_f32 v1, v1, v243\n"
"v_mul_f32 v2, v2, v243\n"
"v_mul_f32 v3, v3, v243\n"
"v_mul_f32 v12, v12, v243\n"
"v_mul_f32 v13, v13, v243\n"
"v_mul_f32 v14, v14, v243\n"
"v_mul_f32 v15, v15, v243\n"
"v_mul_f32 v24, v24, v243\n"
"v_mul_f32 v25, v25, v243\n"
"v_mul_f32 v26, v26, v243\n"
"v_mul_f32 v27, v27, v243\n"
"v_mul_f32 v36, v36, v243\n"
"v_mul_f32 v37, v37, v243\n"
"v_mul_f32 v38, v38, v243\n"
"v_mul_f32 v39, v39, v243\n"
"v_mul_f32 v48, v48, v243\n"
"v_mul_f32 v49, v49, v243\n"
"v_mul_f32 v50, v50, v243\n"
"v_mul_f32 v51, v51, v243\n"
"v_mul_f32 v60, v60, v243\n"
"v_mul_f32 v61, v61, v243\n"
"v_mul_f32 v62, v62, v243\n"
"v_mul_f32 v63, v63, v243\n"
"v_mul_f32 v72, v72, v243\n"
"v_mul_f32 v73, v73, v243\n"
"v_mul_f32 v74, v74, v243\n"
"v_mul_f32 v75, v75, v243\n"
"v_mul_f32 v84, v84, v243\n"
"v_mul_f32 v85, v85, v243\n"
"v_mul_f32 v86, v86, v243\n"
"v_mul_f32 v87, v87, v243\n"
"v_exp_f32 v144, v144\n"
"v_exp_f32 v145, v145\n"
"v_exp_f32 v146, v146\n"
"v_exp_f32 v147, v147\n"
"v_exp_f32 v156, v156\n"
"v_exp_f32 v157, v157\n"
"v_exp_f32 v158, v158\n"
"v_exp_f32 v159, v159\n"
"s_nop 1\n"
"v_cvt_pk_bf16_f32 v144, v144, v145\n"
"v_cvt_pk_bf16_f32 v145, v146, v147\n"
"v_cvt_pk_bf16_f32 v146, v156, v157\n"
"v_cvt_pk_bf16_f32 v147, v158, v159\n"
"v_max3_f32 v242, v148, v149, v150\n"
"v_max3_f32 v242, v242, v151, v160\n"
"v_max3_f32 v242, v242, v161, v162\n"
"v_max_f32 v242, v242, v163\n"
"v_mov_b32 v243, v242\n"
"s_nop 1\n"
"v_permlane32_swap_b32 v242, v243\n"
"v_max_f32 v242, v242, v243\n"
"v_mov_b32 v243, v242\n"
"s_nop 1\n"
"v_permlane16_swap_b32 v242, v243\n"
"v_max_f32 v242, v242, v243\n"
"v_max_f32 v242, 0, v242\n"
"v_sub_f32 v148, v148, v242\n"
"v_sub_f32 v149, v149, v242\n"
"v_sub_f32 v150, v150, v242\n"
"v_sub_f32 v151, v151, v242\n"
"v_sub_f32 v160, v160, v242\n"
"v_sub_f32 v161, v161, v242\n"
"v_sub_f32 v162, v162, v242\n"
"v_sub_f32 v163, v163, v242\n"
"v_sub_f32 v196, v196, v242\n"
"v_sub_f32 v197, v197, v242\n"
"v_sub_f32 v198, v198, v242\n"
"v_sub_f32 v199, v199, v242\n"
"v_sub_f32 v172, v172, v242\n"
"v_sub_f32 v173, v173, v242\n"
"v_sub_f32 v174, v174, v242\n"
"v_sub_f32 v175, v175, v242\n"
"v_sub_f32 v184, v184, v242\n"
"v_sub_f32 v185, v185, v242\n"
"v_sub_f32 v186, v186, v242\n"
"v_sub_f32 v187, v187, v242\n"
"v_exp_f32 v243, -v242\n"
"s_nop 1\n"
"v_mul_f32 v237, v237, v243\n"
"v_mul_f32 v4, v4, v243\n"
"v_mul_f32 v5, v5, v243\n"
"v_mul_f32 v6, v6, v243\n"
"v_mul_f32 v7, v7, v243\n"
"v_mul_f32 v16, v16, v243\n"
"v_mul_f32 v17, v17, v243\n"
"v_mul_f32 v18, v18, v243\n"
"v_mul_f32 v19, v19, v243\n"
"v_mul_f32 v28, v28, v243\n"
"v_mul_f32 v29, v29, v243\n"
"v_mul_f32 v30, v30, v243\n"
"v_mul_f32 v31, v31, v243\n"
"v_mul_f32 v40, v40, v243\n"
"v_mul_f32 v41, v41, v243\n"
"v_mul_f32 v42, v42, v243\n"
"v_mul_f32 v43, v43, v243\n"
"v_mul_f32 v52, v52, v243\n"
"v_mul_f32 v53, v53, v243\n"
"v_mul_f32 v54, v54, v243\n"
"v_mul_f32 v55, v55, v243\n"
"v_mul_f32 v64, v64, v243\n"
"v_mul_f32 v65, v65, v243\n"
"v_mul_f32 v66, v66, v243\n"
"v_mul_f32 v67, v67, v243\n"
"v_mul_f32 v76, v76, v243\n"
"v_mul_f32 v77, v77, v243\n"
"v_mul_f32 v78, v78, v243\n"
"v_mul_f32 v79, v79, v243\n"
"v_mul_f32 v88, v88, v243\n"
"v_mul_f32 v89, v89, v243\n"
"v_mul_f32 v90, v90, v243\n"
"v_mul_f32 v91, v91, v243\n"
"v_exp_f32 v148, v148\n"
"v_exp_f32 v149, v149\n"
"v_exp_f32 v150, v150\n"
"v_exp_f32 v151, v151\n"
"v_exp_f32 v160, v160\n"
"v_exp_f32 v161, v161\n"
"v_exp_f32 v162, v162\n"
"v_exp_f32 v163, v163\n"
"s_nop 1\n"
"v_cvt_pk_bf16_f32 v148, v148, v149\n"
"v_cvt_pk_bf16_f32 v149, v150, v151\n"
"v_cvt_pk_bf16_f32 v150, v160, v161\n"
"v_cvt_pk_bf16_f32 v151, v162, v163\n"
"v_max3_f32 v242, v152, v153, v154\n"
"v_max3_f32 v242, v242, v155, v164\n"
"v_max3_f32 v242, v242, v165, v166\n"
"v_max_f32 v242, v242, v167\n"
"v_mov_b32 v243, v242\n"
"s_nop 1\n"
"v_permlane32_swap_b32 v242, v243\n"
"v_max_f32 v242, v242, v243\n"
"v_mov_b32 v243, v242\n"
"s_nop 1\n"
"v_permlane16_swap_b32 v242, v243\n"
"v_max_f32 v242, v242, v243\n"
"v_max_f32 v242, 0, v242\n"
"v_sub_f32 v152, v152, v242\n"
"v_sub_f32 v153, v153, v242\n"
"v_sub_f32 v154, v154, v242\n"
"v_sub_f32 v155, v155, v242\n"
"v_sub_f32 v164, v164, v242\n"
"v_sub_f32 v165, v165, v242\n"
"v_sub_f32 v166, v166, v242\n"
"v_sub_f32 v167, v167, v242\n"
"v_sub_f32 v200, v200, v242\n"
"v_sub_f32 v201, v201, v242\n"
"v_sub_f32 v202, v202, v242\n"
"v_sub_f32 v203, v203, v242\n"
"v_sub_f32 v176, v176, v242\n"
"v_sub_f32 v177, v177, v242\n"
"v_sub_f32 v178, v178, v242\n"
"v_sub_f32 v179, v179, v242\n"
"v_sub_f32 v188, v188, v242\n"
"v_sub_f32 v189, v189, v242\n"
"v_sub_f32 v190, v190, v242\n"
"v_sub_f32 v191, v191, v242\n"
"v_exp_f32 v243, -v242\n"
"s_nop 1\n"
"v_mul_f32 v238, v238, v243\n"
"v_mul_f32 v8, v8, v243\n"
"v_mul_f32 v9, v9, v243\n"
"v_mul_f32 v10, v10, v243\n"
"v_mul_f32 v11, v11, v243\n"
"v_mul_f32 v20, v20, v243\n"
"v_mul_f32 v21, v21, v243\n"
"v_mul_f32 v22, v22, v243\n"
"v_mul_f32 v23, v23, v243\n"
"v_mul_f32 v32, v32, v243\n"
"v_mul_f32 v33, v33, v243\n"
"v_mul_f32 v34, v34, v243\n"
"v_mul_f32 v35, v35, v243\n"
"v_mul_f32 v44, v44, v243\n"
"v_mul_f32 v45, v45, v243\n"
"v_mul_f32 v46, v46, v243\n"
"v_mul_f32 v47, v47, v243\n"
"v_mul_f32 v56, v56, v243\n"
"v_mul_f32 v57, v57, v243\n"
"v_mul_f32 v58, v58, v243\n"
"v_mul_f32 v59, v59, v243\n"
"v_mul_f32 v68, v68, v243\n"
"v_mul_f32 v69, v69, v243\n"
"v_mul_f32 v70, v70, v243\n"
"v_mul_f32 v71, v71, v243\n"
"v_mul_f32 v80, v80, v243\n"
"v_mul_f32 v81, v81, v243\n"
"v_mul_f32 v82, v82, v243\n"
"v_mul_f32 v83, v83, v243\n"
"v_mul_f32 v92, v92, v243\n"
"v_mul_f32 v93, v93, v243\n"
"v_mul_f32 v94, v94, v243\n"
"v_mul_f32 v95, v95, v243\n"
"v_exp_f32 v152, v152\n"
"v_exp_f32 v153, v153\n"
"v_exp_f32 v154, v154\n"
"v_exp_f32 v155, v155\n"
"v_exp_f32 v164, v164\n"
"v_exp_f32 v165, v165\n"
"v_exp_f32 v166, v166\n"
"v_exp_f32 v167, v167\n"
"s_nop 1\n"
"v_cvt_pk_bf16_f32 v152, v152, v153\n"
"v_cvt_pk_bf16_f32 v153, v154, v155\n"
"v_cvt_pk_bf16_f32 v154, v164, v165\n"
"v_cvt_pk_bf16_f32 v155, v166, v167\n"
"v_mov_b32 v244, 0x3f803f80\n"
"v_mov_b32 v245, 0x3f803f80\n"
"v_mov_b32 v246, 0x3f803f80\n"
"v_mov_b32 v247, 0x3f803f80\n"
"s_nop 1\n"
"v_mfma_f32_16x16x32_bf16 v[156:159], v[244:247], v[144:147], 0\n"
"v_mfma_f32_16x16x32_bf16 v[160:163], v[244:247], v[148:151], 0\n"
"v_mfma_f32_16x16x32_bf16 v[164:167], v[244:247], v[152:155], 0\n"
"s_nop 7\n"
"s_nop 7\n"
"s_nop 3\n"
"s_branch .LrAbx23\n"
".LrAx24:\n"
"s_nop 7\n"
"s_nop 7\n"
"v_bfe_u32 v243, v253, 4, 3\n"
"v_lshlrev_b32 v243, 8, v243\n"
"v_bfe_u32 v242, v253, 7, 1\n"
"v_lshl_add_u32 v243, v242, 11, v243\n"
"v_lshrrev_b32 v242, 8, v253\n"
"v_lshl_add_u32 v243, v242, 4, v243\n"
"s_and_b32 s70, s69, 15\n"
"s_lshr_b32 s71, s70, 0\n"
"s_lshl_b32 s71, s71, 2\n"
"s_and_b32 s70, s70, 0\n"
"s_or_b32 s71, s71, s70\n"
"s_lshl_b32 s71, s71, 11\n"
"s_lshr_b32 s70, s69, 4\n"
"s_lshl_b32 s70, s70, 17\n"
"s_add_u32 s71, s71, s70\n"
"s_add_u32 s70, s71, 0\n"
"buffer_load_dwordx4 v[244:247], v243, s[20:23], s70 offen offset:0\n"
"s_waitcnt vmcnt(0)\n"
"v_mfma_f32_16x16x32_bf16 v[168:171], v[244:247], v[96:99], v[192:195]\n"
"v_mfma_f32_16x16x32_bf16 v[172:175], v[244:247], v[112:115], v[196:199]\n"
"v_mfma_f32_16x16x32_bf16 v[176:179], v[244:247], v[128:131], v[200:203]\n"
"s_nop 7\n"
"buffer_load_dwordx4 v[244:247], v243, s[20:23], s70 offen offset:64\n"
"s_waitcnt vmcnt(0)\n"
"v_mfma_f32_16x16x32_bf16 v[168:171], v[244:247], v[100:103], v[168:171]\n"
"v_mfma_f32_16x16x32_bf16 v[172:175], v[244:247], v[116:119], v[172:175]\n"
"v_mfma_f32_16x16x32_bf16 v[176:179], v[244:247], v[132:135], v[176:179]\n"
"s_nop 7\n"
"buffer_load_dwordx4 v[244:247], v243, s[20:23], s70 offen offset:128\n"
"s_waitcnt vmcnt(0)\n"
"v_mfma_f32_16x16x32_bf16 v[168:171], v[244:247], v[104:107], v[168:171]\n"
"v_mfma_f32_16x16x32_bf16 v[172:175], v[244:247], v[120:123], v[172:175]\n"
"v_mfma_f32_16x16x32_bf16 v[176:179], v[244:247], v[136:139], v[176:179]\n"
"s_nop 7\n"
"buffer_load_dwordx4 v[244:247], v243, s[20:23], s70 offen offset:192\n"
"s_waitcnt vmcnt(0)\n"
"v_mfma_f32_16x16x32_bf16 v[168:171], v[244:247], v[108:111], v[168:171]\n"
"v_mfma_f32_16x16x32_bf16 v[172:175], v[244:247], v[124:127], v[172:175]\n"
"v_mfma_f32_16x16x32_bf16 v[176:179], v[244:247], v[140:143], v[176:179]\n"
"s_nop 7\n"
"s_add_u32 s70, s71, 4096\n"
"buffer_load_dwordx4 v[244:247], v243, s[20:23], s70 offen offset:0\n"
"s_waitcnt vmcnt(0)\n"
"v_mfma_f32_16x16x32_bf16 v[180:183], v[244:247], v[96:99], v[192:195]\n"
"v_mfma_f32_16x16x32_bf16 v[184:187], v[244:247], v[112:115], v[196:199]\n"
"v_mfma_f32_16x16x32_bf16 v[188:191], v[244:247], v[128:131], v[200:203]\n"
"s_nop 7\n"
"buffer_load_dwordx4 v[244:247], v243, s[20:23], s70 offen offset:64\n"
"s_waitcnt vmcnt(0)\n"
"v_mfma_f32_16x16x32_bf16 v[180:183], v[244:247], v[100:103], v[180:183]\n"
"v_mfma_f32_16x16x32_bf16 v[184:187], v[244:247], v[116:119], v[184:187]\n"
"v_mfma_f32_16x16x32_bf16 v[188:191], v[244:247], v[132:135], v[188:191]\n"
"s_nop 7\n"
"buffer_load_dwordx4 v[244:247], v243, s[20:23], s70 offen offset:128\n"
"s_waitcnt vmcnt(0)\n"
"v_mfma_f32_16x16x32_bf16 v[180:183], v[244:247], v[104:107], v[180:183]\n"
"v_mfma_f32_16x16x32_bf16 v[184:187], v[244:247], v[120:123], v[184:187]\n"
"v_mfma_f32_16x16x32_bf16 v[188:191], v[244:247], v[136:139], v[188:191]\n"
"s_nop 7\n"
"buffer_load_dwordx4 v[244:247], v243, s[20:23], s70 offen offset:192\n"
"s_waitcnt vmcnt(0)\n"
"v_mfma_f32_16x16x32_bf16 v[180:183], v[244:247], v[108:111], v[180:183]\n"
"v_mfma_f32_16x16x32_bf16 v[184:187], v[244:247], v[124:127], v[184:187]\n"
"v_mfma_f32_16x16x32_bf16 v[188:191], v[244:247], v[140:143], v[188:191]\n"
"s_nop 7\n"
"s_nop 7\n"
"s_nop 7\n"
"v_max3_f32 v242, v168, v169, v170\n"
"v_max3_f32 v242, v242, v171, v180\n"
"v_max3_f32 v242, v242, v181, v182\n"
"v_max_f32 v242, v242, v183\n"
"v_mov_b32 v243, v242\n"
"s_nop 1\n"
"v_permlane32_swap_b32 v242, v243\n"
"v_max_f32 v242, v242, v243\n"
"v_mov_b32 v243, v242\n"
"s_nop 1\n"
"v_permlane16_swap_b32 v242, v243\n"
"v_max_f32 v242, v242, v243\n"
"v_max_f32 v242, 0, v242\n"
"v_sub_f32 v168, v168, v242\n"
"v_sub_f32 v169, v169, v242\n"
"v_sub_f32 v170, v170, v242\n"
"v_sub_f32 v171, v171, v242\n"
"v_sub_f32 v180, v180, v242\n"
"v_sub_f32 v181, v181, v242\n"
"v_sub_f32 v182, v182, v242\n"
"v_sub_f32 v183, v183, v242\n"
"v_sub_f32 v192, v192, v242\n"
"v_sub_f32 v193, v193, v242\n"
"v_sub_f32 v194, v194, v242\n"
"v_sub_f32 v195, v195, v242\n"
"v_exp_f32 v243, -v242\n"
"s_nop 1\n"
"v_mul_f32 v236, v236, v243\n"
"v_mul_f32 v0, v0, v243\n"
"v_mul_f32 v1, v1, v243\n"
"v_mul_f32 v2, v2, v243\n"
"v_mul_f32 v3, v3, v243\n"
"v_mul_f32 v12, v12, v243\n"
"v_mul_f32 v13, v13, v243\n"
"v_mul_f32 v14, v14, v243\n"
"v_mul_f32 v15, v15, v243\n"
"v_mul_f32 v24, v24, v243\n"
"v_mul_f32 v25, v25, v243\n"
"v_mul_f32 v26, v26, v243\n"
"v_mul_f32 v27, v27, v243\n"
"v_mul_f32 v36, v36, v243\n"
"v_mul_f32 v37, v37, v243\n"
"v_mul_f32 v38, v38, v243\n"
"v_mul_f32 v39, v39, v243\n"
"v_mul_f32 v48, v48, v243\n"
"v_mul_f32 v49, v49, v243\n"
"v_mul_f32 v50, v50, v243\n"
"v_mul_f32 v51, v51, v243\n"
"v_mul_f32 v60, v60, v243\n"
"v_mul_f32 v61, v61, v243\n"
"v_mul_f32 v62, v62, v243\n"
"v_mul_f32 v63, v63, v243\n"
"v_mul_f32 v72, v72, v243\n"
"v_mul_f32 v73, v73, v243\n"
"v_mul_f32 v74, v74, v243\n"
"v_mul_f32 v75, v75, v243\n"
"v_mul_f32 v84, v84, v243\n"
"v_mul_f32 v85, v85, v243\n"
"v_mul_f32 v86, v86, v243\n"
"v_mul_f32 v87, v87, v243\n"
"v_exp_f32 v168, v168\n"
"v_exp_f32 v169, v169\n"
"v_exp_f32 v170, v170\n"
"v_exp_f32 v171, v171\n"
"v_exp_f32 v180, v180\n"
"v_exp_f32 v181, v181\n"
"v_exp_f32 v182, v182\n"
"v_exp_f32 v183, v183\n"
"s_nop 1\n"
"v_cvt_pk_bf16_f32 v168, v168, v169\n"
"v_cvt_pk_bf16_f32 v169, v170, v171\n"
"v_cvt_pk_bf16_f32 v170, v180, v181\n"
"v_cvt_pk_bf16_f32 v171, v182, v183\n"
"v_max3_f32 v242, v172, v173, v174\n"
"v_max3_f32 v242, v242, v175, v184\n"
"v_max3_f32 v242, v242, v185, v186\n"
"v_max_f32 v242, v242, v187\n"
"v_mov_b32 v243, v242\n"
"s_nop 1\n"
"v_permlane32_swap_b32 v242, v243\n"
"v_max_f32 v242, v242, v243\n"
"v_mov_b32 v243, v242\n"
"s_nop 1\n"
"v_permlane16_swap_b32 v242, v243\n"
"v_max_f32 v242, v242, v243\n"
"v_max_f32 v242, 0, v242\n"
"v_sub_f32 v172, v172, v242\n"
"v_sub_f32 v173, v173, v242\n"
"v_sub_f32 v174, v174, v242\n"
"v_sub_f32 v175, v175, v242\n"
"v_sub_f32 v184, v184, v242\n"
"v_sub_f32 v185, v185, v242\n"
"v_sub_f32 v186, v186, v242\n"
"v_sub_f32 v187, v187, v242\n"
"v_sub_f32 v196, v196, v242\n"
"v_sub_f32 v197, v197, v242\n"
"v_sub_f32 v198, v198, v242\n"
"v_sub_f32 v199, v199, v242\n"
"v_exp_f32 v243, -v242\n"
"s_nop 1\n"
"v_mul_f32 v237, v237, v243\n"
"v_mul_f32 v4, v4, v243\n"
"v_mul_f32 v5, v5, v243\n"
"v_mul_f32 v6, v6, v243\n"
"v_mul_f32 v7, v7, v243\n"
"v_mul_f32 v16, v16, v243\n"
"v_mul_f32 v17, v17, v243\n"
"v_mul_f32 v18, v18, v243\n"
"v_mul_f32 v19, v19, v243\n"
"v_mul_f32 v28, v28, v243\n"
"v_mul_f32 v29, v29, v243\n"
"v_mul_f32 v30, v30, v243\n"
"v_mul_f32 v31, v31, v243\n"
"v_mul_f32 v40, v40, v243\n"
"v_mul_f32 v41, v41, v243\n"
"v_mul_f32 v42, v42, v243\n"
"v_mul_f32 v43, v43, v243\n"
"v_mul_f32 v52, v52, v243\n"
"v_mul_f32 v53, v53, v243\n"
"v_mul_f32 v54, v54, v243\n"
"v_mul_f32 v55, v55, v243\n"
"v_mul_f32 v64, v64, v243\n"
"v_mul_f32 v65, v65, v243\n"
"v_mul_f32 v66, v66, v243\n"
"v_mul_f32 v67, v67, v243\n"
"v_mul_f32 v76, v76, v243\n"
"v_mul_f32 v77, v77, v243\n"
"v_mul_f32 v78, v78, v243\n"
"v_mul_f32 v79, v79, v243\n"
"v_mul_f32 v88, v88, v243\n"
"v_mul_f32 v89, v89, v243\n"
"v_mul_f32 v90, v90, v243\n"
"v_mul_f32 v91, v91, v243\n"
"v_exp_f32 v172, v172\n"
"v_exp_f32 v173, v173\n"
"v_exp_f32 v174, v174\n"
"v_exp_f32 v175, v175\n"
"v_exp_f32 v184, v184\n"
"v_exp_f32 v185, v185\n"
"v_exp_f32 v186, v186\n"
"v_exp_f32 v187, v187\n"
"s_nop 1\n"
"v_cvt_pk_bf16_f32 v172, v172, v173\n"
"v_cvt_pk_bf16_f32 v173, v174, v175\n"
"v_cvt_pk_bf16_f32 v174, v184, v185\n"
"v_cvt_pk_bf16_f32 v175, v186, v187\n"
"v_max3_f32 v242, v176, v177, v178\n"
"v_max3_f32 v242, v242, v179, v188\n"
"v_max3_f32 v242, v242, v189, v190\n"
"v_max_f32 v242, v242, v191\n"
"v_mov_b32 v243, v242\n"
"s_nop 1\n"
"v_permlane32_swap_b32 v242, v243\n"
"v_max_f32 v242, v242, v243\n"
"v_mov_b32 v243, v242\n"
"s_nop 1\n"
"v_permlane16_swap_b32 v242, v243\n"
"v_max_f32 v242, v242, v243\n"
"v_max_f32 v242, 0, v242\n"
"v_sub_f32 v176, v176, v242\n"
"v_sub_f32 v177, v177, v242\n"
"v_sub_f32 v178, v178, v242\n"
"v_sub_f32 v179, v179, v242\n"
"v_sub_f32 v188, v188, v242\n"
"v_sub_f32 v189, v189, v242\n"
"v_sub_f32 v190, v190, v242\n"
"v_sub_f32 v191, v191, v242\n"
"v_sub_f32 v200, v200, v242\n"
"v_sub_f32 v201, v201, v242\n"
"v_sub_f32 v202, v202, v242\n"
"v_sub_f32 v203, v203, v242\n"
"v_exp_f32 v243, -v242\n"
"s_nop 1\n"
"v_mul_f32 v238, v238, v243\n"
"v_mul_f32 v8, v8, v243\n"
"v_mul_f32 v9, v9, v243\n"
"v_mul_f32 v10, v10, v243\n"
"v_mul_f32 v11, v11, v243\n"
"v_mul_f32 v20, v20, v243\n"
"v_mul_f32 v21, v21, v243\n"
"v_mul_f32 v22, v22, v243\n"
"v_mul_f32 v23, v23, v243\n"
"v_mul_f32 v32, v32, v243\n"
"v_mul_f32 v33, v33, v243\n"
"v_mul_f32 v34, v34, v243\n"
"v_mul_f32 v35, v35, v243\n"
"v_mul_f32 v44, v44, v243\n"
"v_mul_f32 v45, v45, v243\n"
"v_mul_f32 v46, v46, v243\n"
"v_mul_f32 v47, v47, v243\n"
"v_mul_f32 v56, v56, v243\n"
"v_mul_f32 v57, v57, v243\n"
"v_mul_f32 v58, v58, v243\n"
"v_mul_f32 v59, v59, v243\n"
"v_mul_f32 v68, v68, v243\n"
"v_mul_f32 v69, v69, v243\n"
"v_mul_f32 v70, v70, v243\n"
"v_mul_f32 v71, v71, v243\n"
"v_mul_f32 v80, v80, v243\n"
"v_mul_f32 v81, v81, v243\n"
"v_mul_f32 v82, v82, v243\n"
"v_mul_f32 v83, v83, v243\n"
"v_mul_f32 v92, v92, v243\n"
"v_mul_f32 v93, v93, v243\n"
"v_mul_f32 v94, v94, v243\n"
"v_mul_f32 v95, v95, v243\n"
"v_exp_f32 v176, v176\n"
"v_exp_f32 v177, v177\n"
"v_exp_f32 v178, v178\n"
"v_exp_f32 v179, v179\n"
"v_exp_f32 v188, v188\n"
"v_exp_f32 v189, v189\n"
"v_exp_f32 v190, v190\n"
"v_exp_f32 v191, v191\n"
"s_nop 1\n"
"v_cvt_pk_bf16_f32 v176, v176, v177\n"
"v_cvt_pk_bf16_f32 v177, v178, v179\n"
"v_cvt_pk_bf16_f32 v178, v188, v189\n"
"v_cvt_pk_bf16_f32 v179, v190, v191\n"
"v_mov_b32 v244, 0x3f803f80\n"
"v_mov_b32 v245, 0x3f803f80\n"
"v_mov_b32 v246, 0x3f803f80\n"
"v_mov_b32 v247, 0x3f803f80\n"
"s_nop 1\n"
"v_mfma_f32_16x16x32_bf16 v[180:183], v[244:247], v[168:171], 0\n"
"v_mfma_f32_16x16x32_bf16 v[184:187], v[244:247], v[172:175], 0\n"
"v_mfma_f32_16x16x32_bf16 v[188:191], v[244:247], v[176:179], 0\n"
"s_nop 7\n"
"s_nop 7\n"
"s_nop 3\n"
"s_branch .LrAbx24\n"
".LrAy36:\n"
"s_nop 7\n"
"s_nop 7\n"
"v_bfe_u32 v243, v253, 4, 3\n"
"v_lshlrev_b32 v243, 8, v243\n"
"v_bfe_u32 v242, v253, 7, 1\n"
"v_lshl_add_u32 v243, v242, 11, v243\n"
"v_lshrrev_b32 v242, 8, v253\n"
"v_lshl_add_u32 v243, v242, 4, v243\n"
"s_and_b32 s70, s69, 15\n"
"s_lshr_b32 s71, s70, 0\n"
"s_lshl_b32 s71, s71, 2\n"
"s_and_b32 s70, s70, 0\n"
"s_or_b32 s71, s71, s70\n"
"s_lshl_b32 s71, s71, 11\n"
"s_lshr_b32 s70, s69, 4\n"
"s_lshl_b32 s70, s70, 17\n"
"s_add_u32 s71, s71, s70\n"
"s_add_u32 s70, s71, 0\n"
"buffer_load_dwordx4 v[244:247], v243, s[20:23], s70 offen offset:0\n"
"s_waitcnt vmcnt(0)\n"
"v_mfma_f32_16x16x32_bf16 v[168:171], v[244:247], v[96:99], v[192:195]\n"
"v_mfma_f32_16x16x32_bf16 v[172:175], v[244:247], v[112:115], v[196:199]\n"
"v_mfma_f32_16x16x32_bf16 v[176:179], v[244:247], v[128:131], v[200:203]\n"
"s_nop 7\n"
"buffer_load_dwordx4 v[244:247], v243, s[20:23], s70 offen offset:64\n"
"s_waitcnt vmcnt(0)\n"
"v_mfma_f32_16x16x32_bf16 v[168:171], v[244:247], v[100:103], v[168:171]\n"
"v_mfma_f32_16x16x32_bf16 v[172:175], v[244:247], v[116:119], v[172:175]\n"
"v_mfma_f32_16x16x32_bf16 v[176:179], v[244:247], v[132:135], v[176:179]\n"
"s_nop 7\n"
"buffer_load_dwordx4 v[244:247], v243, s[20:23], s70 offen offset:128\n"
"s_waitcnt vmcnt(0)\n"
"v_mfma_f32_16x16x32_bf16 v[168:171], v[244:247], v[104:107], v[168:171]\n"
"v_mfma_f32_16x16x32_bf16 v[172:175], v[244:247], v[120:123], v[172:175]\n"
"v_mfma_f32_16x16x32_bf16 v[176:179], v[244:247], v[136:139], v[176:179]\n"
"s_nop 7\n"
"buffer_load_dwordx4 v[244:247], v243, s[20:23], s70 offen offset:192\n"
"s_waitcnt vmcnt(0)\n"
"v_mfma_f32_16x16x32_bf16 v[168:171], v[244:247], v[108:111], v[168:171]\n"
"v_mfma_f32_16x16x32_bf16 v[172:175], v[244:247], v[124:127], v[172:175]\n"
"v_mfma_f32_16x16x32_bf16 v[176:179], v[244:247], v[140:143], v[176:179]\n"
"s_nop 7\n"
"s_add_u32 s70, s71, 4096\n"
"buffer_load_dwordx4 v[244:247], v243, s[20:23], s70 offen offset:0\n"
"s_waitcnt vmcnt(0)\n"
"v_mfma_f32_16x16x32_bf16 v[180:183], v[244:247], v[96:99], v[192:195]\n"
"v_mfma_f32_16x16x32_bf16 v[184:187], v[244:247], v[112:115], v[196:199]\n"
"v_mfma_f32_16x16x32_bf16 v[188:191], v[244:247], v[128:131], v[200:203]\n"
"s_nop 7\n"
"buffer_load_dwordx4 v[244:247], v243, s[20:23], s70 offen offset:64\n"
"s_waitcnt vmcnt(0)\n"
"v_mfma_f32_16x16x32_bf16 v[180:183], v[244:247], v[100:103], v[180:183]\n"
"v_mfma_f32_16x16x32_bf16 v[184:187], v[244:247], v[116:119], v[184:187]\n"
"v_mfma_f32_16x16x32_bf16 v[188:191], v[244:247], v[132:135], v[188:191]\n"
"s_nop 7\n"
"buffer_load_dwordx4 v[244:247], v243, s[20:23], s70 offen offset:128\n"
"s_waitcnt vmcnt(0)\n"
"v_mfma_f32_16x16x32_bf16 v[180:183], v[244:247], v[104:107], v[180:183]\n"
"v_mfma_f32_16x16x32_bf16 v[184:187], v[244:247], v[120:123], v[184:187]\n"
"v_mfma_f32_16x16x32_bf16 v[188:191], v[244:247], v[136:139], v[188:191]\n"
"s_nop 7\n"
"buffer_load_dwordx4 v[244:247], v243, s[20:23], s70 offen offset:192\n"
"s_waitcnt vmcnt(0)\n"
"v_mfma_f32_16x16x32_bf16 v[180:183], v[244:247], v[108:111], v[180:183]\n"
"v_mfma_f32_16x16x32_bf16 v[184:187], v[244:247], v[124:127], v[184:187]\n"
"v_mfma_f32_16x16x32_bf16 v[188:191], v[244:247], v[140:143], v[188:191]\n"
"s_nop 7\n"
"s_nop 7\n"
"s_nop 7\n"
"v_max3_f32 v242, v168, v169, v170\n"
"v_max3_f32 v242, v242, v171, v180\n"
"v_max3_f32 v242, v242, v181, v182\n"
"v_max_f32 v242, v242, v183\n"
"v_mov_b32 v243, v242\n"
"s_nop 1\n"
"v_permlane32_swap_b32 v242, v243\n"
"v_max_f32 v242, v242, v243\n"
"v_mov_b32 v243, v242\n"
"s_nop 1\n"
"v_permlane16_swap_b32 v242, v243\n"
"v_max_f32 v242, v242, v243\n"
"v_max_f32 v242, 0, v242\n"
"v_sub_f32 v168, v168, v242\n"
"v_sub_f32 v169, v169, v242\n"
"v_sub_f32 v170, v170, v242\n"
"v_sub_f32 v171, v171, v242\n"
"v_sub_f32 v180, v180, v242\n"
"v_sub_f32 v181, v181, v242\n"
"v_sub_f32 v182, v182, v242\n"
"v_sub_f32 v183, v183, v242\n"
"v_sub_f32 v192, v192, v242\n"
"v_sub_f32 v193, v193, v242\n"
"v_sub_f32 v194, v194, v242\n"
"v_sub_f32 v195, v195, v242\n"
"v_sub_f32 v144, v144, v242\n"
"v_sub_f32 v145, v145, v242\n"
"v_sub_f32 v146, v146, v242\n"
"v_sub_f32 v147, v147, v242\n"
"v_sub_f32 v156, v156, v242\n"
"v_sub_f32 v157, v157, v242\n"
"v_sub_f32 v158, v158, v242\n"
"v_sub_f32 v159, v159, v242\n"
"v_exp_f32 v243, -v242\n"
"s_nop 1\n"
"v_mul_f32 v236, v236, v243\n"
"v_mul_f32 v0, v0, v243\n"
"v_mul_f32 v1, v1, v243\n"
"v_mul_f32 v2, v2, v243\n"
"v_mul_f32 v3, v3, v243\n"
"v_mul_f32 v12, v12, v243\n"
"v_mul_f32 v13, v13, v243\n"
"v_mul_f32 v14, v14, v243\n"
"v_mul_f32 v15, v15, v243\n"
"v_mul_f32 v24, v24, v243\n"
"v_mul_f32 v25, v25, v243\n"
"v_mul_f32 v26, v26, v243\n"
"v_mul_f32 v27, v27, v243\n"
"v_mul_f32 v36, v36, v243\n"
"v_mul_f32 v37, v37, v243\n"
"v_mul_f32 v38, v38, v243\n"
"v_mul_f32 v39, v39, v243\n"
"v_mul_f32 v48, v48, v243\n"
"v_mul_f32 v49, v49, v243\n"
"v_mul_f32 v50, v50, v243\n"
"v_mul_f32 v51, v51, v243\n"
"v_mul_f32 v60, v60, v243\n"
"v_mul_f32 v61, v61, v243\n"
"v_mul_f32 v62, v62, v243\n"
"v_mul_f32 v63, v63, v243\n"
"v_mul_f32 v72, v72, v243\n"
"v_mul_f32 v73, v73, v243\n"
"v_mul_f32 v74, v74, v243\n"
"v_mul_f32 v75, v75, v243\n"
"v_mul_f32 v84, v84, v243\n"
"v_mul_f32 v85, v85, v243\n"
"v_mul_f32 v86, v86, v243\n"
"v_mul_f32 v87, v87, v243\n"
"v_exp_f32 v168, v168\n"
"v_exp_f32 v169, v169\n"
"v_exp_f32 v170, v170\n"
"v_exp_f32 v171, v171\n"
"v_exp_f32 v180, v180\n"
"v_exp_f32 v181, v181\n"
"v_exp_f32 v182, v182\n"
"v_exp_f32 v183, v183\n"
"s_nop 1\n"
"v_cvt_pk_bf16_f32 v168, v168, v169\n"
"v_cvt_pk_bf16_f32 v169, v170, v171\n"
"v_cvt_pk_bf16_f32 v170, v180, v181\n"
"v_cvt_pk_bf16_f32 v171, v182, v183\n"
"v_max3_f32 v242, v172, v173, v174\n"
"v_max3_f32 v242, v242, v175, v184\n"
"v_max3_f32 v242, v242, v185, v186\n"
"v_max_f32 v242, v242, v187\n"
"v_mov_b32 v243, v242\n"
"s_nop 1\n"
"v_permlane32_swap_b32 v242, v243\n"
"v_max_f32 v242, v242, v243\n"
"v_mov_b32 v243, v242\n"
"s_nop 1\n"
"v_permlane16_swap_b32 v242, v243\n"
"v_max_f32 v242, v242, v243\n"
"v_max_f32 v242, 0, v242\n"
"v_sub_f32 v172, v172, v242\n"
"v_sub_f32 v173, v173, v242\n"
"v_sub_f32 v174, v174, v242\n"
"v_sub_f32 v175, v175, v242\n"
"v_sub_f32 v184, v184, v242\n"
"v_sub_f32 v185, v185, v242\n"
"v_sub_f32 v186, v186, v242\n"
"v_sub_f32 v187, v187, v242\n"
"v_sub_f32 v196, v196, v242\n"
"v_sub_f32 v197, v197, v242\n"
"v_sub_f32 v198, v198, v242\n"
"v_sub_f32 v199, v199, v242\n"
"v_sub_f32 v148, v148, v242\n"
"v_sub_f32 v149, v149, v242\n"
"v_sub_f32 v150, v150, v242\n"
"v_sub_f32 v151, v151, v242\n"
"v_sub_f32 v160, v160, v242\n"
"v_sub_f32 v161, v161, v242\n"
"v_sub_f32 v162, v162, v242\n"
"v_sub_f32 v163, v163, v242\n"
"v_exp_f32 v243, -v242\n"
"s_nop 1\n"
"v_mul_f32 v237, v237, v243\n"
"v_mul_f32 v4, v4, v243\n"
"v_mul_f32 v5, v5, v243\n"
"v_mul_f32 v6, v6, v243\n"
"v_mul_f32 v7, v7, v243\n"
"v_mul_f32 v16, v16, v243\n"
"v_mul_f32 v17, v17, v243\n"
"v_mul_f32 v18, v18, v243\n"
"v_mul_f32 v19, v19, v243\n"
"v_mul_f32 v28, v28, v243\n"
"v_mul_f32 v29, v29, v243\n"
"v_mul_f32 v30, v30, v243\n"
"v_mul_f32 v31, v31, v243\n"
"v_mul_f32 v40, v40, v243\n"
"v_mul_f32 v41, v41, v243\n"
"v_mul_f32 v42, v42, v243\n"
"v_mul_f32 v43, v43, v243\n"
"v_mul_f32 v52, v52, v243\n"
"v_mul_f32 v53, v53, v243\n"
"v_mul_f32 v54, v54, v243\n"
"v_mul_f32 v55, v55, v243\n"
"v_mul_f32 v64, v64, v243\n"
"v_mul_f32 v65, v65, v243\n"
"v_mul_f32 v66, v66, v243\n"
"v_mul_f32 v67, v67, v243\n"
"v_mul_f32 v76, v76, v243\n"
"v_mul_f32 v77, v77, v243\n"
"v_mul_f32 v78, v78, v243\n"
"v_mul_f32 v79, v79, v243\n"
"v_mul_f32 v88, v88, v243\n"
"v_mul_f32 v89, v89, v243\n"
"v_mul_f32 v90, v90, v243\n"
"v_mul_f32 v91, v91, v243\n"
"v_exp_f32 v172, v172\n"
"v_exp_f32 v173, v173\n"
"v_exp_f32 v174, v174\n"
"v_exp_f32 v175, v175\n"
"v_exp_f32 v184, v184\n"
"v_exp_f32 v185, v185\n"
"v_exp_f32 v186, v186\n"
"v_exp_f32 v187, v187\n"
"s_nop 1\n"
"v_cvt_pk_bf16_f32 v172, v172, v173\n"
"v_cvt_pk_bf16_f32 v173, v174, v175\n"
"v_cvt_pk_bf16_f32 v174, v184, v185\n"
"v_cvt_pk_bf16_f32 v175, v186, v187\n"
"v_max3_f32 v242, v176, v177, v178\n"
"v_max3_f32 v242, v242, v179, v188\n"
"v_max3_f32 v242, v242, v189, v190\n"
"v_max_f32 v242, v242, v191\n"
"v_mov_b32 v243, v242\n"
"s_nop 1\n"
"v_permlane32_swap_b32 v242, v243\n"
"v_max_f32 v242, v242, v243\n"
"v_mov_b32 v243, v242\n"
"s_nop 1\n"
"v_permlane16_swap_b32 v242, v243\n"
"v_max_f32 v242, v242, v243\n"
"v_max_f32 v242, 0, v242\n"
"v_sub_f32 v176, v176, v242\n"
"v_sub_f32 v177, v177, v242\n"
"v_sub_f32 v178, v178, v242\n"
"v_sub_f32 v179, v179, v242\n"
"v_sub_f32 v188, v188, v242\n"
"v_sub_f32 v189, v189, v242\n"
"v_sub_f32 v190, v190, v242\n"
"v_sub_f32 v191, v191, v242\n"
"v_sub_f32 v200, v200, v242\n"
"v_sub_f32 v201, v201, v242\n"
"v_sub_f32 v202, v202, v242\n"
"v_sub_f32 v203, v203, v242\n"
"v_sub_f32 v152, v152, v242\n"
"v_sub_f32 v153, v153, v242\n"
"v_sub_f32 v154, v154, v242\n"
"v_sub_f32 v155, v155, v242\n"
"v_sub_f32 v164, v164, v242\n"
"v_sub_f32 v165, v165, v242\n"
"v_sub_f32 v166, v166, v242\n"
"v_sub_f32 v167, v167, v242\n"
"v_exp_f32 v243, -v242\n"
"s_nop 1\n"
"v_mul_f32 v238, v238, v243\n"
"v_mul_f32 v8, v8, v243\n"
"v_mul_f32 v9, v9, v243\n"
"v_mul_f32 v10, v10, v243\n"
"v_mul_f32 v11, v11, v243\n"
"v_mul_f32 v20, v20, v243\n"
"v_mul_f32 v21, v21, v243\n"
"v_mul_f32 v22, v22, v243\n"
"v_mul_f32 v23, v23, v243\n"
"v_mul_f32 v32, v32, v243\n"
"v_mul_f32 v33, v33, v243\n"
"v_mul_f32 v34, v34, v243\n"
"v_mul_f32 v35, v35, v243\n"
"v_mul_f32 v44, v44, v243\n"
"v_mul_f32 v45, v45, v243\n"
"v_mul_f32 v46, v46, v243\n"
"v_mul_f32 v47, v47, v243\n"
"v_mul_f32 v56, v56, v243\n"
"v_mul_f32 v57, v57, v243\n"
"v_mul_f32 v58, v58, v243\n"
"v_mul_f32 v59, v59, v243\n"
"v_mul_f32 v68, v68, v243\n"
"v_mul_f32 v69, v69, v243\n"
"v_mul_f32 v70, v70, v243\n"
"v_mul_f32 v71, v71, v243\n"
"v_mul_f32 v80, v80, v243\n"
"v_mul_f32 v81, v81, v243\n"
"v_mul_f32 v82, v82, v243\n"
"v_mul_f32 v83, v83, v243\n"
"v_mul_f32 v92, v92, v243\n"
"v_mul_f32 v93, v93, v243\n"
"v_mul_f32 v94, v94, v243\n"
"v_mul_f32 v95, v95, v243\n"
"v_exp_f32 v176, v176\n"
"v_exp_f32 v177, v177\n"
"v_exp_f32 v178, v178\n"
"v_exp_f32 v179, v179\n"
"v_exp_f32 v188, v188\n"
"v_exp_f32 v189, v189\n"
"v_exp_f32 v190, v190\n"
"v_exp_f32 v191, v191\n"
"s_nop 1\n"
"v_cvt_pk_bf16_f32 v176, v176, v177\n"
"v_cvt_pk_bf16_f32 v177, v178, v179\n"
"v_cvt_pk_bf16_f32 v178, v188, v189\n"
"v_cvt_pk_bf16_f32 v179, v190, v191\n"
"v_mov_b32 v244, 0x3f803f80\n"
"v_mov_b32 v245, 0x3f803f80\n"
"v_mov_b32 v246, 0x3f803f80\n"
"v_mov_b32 v247, 0x3f803f80\n"
"s_nop 1\n"
"v_mfma_f32_16x16x32_bf16 v[180:183], v[244:247], v[168:171], 0\n"
"v_mfma_f32_16x16x32_bf16 v[184:187], v[244:247], v[172:175], 0\n"
"v_mfma_f32_16x16x32_bf16 v[188:191], v[244:247], v[176:179], 0\n"
"s_nop 7\n"
"s_nop 7\n"
"s_nop 3\n"
"s_branch .LrAby36\n"
".LrAy37:\n"
"s_nop 7\n"
"s_nop 7\n"
"v_bfe_u32 v243, v253, 4, 3\n"
"v_lshlrev_b32 v243, 8, v243\n"
"v_bfe_u32 v242, v253, 7, 1\n"
"v_lshl_add_u32 v243, v242, 11, v243\n"
"v_lshrrev_b32 v242, 8, v253\n"
"v_lshl_add_u32 v243, v242, 4, v243\n"
"s_and_b32 s70, s69, 15\n"
"s_lshr_b32 s71, s70, 0\n"
"s_lshl_b32 s71, s71, 2\n"
"s_and_b32 s70, s70, 0\n"
"s_or_b32 s71, s71, s70\n"
"s_lshl_b32 s71, s71, 11\n"
"s_lshr_b32 s70, s69, 4\n"
"s_lshl_b32 s70, s70, 17\n"
"s_add_u32 s71, s71, s70\n"
"s_add_u32 s70, s71, 0\n"
"buffer_load_dwordx4 v[244:247], v243, s[20:23], s70 offen offset:0\n"
"s_waitcnt vmcnt(0)\n"
"v_mfma_f32_16x16x32_bf16 v[144:147], v[244:247], v[96:99], v[192:195]\n"
"v_mfma_f32_16x16x32_bf16 v[148:151], v[244:247], v[112:115], v[196:199]\n"
"v_mfma_f32_16x16x32_bf16 v[152:155], v[244:247], v[128:131], v[200:203]\n"
"s_nop 7\n"
"buffer_load_dwordx4 v[244:247], v243, s[20:23], s70 offen offset:64\n"
"s_waitcnt vmcnt(0)\n"
"v_mfma_f32_16x16x32_bf16 v[144:147], v[244:247], v[100:103], v[144:147]\n"
"v_mfma_f32_16x16x32_bf16 v[148:151], v[244:247], v[116:119], v[148:151]\n"
"v_mfma_f32_16x16x32_bf16 v[152:155], v[244:247], v[132:135], v[152:155]\n"
"s_nop 7\n"
"buffer_load_dwordx4 v[244:247], v243, s[20:23], s70 offen offset:128\n"
"s_waitcnt vmcnt(0)\n"
"v_mfma_f32_16x16x32_bf16 v[144:147], v[244:247], v[104:107], v[144:147]\n"
"v_mfma_f32_16x16x32_bf16 v[148:151], v[244:247], v[120:123], v[148:151]\n"
"v_mfma_f32_16x16x32_bf16 v[152:155], v[244:247], v[136:139], v[152:155]\n"
"s_nop 7\n"
"buffer_load_dwordx4 v[244:247], v243, s[20:23], s70 offen offset:192\n"
"s_waitcnt vmcnt(0)\n"
"v_mfma_f32_16x16x32_bf16 v[144:147], v[244:247], v[108:111], v[144:147]\n"
"v_mfma_f32_16x16x32_bf16 v[148:151], v[244:247], v[124:127], v[148:151]\n"
"v_mfma_f32_16x16x32_bf16 v[152:155], v[244:247], v[140:143], v[152:155]\n"
"s_nop 7\n"
"s_add_u32 s70, s71, 4096\n"
"buffer_load_dwordx4 v[244:247], v243, s[20:23], s70 offen offset:0\n"
"s_waitcnt vmcnt(0)\n"
"v_mfma_f32_16x16x32_bf16 v[156:159], v[244:247], v[96:99], v[192:195]\n"
"v_mfma_f32_16x16x32_bf16 v[160:163], v[244:247], v[112:115], v[196:199]\n"
"v_mfma_f32_16x16x32_bf16 v[164:167], v[244:247], v[128:131], v[200:203]\n"
"s_nop 7\n"
"buffer_load_dwordx4 v[244:247], v243, s[20:23], s70 offen offset:64\n"
"s_waitcnt vmcnt(0)\n"
"v_mfma_f32_16x16x32_bf16 v[156:159], v[244:247], v[100:103], v[156:159]\n"
"v_mfma_f32_16x16x32_bf16 v[160:163], v[244:247], v[116:119], v[160:163]\n"
"v_mfma_f32_16x16x32_bf16 v[164:167], v[244:247], v[132:135], v[164:167]\n"
"s_nop 7\n"
"buffer_load_dwordx4 v[244:247], v243, s[20:23], s70 offen offset:128\n"
"s_waitcnt vmcnt(0)\n"
"v_mfma_f32_16x16x32_bf16 v[156:159], v[244:247], v[104:107], v[156:159]\n"
"v_mfma_f32_16x16x32_bf16 v[160:163], v[244:247], v[120:123], v[160:163]\n"
"v_mfma_f32_16x16x32_bf16 v[164:167], v[244:247], v[136:139], v[164:167]\n"
"s_nop 7\n"
"buffer_load_dwordx4 v[244:247], v243, s[20:23], s70 offen offset:192\n"
"s_waitcnt vmcnt(0)\n"
"v_mfma_f32_16x16x32_bf16 v[156:159], v[244:247], v[108:111], v[156:159]\n"
"v_mfma_f32_16x16x32_bf16 v[160:163], v[244:247], v[124:127], v[160:163]\n"
"v_mfma_f32_16x16x32_bf16 v[164:167], v[244:247], v[140:143], v[164:167]\n"
"s_nop 7\n"
"s_nop 7\n"
"s_nop 7\n"
"v_max3_f32 v242, v144, v145, v146\n"
"v_max3_f32 v242, v242, v147, v156\n"
"v_max3_f32 v242, v242, v157, v158\n"
"v_max_f32 v242, v242, v159\n"
"v_mov_b32 v243, v242\n"
"s_nop 1\n"
"v_permlane32_swap_b32 v242, v243\n"
"v_max_f32 v242, v242, v243\n"
"v_mov_b32 v243, v242\n"
"s_nop 1\n"
"v_permlane16_swap_b32 v242, v243\n"
"v_max_f32 v242, v242, v243\n"
"v_max_f32 v242, 0, v242\n"
"v_sub_f32 v144, v144, v242\n"
"v_sub_f32 v145, v145, v242\n"
"v_sub_f32 v146, v146, v242\n"
"v_sub_f32 v147, v147, v242\n"
"v_sub_f32 v156, v156, v242\n"
"v_sub_f32 v157, v157, v242\n"
"v_sub_f32 v158, v158, v242\n"
"v_sub_f32 v159, v159, v242\n"
"v_sub_f32 v192, v192, v242\n"
"v_sub_f32 v193, v193, v242\n"
"v_sub_f32 v194, v194, v242\n"
"v_sub_f32 v195, v195, v242\n"
"v_sub_f32 v168, v168, v242\n"
"v_sub_f32 v169, v169, v242\n"
"v_sub_f32 v170, v170, v242\n"
"v_sub_f32 v171, v171, v242\n"
"v_sub_f32 v180, v180, v242\n"
"v_sub_f32 v181, v181, v242\n"
"v_sub_f32 v182, v182, v242\n"
"v_sub_f32 v183, v183, v242\n"
"v_exp_f32 v243, -v242\n"
"s_nop 1\n"
"v_mul_f32 v236, v236, v243\n"
"v_mul_f32 v0, v0, v243\n"
"v_mul_f32 v1, v1, v243\n"
"v_mul_f32 v2, v2, v243\n"
"v_mul_f32 v3, v3, v243\n"
"v_mul_f32 v12, v12, v243\n"
"v_mul_f32 v13, v13, v243\n"
"v_mul_f32 v14, v14, v243\n"
"v_mul_f32 v15, v15, v243\n"
"v_mul_f32 v24, v24, v243\n"
"v_mul_f32 v25, v25, v243\n"
"v_mul_f32 v26, v26, v243\n"
"v_mul_f32 v27, v27, v243\n"
"v_mul_f32 v36, v36, v243\n"
"v_mul_f32 v37, v37, v243\n"
"v_mul_f32 v38, v38, v243\n"
"v_mul_f32 v39, v39, v243\n"
"v_mul_f32 v48, v48, v243\n"
"v_mul_f32 v49, v49, v243\n"
"v_mul_f32 v50, v50, v243\n"
"v_mul_f32 v51, v51, v243\n"
"v_mul_f32 v60, v60, v243\n"
"v_mul_f32 v61, v61, v243\n"
"v_mul_f32 v62, v62, v243\n"
"v_mul_f32 v63, v63, v243\n"
"v_mul_f32 v72, v72, v243\n"
"v_mul_f32 v73, v73, v243\n"
"v_mul_f32 v74, v74, v243\n"
"v_mul_f32 v75, v75, v243\n"
"v_mul_f32 v84, v84, v243\n"
"v_mul_f32 v85, v85, v243\n"
"v_mul_f32 v86, v86, v243\n"
"v_mul_f32 v87, v87, v243\n"
"v_exp_f32 v144, v144\n"
"v_exp_f32 v145, v145\n"
"v_exp_f32 v146, v146\n"
"v_exp_f32 v147, v147\n"
"v_exp_f32 v156, v156\n"
"v_exp_f32 v157, v157\n"
"v_exp_f32 v158, v158\n"
"v_exp_f32 v159, v159\n"
"s_nop 1\n"
"v_cvt_pk_bf16_f32 v144, v144, v145\n"
"v_cvt_pk_bf16_f32 v145, v146, v147\n"
"v_cvt_pk_bf16_f32 v146, v156, v157\n"
"v_cvt_pk_bf16_f32 v147, v158, v159\n"
"v_max3_f32 v242, v148, v149, v150\n"
"v_max3_f32 v242, v242, v151, v160\n"
"v_max3_f32 v242, v242, v161, v162\n"
"v_max_f32 v242, v242, v163\n"
"v_mov_b32 v243, v242\n"
"s_nop 1\n"
"v_permlane32_swap_b32 v242, v243\n"
"v_max_f32 v242, v242, v243\n"
"v_mov_b32 v243, v242\n"
"s_nop 1\n"
"v_permlane16_swap_b32 v242, v243\n"
"v_max_f32 v242, v242, v243\n"
"v_max_f32 v242, 0, v242\n"
"v_sub_f32 v148, v148, v242\n"
"v_sub_f32 v149, v149, v242\n"
"v_sub_f32 v150, v150, v242\n"
"v_sub_f32 v151, v151, v242\n"
"v_sub_f32 v160, v160, v242\n"
"v_sub_f32 v161, v161, v242\n"
"v_sub_f32 v162, v162, v242\n"
"v_sub_f32 v163, v163, v242\n"
"v_sub_f32 v196, v196, v242\n"
"v_sub_f32 v197, v197, v242\n"
"v_sub_f32 v198, v198, v242\n"
"v_sub_f32 v199, v199, v242\n"
"v_sub_f32 v172, v172, v242\n"
"v_sub_f32 v173, v173, v242\n"
"v_sub_f32 v174, v174, v242\n"
"v_sub_f32 v175, v175, v242\n"
"v_sub_f32 v184, v184, v242\n"
"v_sub_f32 v185, v185, v242\n"
"v_sub_f32 v186, v186, v242\n"
"v_sub_f32 v187, v187, v242\n"
"v_exp_f32 v243, -v242\n"
"s_nop 1\n"
"v_mul_f32 v237, v237, v243\n"
"v_mul_f32 v4, v4, v243\n"
"v_mul_f32 v5, v5, v243\n"
"v_mul_f32 v6, v6, v243\n"
"v_mul_f32 v7, v7, v243\n"
"v_mul_f32 v16, v16, v243\n"
"v_mul_f32 v17, v17, v243\n"
"v_mul_f32 v18, v18, v243\n"
"v_mul_f32 v19, v19, v243\n"
"v_mul_f32 v28, v28, v243\n"
"v_mul_f32 v29, v29, v243\n"
"v_mul_f32 v30, v30, v243\n"
"v_mul_f32 v31, v31, v243\n"
"v_mul_f32 v40, v40, v243\n"
"v_mul_f32 v41, v41, v243\n"
"v_mul_f32 v42, v42, v243\n"
"v_mul_f32 v43, v43, v243\n"
"v_mul_f32 v52, v52, v243\n"
"v_mul_f32 v53, v53, v243\n"
"v_mul_f32 v54, v54, v243\n"
"v_mul_f32 v55, v55, v243\n"
"v_mul_f32 v64, v64, v243\n"
"v_mul_f32 v65, v65, v243\n"
"v_mul_f32 v66, v66, v243\n"
"v_mul_f32 v67, v67, v243\n"
"v_mul_f32 v76, v76, v243\n"
"v_mul_f32 v77, v77, v243\n"
"v_mul_f32 v78, v78, v243\n"
"v_mul_f32 v79, v79, v243\n"
"v_mul_f32 v88, v88, v243\n"
"v_mul_f32 v89, v89, v243\n"
"v_mul_f32 v90, v90, v243\n"
"v_mul_f32 v91, v91, v243\n"
"v_exp_f32 v148, v148\n"
"v_exp_f32 v149, v149\n"
"v_exp_f32 v150, v150\n"
"v_exp_f32 v151, v151\n"
"v_exp_f32 v160, v160\n"
"v_exp_f32 v161, v161\n"
"v_exp_f32 v162, v162\n"
"v_exp_f32 v163, v163\n"
"s_nop 1\n"
"v_cvt_pk_bf16_f32 v148, v148, v149\n"
"v_cvt_pk_bf16_f32 v149, v150, v151\n"
"v_cvt_pk_bf16_f32 v150, v160, v161\n"
"v_cvt_pk_bf16_f32 v151, v162, v163\n"
"v_max3_f32 v242, v152, v153, v154\n"
"v_max3_f32 v242, v242, v155, v164\n"
"v_max3_f32 v242, v242, v165, v166\n"
"v_max_f32 v242, v242, v167\n"
"v_mov_b32 v243, v242\n"
"s_nop 1\n"
"v_permlane32_swap_b32 v242, v243\n"
"v_max_f32 v242, v242, v243\n"
"v_mov_b32 v243, v242\n"
"s_nop 1\n"
"v_permlane16_swap_b32 v242, v243\n"
"v_max_f32 v242, v242, v243\n"
"v_max_f32 v242, 0, v242\n"
"v_sub_f32 v152, v152, v242\n"
"v_sub_f32 v153, v153, v242\n"
"v_sub_f32 v154, v154, v242\n"
"v_sub_f32 v155, v155, v242\n"
"v_sub_f32 v164, v164, v242\n"
"v_sub_f32 v165, v165, v242\n"
"v_sub_f32 v166, v166, v242\n"
"v_sub_f32 v167, v167, v242\n"
"v_sub_f32 v200, v200, v242\n"
"v_sub_f32 v201, v201, v242\n"
"v_sub_f32 v202, v202, v242\n"
"v_sub_f32 v203, v203, v242\n"
"v_sub_f32 v176, v176, v242\n"
"v_sub_f32 v177, v177, v242\n"
"v_sub_f32 v178, v178, v242\n"
"v_sub_f32 v179, v179, v242\n"
"v_sub_f32 v188, v188, v242\n"
"v_sub_f32 v189, v189, v242\n"
"v_sub_f32 v190, v190, v242\n"
"v_sub_f32 v191, v191, v242\n"
"v_exp_f32 v243, -v242\n"
"s_nop 1\n"
"v_mul_f32 v238, v238, v243\n"
"v_mul_f32 v8, v8, v243\n"
"v_mul_f32 v9, v9, v243\n"
"v_mul_f32 v10, v10, v243\n"
"v_mul_f32 v11, v11, v243\n"
"v_mul_f32 v20, v20, v243\n"
"v_mul_f32 v21, v21, v243\n"
"v_mul_f32 v22, v22, v243\n"
"v_mul_f32 v23, v23, v243\n"
"v_mul_f32 v32, v32, v243\n"
"v_mul_f32 v33, v33, v243\n"
"v_mul_f32 v34, v34, v243\n"
"v_mul_f32 v35, v35, v243\n"
"v_mul_f32 v44, v44, v243\n"
"v_mul_f32 v45, v45, v243\n"
"v_mul_f32 v46, v46, v243\n"
"v_mul_f32 v47, v47, v243\n"
"v_mul_f32 v56, v56, v243\n"
"v_mul_f32 v57, v57, v243\n"
"v_mul_f32 v58, v58, v243\n"
"v_mul_f32 v59, v59, v243\n"
"v_mul_f32 v68, v68, v243\n"
"v_mul_f32 v69, v69, v243\n"
"v_mul_f32 v70, v70, v243\n"
"v_mul_f32 v71, v71, v243\n"
"v_mul_f32 v80, v80, v243\n"
"v_mul_f32 v81, v81, v243\n"
"v_mul_f32 v82, v82, v243\n"
"v_mul_f32 v83, v83, v243\n"
"v_mul_f32 v92, v92, v243\n"
"v_mul_f32 v93, v93, v243\n"
"v_mul_f32 v94, v94, v243\n"
"v_mul_f32 v95, v95, v243\n"
"v_exp_f32 v152, v152\n"
"v_exp_f32 v153, v153\n"
"v_exp_f32 v154, v154\n"
"v_exp_f32 v155, v155\n"
"v_exp_f32 v164, v164\n"
"v_exp_f32 v165, v165\n"
"v_exp_f32 v166, v166\n"
"v_exp_f32 v167, v167\n"
"s_nop 1\n"
"v_cvt_pk_bf16_f32 v152, v152, v153\n"
"v_cvt_pk_bf16_f32 v153, v154, v155\n"
"v_cvt_pk_bf16_f32 v154, v164, v165\n"
"v_cvt_pk_bf16_f32 v155, v166, v167\n"
"v_mov_b32 v244, 0x3f803f80\n"
"v_mov_b32 v245, 0x3f803f80\n"
"v_mov_b32 v246, 0x3f803f80\n"
"v_mov_b32 v247, 0x3f803f80\n"
"s_nop 1\n"
"v_mfma_f32_16x16x32_bf16 v[156:159], v[244:247], v[144:147], 0\n"
"v_mfma_f32_16x16x32_bf16 v[160:163], v[244:247], v[148:151], 0\n"
"v_mfma_f32_16x16x32_bf16 v[164:167], v[244:247], v[152:155], 0\n"
"s_nop 7\n"
"s_nop 7\n"
"s_nop 3\n"
"s_branch .LrAby37\n"
".LrAy38:\n"
"s_nop 7\n"
"s_nop 7\n"
"v_bfe_u32 v243, v253, 4, 3\n"
"v_lshlrev_b32 v243, 8, v243\n"
"v_bfe_u32 v242, v253, 7, 1\n"
"v_lshl_add_u32 v243, v242, 11, v243\n"
"v_lshrrev_b32 v242, 8, v253\n"
"v_lshl_add_u32 v243, v242, 4, v243\n"
"s_and_b32 s70, s69, 15\n"
"s_lshr_b32 s71, s70, 0\n"
"s_lshl_b32 s71, s71, 2\n"
"s_and_b32 s70, s70, 0\n"
"s_or_b32 s71, s71, s70\n"
"s_lshl_b32 s71, s71, 11\n"
"s_lshr_b32 s70, s69, 4\n"
"s_lshl_b32 s70, s70, 17\n"
"s_add_u32 s71, s71, s70\n"
"s_add_u32 s70, s71, 0\n"
"buffer_load_dwordx4 v[244:247], v243, s[20:23], s70 offen offset:0\n"
"s_waitcnt vmcnt(0)\n"
"v_mfma_f32_16x16x32_bf16 v[168:171], v[244:247], v[96:99], v[192:195]\n"
"v_mfma_f32_16x16x32_bf16 v[172:175], v[244:247], v[112:115], v[196:199]\n"
"v_mfma_f32_16x16x32_bf16 v[176:179], v[244:247], v[128:131], v[200:203]\n"
"s_nop 7\n"
"buffer_load_dwordx4 v[244:247], v243, s[20:23], s70 offen offset:64\n"
"s_waitcnt vmcnt(0)\n"
"v_mfma_f32_16x16x32_bf16 v[168:171], v[244:247], v[100:103], v[168:171]\n"
"v_mfma_f32_16x16x32_bf16 v[172:175], v[244:247], v[116:119], v[172:175]\n"
"v_mfma_f32_16x16x32_bf16 v[176:179], v[244:247], v[132:135], v[176:179]\n"
"s_nop 7\n"
"buffer_load_dwordx4 v[244:247], v243, s[20:23], s70 offen offset:128\n"
"s_waitcnt vmcnt(0)\n"
"v_mfma_f32_16x16x32_bf16 v[168:171], v[244:247], v[104:107], v[168:171]\n"
"v_mfma_f32_16x16x32_bf16 v[172:175], v[244:247], v[120:123], v[172:175]\n"
"v_mfma_f32_16x16x32_bf16 v[176:179], v[244:247], v[136:139], v[176:179]\n"
"s_nop 7\n"
"buffer_load_dwordx4 v[244:247], v243, s[20:23], s70 offen offset:192\n"
"s_waitcnt vmcnt(0)\n"
"v_mfma_f32_16x16x32_bf16 v[168:171], v[244:247], v[108:111], v[168:171]\n"
"v_mfma_f32_16x16x32_bf16 v[172:175], v[244:247], v[124:127], v[172:175]\n"
"v_mfma_f32_16x16x32_bf16 v[176:179], v[244:247], v[140:143], v[176:179]\n"
"s_nop 7\n"
"s_add_u32 s70, s71, 4096\n"
"buffer_load_dwordx4 v[244:247], v243, s[20:23], s70 offen offset:0\n"
"s_waitcnt vmcnt(0)\n"
"v_mfma_f32_16x16x32_bf16 v[180:183], v[244:247], v[96:99], v[192:195]\n"
"v_mfma_f32_16x16x32_bf16 v[184:187], v[244:247], v[112:115], v[196:199]\n"
"v_mfma_f32_16x16x32_bf16 v[188:191], v[244:247], v[128:131], v[200:203]\n"
"s_nop 7\n"
"buffer_load_dwordx4 v[244:247], v243, s[20:23], s70 offen offset:64\n"
"s_waitcnt vmcnt(0)\n"
"v_mfma_f32_16x16x32_bf16 v[180:183], v[244:247], v[100:103], v[180:183]\n"
"v_mfma_f32_16x16x32_bf16 v[184:187], v[244:247], v[116:119], v[184:187]\n"
"v_mfma_f32_16x16x32_bf16 v[188:191], v[244:247], v[132:135], v[188:191]\n"
"s_nop 7\n"
"buffer_load_dwordx4 v[244:247], v243, s[20:23], s70 offen offset:128\n"
"s_waitcnt vmcnt(0)\n"
"v_mfma_f32_16x16x32_bf16 v[180:183], v[244:247], v[104:107], v[180:183]\n"
"v_mfma_f32_16x16x32_bf16 v[184:187], v[244:247], v[120:123], v[184:187]\n"
"v_mfma_f32_16x16x32_bf16 v[188:191], v[244:247], v[136:139], v[188:191]\n"
"s_nop 7\n"
"buffer_load_dwordx4 v[244:247], v243, s[20:23], s70 offen offset:192\n"
"s_waitcnt vmcnt(0)\n"
"v_mfma_f32_16x16x32_bf16 v[180:183], v[244:247], v[108:111], v[180:183]\n"
"v_mfma_f32_16x16x32_bf16 v[184:187], v[244:247], v[124:127], v[184:187]\n"
"v_mfma_f32_16x16x32_bf16 v[188:191], v[244:247], v[140:143], v[188:191]\n"
"s_nop 7\n"
"s_nop 7\n"
"s_nop 7\n"
"v_max3_f32 v242, v168, v169, v170\n"
"v_max3_f32 v242, v242, v171, v180\n"
"v_max3_f32 v242, v242, v181, v182\n"
"v_max_f32 v242, v242, v183\n"
"v_mov_b32 v243, v242\n"
"s_nop 1\n"
"v_permlane32_swap_b32 v242, v243\n"
"v_max_f32 v242, v242, v243\n"
"v_mov_b32 v243, v242\n"
"s_nop 1\n"
"v_permlane16_swap_b32 v242, v243\n"
"v_max_f32 v242, v242, v243\n"
"v_max_f32 v242, 0, v242\n"
"v_sub_f32 v168, v168, v242\n"
"v_sub_f32 v169, v169, v242\n"
"v_sub_f32 v170, v170, v242\n"
"v_sub_f32 v171, v171, v242\n"
"v_sub_f32 v180, v180, v242\n"
"v_sub_f32 v181, v181, v242\n"
"v_sub_f32 v182, v182, v242\n"
"v_sub_f32 v183, v183, v242\n"
"v_sub_f32 v192, v192, v242\n"
"v_sub_f32 v193, v193, v242\n"
"v_sub_f32 v194, v194, v242\n"
"v_sub_f32 v195, v195, v242\n"
"v_sub_f32 v144, v144, v242\n"
"v_sub_f32 v145, v145, v242\n"
"v_sub_f32 v146, v146, v242\n"
"v_sub_f32 v147, v147, v242\n"
"v_sub_f32 v156, v156, v242\n"
"v_sub_f32 v157, v157, v242\n"
"v_sub_f32 v158, v158, v242\n"
"v_sub_f32 v159, v159, v242\n"
"v_exp_f32 v243, -v242\n"
"s_nop 1\n"
"v_mul_f32 v236, v236, v243\n"
"v_mul_f32 v0, v0, v243\n"
"v_mul_f32 v1, v1, v243\n"
"v_mul_f32 v2, v2, v243\n"
"v_mul_f32 v3, v3, v243\n"
"v_mul_f32 v12, v12, v243\n"
"v_mul_f32 v13, v13, v243\n"
"v_mul_f32 v14, v14, v243\n"
"v_mul_f32 v15, v15, v243\n"
"v_mul_f32 v24, v24, v243\n"
"v_mul_f32 v25, v25, v243\n"
"v_mul_f32 v26, v26, v243\n"
"v_mul_f32 v27, v27, v243\n"
"v_mul_f32 v36, v36, v243\n"
"v_mul_f32 v37, v37, v243\n"
"v_mul_f32 v38, v38, v243\n"
"v_mul_f32 v39, v39, v243\n"
"v_mul_f32 v48, v48, v243\n"
"v_mul_f32 v49, v49, v243\n"
"v_mul_f32 v50, v50, v243\n"
"v_mul_f32 v51, v51, v243\n"
"v_mul_f32 v60, v60, v243\n"
"v_mul_f32 v61, v61, v243\n"
"v_mul_f32 v62, v62, v243\n"
"v_mul_f32 v63, v63, v243\n"
"v_mul_f32 v72, v72, v243\n"
"v_mul_f32 v73, v73, v243\n"
"v_mul_f32 v74, v74, v243\n"
"v_mul_f32 v75, v75, v243\n"
"v_mul_f32 v84, v84, v243\n"
"v_mul_f32 v85, v85, v243\n"
"v_mul_f32 v86, v86, v243\n"
"v_mul_f32 v87, v87, v243\n"
"v_exp_f32 v168, v168\n"
"v_exp_f32 v169, v169\n"
"v_exp_f32 v170, v170\n"
"v_exp_f32 v171, v171\n"
"v_exp_f32 v180, v180\n"
"v_exp_f32 v181, v181\n"
"v_exp_f32 v182, v182\n"
"v_exp_f32 v183, v183\n"
"s_nop 1\n"
"v_cvt_pk_bf16_f32 v168, v168, v169\n"
"v_cvt_pk_bf16_f32 v169, v170, v171\n"
"v_cvt_pk_bf16_f32 v170, v180, v181\n"
"v_cvt_pk_bf16_f32 v171, v182, v183\n"
"v_max3_f32 v242, v172, v173, v174\n"
"v_max3_f32 v242, v242, v175, v184\n"
"v_max3_f32 v242, v242, v185, v186\n"
"v_max_f32 v242, v242, v187\n"
"v_mov_b32 v243, v242\n"
"s_nop 1\n"
"v_permlane32_swap_b32 v242, v243\n"
"v_max_f32 v242, v242, v243\n"
"v_mov_b32 v243, v242\n"
"s_nop 1\n"
"v_permlane16_swap_b32 v242, v243\n"
"v_max_f32 v242, v242, v243\n"
"v_max_f32 v242, 0, v242\n"
"v_sub_f32 v172, v172, v242\n"
"v_sub_f32 v173, v173, v242\n"
"v_sub_f32 v174, v174, v242\n"
"v_sub_f32 v175, v175, v242\n"
"v_sub_f32 v184, v184, v242\n"
"v_sub_f32 v185, v185, v242\n"
"v_sub_f32 v186, v186, v242\n"
"v_sub_f32 v187, v187, v242\n"
"v_sub_f32 v196, v196, v242\n"
"v_sub_f32 v197, v197, v242\n"
"v_sub_f32 v198, v198, v242\n"
"v_sub_f32 v199, v199, v242\n"
"v_sub_f32 v148, v148, v242\n"
"v_sub_f32 v149, v149, v242\n"
"v_sub_f32 v150, v150, v242\n"
"v_sub_f32 v151, v151, v242\n"
"v_sub_f32 v160, v160, v242\n"
"v_sub_f32 v161, v161, v242\n"
"v_sub_f32 v162, v162, v242\n"
"v_sub_f32 v163, v163, v242\n"
"v_exp_f32 v243, -v242\n"
"s_nop 1\n"
"v_mul_f32 v237, v237, v243\n"
"v_mul_f32 v4, v4, v243\n"
"v_mul_f32 v5, v5, v243\n"
"v_mul_f32 v6, v6, v243\n"
"v_mul_f32 v7, v7, v243\n"
"v_mul_f32 v16, v16, v243\n"
"v_mul_f32 v17, v17, v243\n"
"v_mul_f32 v18, v18, v243\n"
"v_mul_f32 v19, v19, v243\n"
"v_mul_f32 v28, v28, v243\n"
"v_mul_f32 v29, v29, v243\n"
"v_mul_f32 v30, v30, v243\n"
"v_mul_f32 v31, v31, v243\n"
"v_mul_f32 v40, v40, v243\n"
"v_mul_f32 v41, v41, v243\n"
"v_mul_f32 v42, v42, v243\n"
"v_mul_f32 v43, v43, v243\n"
"v_mul_f32 v52, v52, v243\n"
"v_mul_f32 v53, v53, v243\n"
"v_mul_f32 v54, v54, v243\n"
"v_mul_f32 v55, v55, v243\n"
"v_mul_f32 v64, v64, v243\n"
"v_mul_f32 v65, v65, v243\n"
"v_mul_f32 v66, v66, v243\n"
"v_mul_f32 v67, v67, v243\n"
"v_mul_f32 v76, v76, v243\n"
"v_mul_f32 v77, v77, v243\n"
"v_mul_f32 v78, v78, v243\n"
"v_mul_f32 v79, v79, v243\n"
"v_mul_f32 v88, v88, v243\n"
"v_mul_f32 v89, v89, v243\n"
"v_mul_f32 v90, v90, v243\n"
"v_mul_f32 v91, v91, v243\n"
"v_exp_f32 v172, v172\n"
"v_exp_f32 v173, v173\n"
"v_exp_f32 v174, v174\n"
"v_exp_f32 v175, v175\n"
"v_exp_f32 v184, v184\n"
"v_exp_f32 v185, v185\n"
"v_exp_f32 v186, v186\n"
"v_exp_f32 v187, v187\n"
"s_nop 1\n"
"v_cvt_pk_bf16_f32 v172, v172, v173\n"
"v_cvt_pk_bf16_f32 v173, v174, v175\n"
"v_cvt_pk_bf16_f32 v174, v184, v185\n"
"v_cvt_pk_bf16_f32 v175, v186, v187\n"
"v_max3_f32 v242, v176, v177, v178\n"
"v_max3_f32 v242, v242, v179, v188\n"
"v_max3_f32 v242, v242, v189, v190\n"
"v_max_f32 v242, v242, v191\n"
"v_mov_b32 v243, v242\n"
"s_nop 1\n"
"v_permlane32_swap_b32 v242, v243\n"
"v_max_f32 v242, v242, v243\n"
"v_mov_b32 v243, v242\n"
"s_nop 1\n"
"v_permlane16_swap_b32 v242, v243\n"
"v_max_f32 v242, v242, v243\n"
"v_max_f32 v242, 0, v242\n"
"v_sub_f32 v176, v176, v242\n"
"v_sub_f32 v177, v177, v242\n"
"v_sub_f32 v178, v178, v242\n"
"v_sub_f32 v179, v179, v242\n"
"v_sub_f32 v188, v188, v242\n"
"v_sub_f32 v189, v189, v242\n"
"v_sub_f32 v190, v190, v242\n"
"v_sub_f32 v191, v191, v242\n"
"v_sub_f32 v200, v200, v242\n"
"v_sub_f32 v201, v201, v242\n"
"v_sub_f32 v202, v202, v242\n"
"v_sub_f32 v203, v203, v242\n"
"v_sub_f32 v152, v152, v242\n"
"v_sub_f32 v153, v153, v242\n"
"v_sub_f32 v154, v154, v242\n"
"v_sub_f32 v155, v155, v242\n"
"v_sub_f32 v164, v164, v242\n"
"v_sub_f32 v165, v165, v242\n"
"v_sub_f32 v166, v166, v242\n"
"v_sub_f32 v167, v167, v242\n"
"v_exp_f32 v243, -v242\n"
"s_nop 1\n"
"v_mul_f32 v238, v238, v243\n"
"v_mul_f32 v8, v8, v243\n"
"v_mul_f32 v9, v9, v243\n"
"v_mul_f32 v10, v10, v243\n"
"v_mul_f32 v11, v11, v243\n"
"v_mul_f32 v20, v20, v243\n"
"v_mul_f32 v21, v21, v243\n"
"v_mul_f32 v22, v22, v243\n"
"v_mul_f32 v23, v23, v243\n"
"v_mul_f32 v32, v32, v243\n"
"v_mul_f32 v33, v33, v243\n"
"v_mul_f32 v34, v34, v243\n"
"v_mul_f32 v35, v35, v243\n"
"v_mul_f32 v44, v44, v243\n"
"v_mul_f32 v45, v45, v243\n"
"v_mul_f32 v46, v46, v243\n"
"v_mul_f32 v47, v47, v243\n"
"v_mul_f32 v56, v56, v243\n"
"v_mul_f32 v57, v57, v243\n"
"v_mul_f32 v58, v58, v243\n"
"v_mul_f32 v59, v59, v243\n"
"v_mul_f32 v68, v68, v243\n"
"v_mul_f32 v69, v69, v243\n"
"v_mul_f32 v70, v70, v243\n"
"v_mul_f32 v71, v71, v243\n"
"v_mul_f32 v80, v80, v243\n"
"v_mul_f32 v81, v81, v243\n"
"v_mul_f32 v82, v82, v243\n"
"v_mul_f32 v83, v83, v243\n"
"v_mul_f32 v92, v92, v243\n"
"v_mul_f32 v93, v93, v243\n"
"v_mul_f32 v94, v94, v243\n"
"v_mul_f32 v95, v95, v243\n"
"v_exp_f32 v176, v176\n"
"v_exp_f32 v177, v177\n"
"v_exp_f32 v178, v178\n"
"v_exp_f32 v179, v179\n"
"v_exp_f32 v188, v188\n"
"v_exp_f32 v189, v189\n"
"v_exp_f32 v190, v190\n"
"v_exp_f32 v191, v191\n"
"s_nop 1\n"
"v_cvt_pk_bf16_f32 v176, v176, v177\n"
"v_cvt_pk_bf16_f32 v177, v178, v179\n"
"v_cvt_pk_bf16_f32 v178, v188, v189\n"
"v_cvt_pk_bf16_f32 v179, v190, v191\n"
"v_mov_b32 v244, 0x3f803f80\n"
"v_mov_b32 v245, 0x3f803f80\n"
"v_mov_b32 v246, 0x3f803f80\n"
"v_mov_b32 v247, 0x3f803f80\n"
"s_nop 1\n"
"v_mfma_f32_16x16x32_bf16 v[180:183], v[244:247], v[168:171], 0\n"
"v_mfma_f32_16x16x32_bf16 v[184:187], v[244:247], v[172:175], 0\n"
"v_mfma_f32_16x16x32_bf16 v[188:191], v[244:247], v[176:179], 0\n"
"s_nop 7\n"
"s_nop 7\n"
"s_nop 3\n"
"s_branch .LrAby38\n"
".LrAy39:\n"
"s_nop 7\n"
"s_nop 7\n"
"v_bfe_u32 v243, v253, 4, 3\n"
"v_lshlrev_b32 v243, 8, v243\n"
"v_bfe_u32 v242, v253, 7, 1\n"
"v_lshl_add_u32 v243, v242, 11, v243\n"
"v_lshrrev_b32 v242, 8, v253\n"
"v_lshl_add_u32 v243, v242, 4, v243\n"
"s_and_b32 s70, s69, 15\n"
"s_lshr_b32 s71, s70, 0\n"
"s_lshl_b32 s71, s71, 2\n"
"s_and_b32 s70, s70, 0\n"
"s_or_b32 s71, s71, s70\n"
"s_lshl_b32 s71, s71, 11\n"
"s_lshr_b32 s70, s69, 4\n"
"s_lshl_b32 s70, s70, 17\n"
"s_add_u32 s71, s71, s70\n"
"s_add_u32 s70, s71, 0\n"
"buffer_load_dwordx4 v[244:247], v243, s[20:23], s70 offen offset:0\n"
"s_waitcnt vmcnt(0)\n"
"v_mfma_f32_16x16x32_bf16 v[144:147], v[244:247], v[96:99], v[192:195]\n"
"v_mfma_f32_16x16x32_bf16 v[148:151], v[244:247], v[112:115], v[196:199]\n"
"v_mfma_f32_16x16x32_bf16 v[152:155], v[244:247], v[128:131], v[200:203]\n"
"s_nop 7\n"
"buffer_load_dwordx4 v[244:247], v243, s[20:23], s70 offen offset:64\n"
"s_waitcnt vmcnt(0)\n"
"v_mfma_f32_16x16x32_bf16 v[144:147], v[244:247], v[100:103], v[144:147]\n"
"v_mfma_f32_16x16x32_bf16 v[148:151], v[244:247], v[116:119], v[148:151]\n"
"v_mfma_f32_16x16x32_bf16 v[152:155], v[244:247], v[132:135], v[152:155]\n"
"s_nop 7\n"
"buffer_load_dwordx4 v[244:247], v243, s[20:23], s70 offen offset:128\n"
"s_waitcnt vmcnt(0)\n"
"v_mfma_f32_16x16x32_bf16 v[144:147], v[244:247], v[104:107], v[144:147]\n"
"v_mfma_f32_16x16x32_bf16 v[148:151], v[244:247], v[120:123], v[148:151]\n"
"v_mfma_f32_16x16x32_bf16 v[152:155], v[244:247], v[136:139], v[152:155]\n"
"s_nop 7\n"
"buffer_load_dwordx4 v[244:247], v243, s[20:23], s70 offen offset:192\n"
"s_waitcnt vmcnt(0)\n"
"v_mfma_f32_16x16x32_bf16 v[144:147], v[244:247], v[108:111], v[144:147]\n"
"v_mfma_f32_16x16x32_bf16 v[148:151], v[244:247], v[124:127], v[148:151]\n"
"v_mfma_f32_16x16x32_bf16 v[152:155], v[244:247], v[140:143], v[152:155]\n"
"s_nop 7\n"
"s_add_u32 s70, s71, 4096\n"
"buffer_load_dwordx4 v[244:247], v243, s[20:23], s70 offen offset:0\n"
"s_waitcnt vmcnt(0)\n"
"v_mfma_f32_16x16x32_bf16 v[156:159], v[244:247], v[96:99], v[192:195]\n"
"v_mfma_f32_16x16x32_bf16 v[160:163], v[244:247], v[112:115], v[196:199]\n"
"v_mfma_f32_16x16x32_bf16 v[164:167], v[244:247], v[128:131], v[200:203]\n"
"s_nop 7\n"
"buffer_load_dwordx4 v[244:247], v243, s[20:23], s70 offen offset:64\n"
"s_waitcnt vmcnt(0)\n"
"v_mfma_f32_16x16x32_bf16 v[156:159], v[244:247], v[100:103], v[156:159]\n"
"v_mfma_f32_16x16x32_bf16 v[160:163], v[244:247], v[116:119], v[160:163]\n"
"v_mfma_f32_16x16x32_bf16 v[164:167], v[244:247], v[132:135], v[164:167]\n"
"s_nop 7\n"
"buffer_load_dwordx4 v[244:247], v243, s[20:23], s70 offen offset:128\n"
"s_waitcnt vmcnt(0)\n"
"v_mfma_f32_16x16x32_bf16 v[156:159], v[244:247], v[104:107], v[156:159]\n"
"v_mfma_f32_16x16x32_bf16 v[160:163], v[244:247], v[120:123], v[160:163]\n"
"v_mfma_f32_16x16x32_bf16 v[164:167], v[244:247], v[136:139], v[164:167]\n"
"s_nop 7\n"
"buffer_load_dwordx4 v[244:247], v243, s[20:23], s70 offen offset:192\n"
"s_waitcnt vmcnt(0)\n"
"v_mfma_f32_16x16x32_bf16 v[156:159], v[244:247], v[108:111], v[156:159]\n"
"v_mfma_f32_16x16x32_bf16 v[160:163], v[244:247], v[124:127], v[160:163]\n"
"v_mfma_f32_16x16x32_bf16 v[164:167], v[244:247], v[140:143], v[164:167]\n"
"s_nop 7\n"
"s_nop 7\n"
"s_nop 7\n"
"v_max3_f32 v242, v144, v145, v146\n"
"v_max3_f32 v242, v242, v147, v156\n"
"v_max3_f32 v242, v242, v157, v158\n"
"v_max_f32 v242, v242, v159\n"
"v_mov_b32 v243, v242\n"
"s_nop 1\n"
"v_permlane32_swap_b32 v242, v243\n"
"v_max_f32 v242, v242, v243\n"
"v_mov_b32 v243, v242\n"
"s_nop 1\n"
"v_permlane16_swap_b32 v242, v243\n"
"v_max_f32 v242, v242, v243\n"
"v_max_f32 v242, 0, v242\n"
"v_sub_f32 v144, v144, v242\n"
"v_sub_f32 v145, v145, v242\n"
"v_sub_f32 v146, v146, v242\n"
"v_sub_f32 v147, v147, v242\n"
"v_sub_f32 v156, v156, v242\n"
"v_sub_f32 v157, v157, v242\n"
"v_sub_f32 v158, v158, v242\n"
"v_sub_f32 v159, v159, v242\n"
"v_sub_f32 v192, v192, v242\n"
"v_sub_f32 v193, v193, v242\n"
"v_sub_f32 v194, v194, v242\n"
"v_sub_f32 v195, v195, v242\n"
"v_sub_f32 v168, v168, v242\n"
"v_sub_f32 v169, v169, v242\n"
"v_sub_f32 v170, v170, v242\n"
"v_sub_f32 v171, v171, v242\n"
"v_sub_f32 v180, v180, v242\n"
"v_sub_f32 v181, v181, v242\n"
"v_sub_f32 v182, v182, v242\n"
"v_sub_f32 v183, v183, v242\n"
"v_exp_f32 v243, -v242\n"
"s_nop 1\n"
"v_mul_f32 v236, v236, v243\n"
"v_mul_f32 v0, v0, v243\n"
"v_mul_f32 v1, v1, v243\n"
"v_mul_f32 v2, v2, v243\n"
"v_mul_f32 v3, v3, v243\n"
"v_mul_f32 v12, v12, v243\n"
"v_mul_f32 v13, v13, v243\n"
"v_mul_f32 v14, v14, v243\n"
"v_mul_f32 v15, v15, v243\n"
"v_mul_f32 v24, v24, v243\n"
"v_mul_f32 v25, v25, v243\n"
"v_mul_f32 v26, v26, v243\n"
"v_mul_f32 v27, v27, v243\n"
"v_mul_f32 v36, v36, v243\n"
"v_mul_f32 v37, v37, v243\n"
"v_mul_f32 v38, v38, v243\n"
"v_mul_f32 v39, v39, v243\n"
"v_mul_f32 v48, v48, v243\n"
"v_mul_f32 v49, v49, v243\n"
"v_mul_f32 v50, v50, v243\n"
"v_mul_f32 v51, v51, v243\n"
"v_mul_f32 v60, v60, v243\n"
"v_mul_f32 v61, v61, v243\n"
"v_mul_f32 v62, v62, v243\n"
"v_mul_f32 v63, v63, v243\n"
"v_mul_f32 v72, v72, v243\n"
"v_mul_f32 v73, v73, v243\n"
"v_mul_f32 v74, v74, v243\n"
"v_mul_f32 v75, v75, v243\n"
"v_mul_f32 v84, v84, v243\n"
"v_mul_f32 v85, v85, v243\n"
"v_mul_f32 v86, v86, v243\n"
"v_mul_f32 v87, v87, v243\n"
"v_exp_f32 v144, v144\n"
"v_exp_f32 v145, v145\n"
"v_exp_f32 v146, v146\n"
"v_exp_f32 v147, v147\n"
"v_exp_f32 v156, v156\n"
"v_exp_f32 v157, v157\n"
"v_exp_f32 v158, v158\n"
"v_exp_f32 v159, v159\n"
"s_nop 1\n"
"v_cvt_pk_bf16_f32 v144, v144, v145\n"
"v_cvt_pk_bf16_f32 v145, v146, v147\n"
"v_cvt_pk_bf16_f32 v146, v156, v157\n"
"v_cvt_pk_bf16_f32 v147, v158, v159\n"
"v_max3_f32 v242, v148, v149, v150\n"
"v_max3_f32 v242, v242, v151, v160\n"
"v_max3_f32 v242, v242, v161, v162\n"
"v_max_f32 v242, v242, v163\n"
"v_mov_b32 v243, v242\n"
"s_nop 1\n"
"v_permlane32_swap_b32 v242, v243\n"
"v_max_f32 v242, v242, v243\n"
"v_mov_b32 v243, v242\n"
"s_nop 1\n"
"v_permlane16_swap_b32 v242, v243\n"
"v_max_f32 v242, v242, v243\n"
"v_max_f32 v242, 0, v242\n"
"v_sub_f32 v148, v148, v242\n"
"v_sub_f32 v149, v149, v242\n"
"v_sub_f32 v150, v150, v242\n"
"v_sub_f32 v151, v151, v242\n"
"v_sub_f32 v160, v160, v242\n"
"v_sub_f32 v161, v161, v242\n"
"v_sub_f32 v162, v162, v242\n"
"v_sub_f32 v163, v163, v242\n"
"v_sub_f32 v196, v196, v242\n"
"v_sub_f32 v197, v197, v242\n"
"v_sub_f32 v198, v198, v242\n"
"v_sub_f32 v199, v199, v242\n"
"v_sub_f32 v172, v172, v242\n"
"v_sub_f32 v173, v173, v242\n"
"v_sub_f32 v174, v174, v242\n"
"v_sub_f32 v175, v175, v242\n"
"v_sub_f32 v184, v184, v242\n"
"v_sub_f32 v185, v185, v242\n"
"v_sub_f32 v186, v186, v242\n"
"v_sub_f32 v187, v187, v242\n"
"v_exp_f32 v243, -v242\n"
"s_nop 1\n"
"v_mul_f32 v237, v237, v243\n"
"v_mul_f32 v4, v4, v243\n"
"v_mul_f32 v5, v5, v243\n"
"v_mul_f32 v6, v6, v243\n"
"v_mul_f32 v7, v7, v243\n"
"v_mul_f32 v16, v16, v243\n"
"v_mul_f32 v17, v17, v243\n"
"v_mul_f32 v18, v18, v243\n"
"v_mul_f32 v19, v19, v243\n"
"v_mul_f32 v28, v28, v243\n"
"v_mul_f32 v29, v29, v243\n"
"v_mul_f32 v30, v30, v243\n"
"v_mul_f32 v31, v31, v243\n"
"v_mul_f32 v40, v40, v243\n"
"v_mul_f32 v41, v41, v243\n"
"v_mul_f32 v42, v42, v243\n"
"v_mul_f32 v43, v43, v243\n"
"v_mul_f32 v52, v52, v243\n"
"v_mul_f32 v53, v53, v243\n"
"v_mul_f32 v54, v54, v243\n"
"v_mul_f32 v55, v55, v243\n"
"v_mul_f32 v64, v64, v243\n"
"v_mul_f32 v65, v65, v243\n"
"v_mul_f32 v66, v66, v243\n"
"v_mul_f32 v67, v67, v243\n"
"v_mul_f32 v76, v76, v243\n"
"v_mul_f32 v77, v77, v243\n"
"v_mul_f32 v78, v78, v243\n"
"v_mul_f32 v79, v79, v243\n"
"v_mul_f32 v88, v88, v243\n"
"v_mul_f32 v89, v89, v243\n"
"v_mul_f32 v90, v90, v243\n"
"v_mul_f32 v91, v91, v243\n"
"v_exp_f32 v148, v148\n"
"v_exp_f32 v149, v149\n"
"v_exp_f32 v150, v150\n"
"v_exp_f32 v151, v151\n"
"v_exp_f32 v160, v160\n"
"v_exp_f32 v161, v161\n"
"v_exp_f32 v162, v162\n"
"v_exp_f32 v163, v163\n"
"s_nop 1\n"
"v_cvt_pk_bf16_f32 v148, v148, v149\n"
"v_cvt_pk_bf16_f32 v149, v150, v151\n"
"v_cvt_pk_bf16_f32 v150, v160, v161\n"
"v_cvt_pk_bf16_f32 v151, v162, v163\n"
"v_max3_f32 v242, v152, v153, v154\n"
"v_max3_f32 v242, v242, v155, v164\n"
"v_max3_f32 v242, v242, v165, v166\n"
"v_max_f32 v242, v242, v167\n"
"v_mov_b32 v243, v242\n"
"s_nop 1\n"
"v_permlane32_swap_b32 v242, v243\n"
"v_max_f32 v242, v242, v243\n"
"v_mov_b32 v243, v242\n"
"s_nop 1\n"
"v_permlane16_swap_b32 v242, v243\n"
"v_max_f32 v242, v242, v243\n"
"v_max_f32 v242, 0, v242\n"
"v_sub_f32 v152, v152, v242\n"
"v_sub_f32 v153, v153, v242\n"
"v_sub_f32 v154, v154, v242\n"
"v_sub_f32 v155, v155, v242\n"
"v_sub_f32 v164, v164, v242\n"
"v_sub_f32 v165, v165, v242\n"
"v_sub_f32 v166, v166, v242\n"
"v_sub_f32 v167, v167, v242\n"
"v_sub_f32 v200, v200, v242\n"
"v_sub_f32 v201, v201, v242\n"
"v_sub_f32 v202, v202, v242\n"
"v_sub_f32 v203, v203, v242\n"
"v_sub_f32 v176, v176, v242\n"
"v_sub_f32 v177, v177, v242\n"
"v_sub_f32 v178, v178, v242\n"
"v_sub_f32 v179, v179, v242\n"
"v_sub_f32 v188, v188, v242\n"
"v_sub_f32 v189, v189, v242\n"
"v_sub_f32 v190, v190, v242\n"
"v_sub_f32 v191, v191, v242\n"
"v_exp_f32 v243, -v242\n"
"s_nop 1\n"
"v_mul_f32 v238, v238, v243\n"
"v_mul_f32 v8, v8, v243\n"
"v_mul_f32 v9, v9, v243\n"
"v_mul_f32 v10, v10, v243\n"
"v_mul_f32 v11, v11, v243\n"
"v_mul_f32 v20, v20, v243\n"
"v_mul_f32 v21, v21, v243\n"
"v_mul_f32 v22, v22, v243\n"
"v_mul_f32 v23, v23, v243\n"
"v_mul_f32 v32, v32, v243\n"
"v_mul_f32 v33, v33, v243\n"
"v_mul_f32 v34, v34, v243\n"
"v_mul_f32 v35, v35, v243\n"
"v_mul_f32 v44, v44, v243\n"
"v_mul_f32 v45, v45, v243\n"
"v_mul_f32 v46, v46, v243\n"
"v_mul_f32 v47, v47, v243\n"
"v_mul_f32 v56, v56, v243\n"
"v_mul_f32 v57, v57, v243\n"
"v_mul_f32 v58, v58, v243\n"
"v_mul_f32 v59, v59, v243\n"
"v_mul_f32 v68, v68, v243\n"
"v_mul_f32 v69, v69, v243\n"
"v_mul_f32 v70, v70, v243\n"
"v_mul_f32 v71, v71, v243\n"
"v_mul_f32 v80, v80, v243\n"
"v_mul_f32 v81, v81, v243\n"
"v_mul_f32 v82, v82, v243\n"
"v_mul_f32 v83, v83, v243\n"
"v_mul_f32 v92, v92, v243\n"
"v_mul_f32 v93, v93, v243\n"
"v_mul_f32 v94, v94, v243\n"
"v_mul_f32 v95, v95, v243\n"
"v_exp_f32 v152, v152\n"
"v_exp_f32 v153, v153\n"
"v_exp_f32 v154, v154\n"
"v_exp_f32 v155, v155\n"
"v_exp_f32 v164, v164\n"
"v_exp_f32 v165, v165\n"
"v_exp_f32 v166, v166\n"
"v_exp_f32 v167, v167\n"
"s_nop 1\n"
"v_cvt_pk_bf16_f32 v152, v152, v153\n"
"v_cvt_pk_bf16_f32 v153, v154, v155\n"
"v_cvt_pk_bf16_f32 v154, v164, v165\n"
"v_cvt_pk_bf16_f32 v155, v166, v167\n"
"v_mov_b32 v244, 0x3f803f80\n"
"v_mov_b32 v245, 0x3f803f80\n"
"v_mov_b32 v246, 0x3f803f80\n"
"v_mov_b32 v247, 0x3f803f80\n"
"s_nop 1\n"
"v_mfma_f32_16x16x32_bf16 v[156:159], v[244:247], v[144:147], 0\n"
"v_mfma_f32_16x16x32_bf16 v[160:163], v[244:247], v[148:151], 0\n"
"v_mfma_f32_16x16x32_bf16 v[164:167], v[244:247], v[152:155], 0\n"
"s_nop 7\n"
"s_nop 7\n"
"s_nop 3\n"
"s_branch .LrAby39\n"
".LrAy40:\n"
"s_nop 7\n"
"s_nop 7\n"
"v_bfe_u32 v243, v253, 4, 3\n"
"v_lshlrev_b32 v243, 8, v243\n"
"v_bfe_u32 v242, v253, 7, 1\n"
"v_lshl_add_u32 v243, v242, 11, v243\n"
"v_lshrrev_b32 v242, 8, v253\n"
"v_lshl_add_u32 v243, v242, 4, v243\n"
"s_and_b32 s70, s69, 15\n"
"s_lshr_b32 s71, s70, 0\n"
"s_lshl_b32 s71, s71, 2\n"
"s_and_b32 s70, s70, 0\n"
"s_or_b32 s71, s71, s70\n"
"s_lshl_b32 s71, s71, 11\n"
"s_lshr_b32 s70, s69, 4\n"
"s_lshl_b32 s70, s70, 17\n"
"s_add_u32 s71, s71, s70\n"
"s_add_u32 s70, s71, 0\n"
"buffer_load_dwordx4 v[244:247], v243, s[20:23], s70 offen offset:0\n"
"s_waitcnt vmcnt(0)\n"
"v_mfma_f32_16x16x32_bf16 v[168:171], v[244:247], v[96:99], v[192:195]\n"
"v_mfma_f32_16x16x32_bf16 v[172:175], v[244:247], v[112:115], v[196:199]\n"
"v_mfma_f32_16x16x32_bf16 v[176:179], v[244:247], v[128:131], v[200:203]\n"
"s_nop 7\n"
"buffer_load_dwordx4 v[244:247], v243, s[20:23], s70 offen offset:64\n"
"s_waitcnt vmcnt(0)\n"
"v_mfma_f32_16x16x32_bf16 v[168:171], v[244:247], v[100:103], v[168:171]\n"
"v_mfma_f32_16x16x32_bf16 v[172:175], v[244:247], v[116:119], v[172:175]\n"
"v_mfma_f32_16x16x32_bf16 v[176:179], v[244:247], v[132:135], v[176:179]\n"
"s_nop 7\n"
"buffer_load_dwordx4 v[244:247], v243, s[20:23], s70 offen offset:128\n"
"s_waitcnt vmcnt(0)\n"
"v_mfma_f32_16x16x32_bf16 v[168:171], v[244:247], v[104:107], v[168:171]\n"
"v_mfma_f32_16x16x32_bf16 v[172:175], v[244:247], v[120:123], v[172:175]\n"
"v_mfma_f32_16x16x32_bf16 v[176:179], v[244:247], v[136:139], v[176:179]\n"
"s_nop 7\n"
"buffer_load_dwordx4 v[244:247], v243, s[20:23], s70 offen offset:192\n"
"s_waitcnt vmcnt(0)\n"
"v_mfma_f32_16x16x32_bf16 v[168:171], v[244:247], v[108:111], v[168:171]\n"
"v_mfma_f32_16x16x32_bf16 v[172:175], v[244:247], v[124:127], v[172:175]\n"
"v_mfma_f32_16x16x32_bf16 v[176:179], v[244:247], v[140:143], v[176:179]\n"
"s_nop 7\n"
"s_add_u32 s70, s71, 4096\n"
"buffer_load_dwordx4 v[244:247], v243, s[20:23], s70 offen offset:0\n"
"s_waitcnt vmcnt(0)\n"
"v_mfma_f32_16x16x32_bf16 v[180:183], v[244:247], v[96:99], v[192:195]\n"
"v_mfma_f32_16x16x32_bf16 v[184:187], v[244:247], v[112:115], v[196:199]\n"
"v_mfma_f32_16x16x32_bf16 v[188:191], v[244:247], v[128:131], v[200:203]\n"
"s_nop 7\n"
"buffer_load_dwordx4 v[244:247], v243, s[20:23], s70 offen offset:64\n"
"s_waitcnt vmcnt(0)\n"
"v_mfma_f32_16x16x32_bf16 v[180:183], v[244:247], v[100:103], v[180:183]\n"
"v_mfma_f32_16x16x32_bf16 v[184:187], v[244:247], v[116:119], v[184:187]\n"
"v_mfma_f32_16x16x32_bf16 v[188:191], v[244:247], v[132:135], v[188:191]\n"
"s_nop 7\n"
"buffer_load_dwordx4 v[244:247], v243, s[20:23], s70 offen offset:128\n"
"s_waitcnt vmcnt(0)\n"
"v_mfma_f32_16x16x32_bf16 v[180:183], v[244:247], v[104:107], v[180:183]\n"
"v_mfma_f32_16x16x32_bf16 v[184:187], v[244:247], v[120:123], v[184:187]\n"
"v_mfma_f32_16x16x32_bf16 v[188:191], v[244:247], v[136:139], v[188:191]\n"
"s_nop 7\n"
"buffer_load_dwordx4 v[244:247], v243, s[20:23], s70 offen offset:192\n"
"s_waitcnt vmcnt(0)\n"
"v_mfma_f32_16x16x32_bf16 v[180:183], v[244:247], v[108:111], v[180:183]\n"
"v_mfma_f32_16x16x32_bf16 v[184:187], v[244:247], v[124:127], v[184:187]\n"
"v_mfma_f32_16x16x32_bf16 v[188:191], v[244:247], v[140:143], v[188:191]\n"
"s_nop 7\n"
"s_nop 7\n"
"s_nop 7\n"
"v_max3_f32 v242, v168, v169, v170\n"
"v_max3_f32 v242, v242, v171, v180\n"
"v_max3_f32 v242, v242, v181, v182\n"
"v_max_f32 v242, v242, v183\n"
"v_mov_b32 v243, v242\n"
"s_nop 1\n"
"v_permlane32_swap_b32 v242, v243\n"
"v_max_f32 v242, v242, v243\n"
"v_mov_b32 v243, v242\n"
"s_nop 1\n"
"v_permlane16_swap_b32 v242, v243\n"
"v_max_f32 v242, v242, v243\n"
"v_max_f32 v242, 0, v242\n"
"v_sub_f32 v168, v168, v242\n"
"v_sub_f32 v169, v169, v242\n"
"v_sub_f32 v170, v170, v242\n"
"v_sub_f32 v171, v171, v242\n"
"v_sub_f32 v180, v180, v242\n"
"v_sub_f32 v181, v181, v242\n"
"v_sub_f32 v182, v182, v242\n"
"v_sub_f32 v183, v183, v242\n"
"v_sub_f32 v192, v192, v242\n"
"v_sub_f32 v193, v193, v242\n"
"v_sub_f32 v194, v194, v242\n"
"v_sub_f32 v195, v195, v242\n"
"v_sub_f32 v144, v144, v242\n"
"v_sub_f32 v145, v145, v242\n"
"v_sub_f32 v146, v146, v242\n"
"v_sub_f32 v147, v147, v242\n"
"v_sub_f32 v156, v156, v242\n"
"v_sub_f32 v157, v157, v242\n"
"v_sub_f32 v158, v158, v242\n"
"v_sub_f32 v159, v159, v242\n"
"v_exp_f32 v243, -v242\n"
"s_nop 1\n"
"v_mul_f32 v236, v236, v243\n"
"v_mul_f32 v0, v0, v243\n"
"v_mul_f32 v1, v1, v243\n"
"v_mul_f32 v2, v2, v243\n"
"v_mul_f32 v3, v3, v243\n"
"v_mul_f32 v12, v12, v243\n"
"v_mul_f32 v13, v13, v243\n"
"v_mul_f32 v14, v14, v243\n"
"v_mul_f32 v15, v15, v243\n"
"v_mul_f32 v24, v24, v243\n"
"v_mul_f32 v25, v25, v243\n"
"v_mul_f32 v26, v26, v243\n"
"v_mul_f32 v27, v27, v243\n"
"v_mul_f32 v36, v36, v243\n"
"v_mul_f32 v37, v37, v243\n"
"v_mul_f32 v38, v38, v243\n"
"v_mul_f32 v39, v39, v243\n"
"v_mul_f32 v48, v48, v243\n"
"v_mul_f32 v49, v49, v243\n"
"v_mul_f32 v50, v50, v243\n"
"v_mul_f32 v51, v51, v243\n"
"v_mul_f32 v60, v60, v243\n"
"v_mul_f32 v61, v61, v243\n"
"v_mul_f32 v62, v62, v243\n"
"v_mul_f32 v63, v63, v243\n"
"v_mul_f32 v72, v72, v243\n"
"v_mul_f32 v73, v73, v243\n"
"v_mul_f32 v74, v74, v243\n"
"v_mul_f32 v75, v75, v243\n"
"v_mul_f32 v84, v84, v243\n"
"v_mul_f32 v85, v85, v243\n"
"v_mul_f32 v86, v86, v243\n"
"v_mul_f32 v87, v87, v243\n"
"v_exp_f32 v168, v168\n"
"v_exp_f32 v169, v169\n"
"v_exp_f32 v170, v170\n"
"v_exp_f32 v171, v171\n"
"v_exp_f32 v180, v180\n"
"v_exp_f32 v181, v181\n"
"v_exp_f32 v182, v182\n"
"v_exp_f32 v183, v183\n"
"s_nop 1\n"
"v_cvt_pk_bf16_f32 v168, v168, v169\n"
"v_cvt_pk_bf16_f32 v169, v170, v171\n"
"v_cvt_pk_bf16_f32 v170, v180, v181\n"
"v_cvt_pk_bf16_f32 v171, v182, v183\n"
"v_max3_f32 v242, v172, v173, v174\n"
"v_max3_f32 v242, v242, v175, v184\n"
"v_max3_f32 v242, v242, v185, v186\n"
"v_max_f32 v242, v242, v187\n"
"v_mov_b32 v243, v242\n"
"s_nop 1\n"
"v_permlane32_swap_b32 v242, v243\n"
"v_max_f32 v242, v242, v243\n"
"v_mov_b32 v243, v242\n"
"s_nop 1\n"
"v_permlane16_swap_b32 v242, v243\n"
"v_max_f32 v242, v242, v243\n"
"v_max_f32 v242, 0, v242\n"
"v_sub_f32 v172, v172, v242\n"
"v_sub_f32 v173, v173, v242\n"
"v_sub_f32 v174, v174, v242\n"
"v_sub_f32 v175, v175, v242\n"
"v_sub_f32 v184, v184, v242\n"
"v_sub_f32 v185, v185, v242\n"
"v_sub_f32 v186, v186, v242\n"
"v_sub_f32 v187, v187, v242\n"
"v_sub_f32 v196, v196, v242\n"
"v_sub_f32 v197, v197, v242\n"
"v_sub_f32 v198, v198, v242\n"
"v_sub_f32 v199, v199, v242\n"
"v_sub_f32 v148, v148, v242\n"
"v_sub_f32 v149, v149, v242\n"
"v_sub_f32 v150, v150, v242\n"
"v_sub_f32 v151, v151, v242\n"
"v_sub_f32 v160, v160, v242\n"
"v_sub_f32 v161, v161, v242\n"
"v_sub_f32 v162, v162, v242\n"
"v_sub_f32 v163, v163, v242\n"
"v_exp_f32 v243, -v242\n"
"s_nop 1\n"
"v_mul_f32 v237, v237, v243\n"
"v_mul_f32 v4, v4, v243\n"
"v_mul_f32 v5, v5, v243\n"
"v_mul_f32 v6, v6, v243\n"
"v_mul_f32 v7, v7, v243\n"
"v_mul_f32 v16, v16, v243\n"
"v_mul_f32 v17, v17, v243\n"
"v_mul_f32 v18, v18, v243\n"
"v_mul_f32 v19, v19, v243\n"
"v_mul_f32 v28, v28, v243\n"
"v_mul_f32 v29, v29, v243\n"
"v_mul_f32 v30, v30, v243\n"
"v_mul_f32 v31, v31, v243\n"
"v_mul_f32 v40, v40, v243\n"
"v_mul_f32 v41, v41, v243\n"
"v_mul_f32 v42, v42, v243\n"
"v_mul_f32 v43, v43, v243\n"
"v_mul_f32 v52, v52, v243\n"
"v_mul_f32 v53, v53, v243\n"
"v_mul_f32 v54, v54, v243\n"
"v_mul_f32 v55, v55, v243\n"
"v_mul_f32 v64, v64, v243\n"
"v_mul_f32 v65, v65, v243\n"
"v_mul_f32 v66, v66, v243\n"
"v_mul_f32 v67, v67, v243\n"
"v_mul_f32 v76, v76, v243\n"
"v_mul_f32 v77, v77, v243\n"
"v_mul_f32 v78, v78, v243\n"
"v_mul_f32 v79, v79, v243\n"
"v_mul_f32 v88, v88, v243\n"
"v_mul_f32 v89, v89, v243\n"
"v_mul_f32 v90, v90, v243\n"
"v_mul_f32 v91, v91, v243\n"
"v_exp_f32 v172, v172\n"
"v_exp_f32 v173, v173\n"
"v_exp_f32 v174, v174\n"
"v_exp_f32 v175, v175\n"
"v_exp_f32 v184, v184\n"
"v_exp_f32 v185, v185\n"
"v_exp_f32 v186, v186\n"
"v_exp_f32 v187, v187\n"
"s_nop 1\n"
"v_cvt_pk_bf16_f32 v172, v172, v173\n"
"v_cvt_pk_bf16_f32 v173, v174, v175\n"
"v_cvt_pk_bf16_f32 v174, v184, v185\n"
"v_cvt_pk_bf16_f32 v175, v186, v187\n"
"v_max3_f32 v242, v176, v177, v178\n"
"v_max3_f32 v242, v242, v179, v188\n"
"v_max3_f32 v242, v242, v189, v190\n"
"v_max_f32 v242, v242, v191\n"
"v_mov_b32 v243, v242\n"
"s_nop 1\n"
"v_permlane32_swap_b32 v242, v243\n"
"v_max_f32 v242, v242, v243\n"
"v_mov_b32 v243, v242\n"
"s_nop 1\n"
"v_permlane16_swap_b32 v242, v243\n"
"v_max_f32 v242, v242, v243\n"
"v_max_f32 v242, 0, v242\n"
"v_sub_f32 v176, v176, v242\n"
"v_sub_f32 v177, v177, v242\n"
"v_sub_f32 v178, v178, v242\n"
"v_sub_f32 v179, v179, v242\n"
"v_sub_f32 v188, v188, v242\n"
"v_sub_f32 v189, v189, v242\n"
"v_sub_f32 v190, v190, v242\n"
"v_sub_f32 v191, v191, v242\n"
"v_sub_f32 v200, v200, v242\n"
"v_sub_f32 v201, v201, v242\n"
"v_sub_f32 v202, v202, v242\n"
"v_sub_f32 v203, v203, v242\n"
"v_sub_f32 v152, v152, v242\n"
"v_sub_f32 v153, v153, v242\n"
"v_sub_f32 v154, v154, v242\n"
"v_sub_f32 v155, v155, v242\n"
"v_sub_f32 v164, v164, v242\n"
"v_sub_f32 v165, v165, v242\n"
"v_sub_f32 v166, v166, v242\n"
"v_sub_f32 v167, v167, v242\n"
"v_exp_f32 v243, -v242\n"
"s_nop 1\n"
"v_mul_f32 v238, v238, v243\n"
"v_mul_f32 v8, v8, v243\n"
"v_mul_f32 v9, v9, v243\n"
"v_mul_f32 v10, v10, v243\n"
"v_mul_f32 v11, v11, v243\n"
"v_mul_f32 v20, v20, v243\n"
"v_mul_f32 v21, v21, v243\n"
"v_mul_f32 v22, v22, v243\n"
"v_mul_f32 v23, v23, v243\n"
"v_mul_f32 v32, v32, v243\n"
"v_mul_f32 v33, v33, v243\n"
"v_mul_f32 v34, v34, v243\n"
"v_mul_f32 v35, v35, v243\n"
"v_mul_f32 v44, v44, v243\n"
"v_mul_f32 v45, v45, v243\n"
"v_mul_f32 v46, v46, v243\n"
"v_mul_f32 v47, v47, v243\n"
"v_mul_f32 v56, v56, v243\n"
"v_mul_f32 v57, v57, v243\n"
"v_mul_f32 v58, v58, v243\n"
"v_mul_f32 v59, v59, v243\n"
"v_mul_f32 v68, v68, v243\n"
"v_mul_f32 v69, v69, v243\n"
"v_mul_f32 v70, v70, v243\n"
"v_mul_f32 v71, v71, v243\n"
"v_mul_f32 v80, v80, v243\n"
"v_mul_f32 v81, v81, v243\n"
"v_mul_f32 v82, v82, v243\n"
"v_mul_f32 v83, v83, v243\n"
"v_mul_f32 v92, v92, v243\n"
"v_mul_f32 v93, v93, v243\n"
"v_mul_f32 v94, v94, v243\n"
"v_mul_f32 v95, v95, v243\n"
"v_exp_f32 v176, v176\n"
"v_exp_f32 v177, v177\n"
"v_exp_f32 v178, v178\n"
"v_exp_f32 v179, v179\n"
"v_exp_f32 v188, v188\n"
"v_exp_f32 v189, v189\n"
"v_exp_f32 v190, v190\n"
"v_exp_f32 v191, v191\n"
"s_nop 1\n"
"v_cvt_pk_bf16_f32 v176, v176, v177\n"
"v_cvt_pk_bf16_f32 v177, v178, v179\n"
"v_cvt_pk_bf16_f32 v178, v188, v189\n"
"v_cvt_pk_bf16_f32 v179, v190, v191\n"
"v_mov_b32 v244, 0x3f803f80\n"
"v_mov_b32 v245, 0x3f803f80\n"
"v_mov_b32 v246, 0x3f803f80\n"
"v_mov_b32 v247, 0x3f803f80\n"
"s_nop 1\n"
"v_mfma_f32_16x16x32_bf16 v[180:183], v[244:247], v[168:171], 0\n"
"v_mfma_f32_16x16x32_bf16 v[184:187], v[244:247], v[172:175], 0\n"
"v_mfma_f32_16x16x32_bf16 v[188:191], v[244:247], v[176:179], 0\n"
"s_nop 7\n"
"s_nop 7\n"
"s_nop 3\n"
"s_branch .LrAby40\n"
".LrAy41:\n"
"s_nop 7\n"
"s_nop 7\n"
"v_bfe_u32 v243, v253, 4, 3\n"
"v_lshlrev_b32 v243, 8, v243\n"
"v_bfe_u32 v242, v253, 7, 1\n"
"v_lshl_add_u32 v243, v242, 11, v243\n"
"v_lshrrev_b32 v242, 8, v253\n"
"v_lshl_add_u32 v243, v242, 4, v243\n"
"s_and_b32 s70, s69, 15\n"
"s_lshr_b32 s71, s70, 0\n"
"s_lshl_b32 s71, s71, 2\n"
"s_and_b32 s70, s70, 0\n"
"s_or_b32 s71, s71, s70\n"
"s_lshl_b32 s71, s71, 11\n"
"s_lshr_b32 s70, s69, 4\n"
"s_lshl_b32 s70, s70, 17\n"
"s_add_u32 s71, s71, s70\n"
"s_add_u32 s70, s71, 0\n"
"buffer_load_dwordx4 v[244:247], v243, s[20:23], s70 offen offset:0\n"
"s_waitcnt vmcnt(0)\n"
"v_mfma_f32_16x16x32_bf16 v[144:147], v[244:247], v[96:99], v[192:195]\n"
"v_mfma_f32_16x16x32_bf16 v[148:151], v[244:247], v[112:115], v[196:199]\n"
"v_mfma_f32_16x16x32_bf16 v[152:155], v[244:247], v[128:131], v[200:203]\n"
"s_nop 7\n"
"buffer_load_dwordx4 v[244:247], v243, s[20:23], s70 offen offset:64\n"
"s_waitcnt vmcnt(0)\n"
"v_mfma_f32_16x16x32_bf16 v[144:147], v[244:247], v[100:103], v[144:147]\n"
"v_mfma_f32_16x16x32_bf16 v[148:151], v[244:247], v[116:119], v[148:151]\n"
"v_mfma_f32_16x16x32_bf16 v[152:155], v[244:247], v[132:135], v[152:155]\n"
"s_nop 7\n"
"buffer_load_dwordx4 v[244:247], v243, s[20:23], s70 offen offset:128\n"
"s_waitcnt vmcnt(0)\n"
"v_mfma_f32_16x16x32_bf16 v[144:147], v[244:247], v[104:107], v[144:147]\n"
"v_mfma_f32_16x16x32_bf16 v[148:151], v[244:247], v[120:123], v[148:151]\n"
"v_mfma_f32_16x16x32_bf16 v[152:155], v[244:247], v[136:139], v[152:155]\n"
"s_nop 7\n"
"buffer_load_dwordx4 v[244:247], v243, s[20:23], s70 offen offset:192\n"
"s_waitcnt vmcnt(0)\n"
"v_mfma_f32_16x16x32_bf16 v[144:147], v[244:247], v[108:111], v[144:147]\n"
"v_mfma_f32_16x16x32_bf16 v[148:151], v[244:247], v[124:127], v[148:151]\n"
"v_mfma_f32_16x16x32_bf16 v[152:155], v[244:247], v[140:143], v[152:155]\n"
"s_nop 7\n"
"s_add_u32 s70, s71, 4096\n"
"buffer_load_dwordx4 v[244:247], v243, s[20:23], s70 offen offset:0\n"
"s_waitcnt vmcnt(0)\n"
"v_mfma_f32_16x16x32_bf16 v[156:159], v[244:247], v[96:99], v[192:195]\n"
"v_mfma_f32_16x16x32_bf16 v[160:163], v[244:247], v[112:115], v[196:199]\n"
"v_mfma_f32_16x16x32_bf16 v[164:167], v[244:247], v[128:131], v[200:203]\n"
"s_nop 7\n"
"buffer_load_dwordx4 v[244:247], v243, s[20:23], s70 offen offset:64\n"
"s_waitcnt vmcnt(0)\n"
"v_mfma_f32_16x16x32_bf16 v[156:159], v[244:247], v[100:103], v[156:159]\n"
"v_mfma_f32_16x16x32_bf16 v[160:163], v[244:247], v[116:119], v[160:163]\n"
"v_mfma_f32_16x16x32_bf16 v[164:167], v[244:247], v[132:135], v[164:167]\n"
"s_nop 7\n"
"buffer_load_dwordx4 v[244:247], v243, s[20:23], s70 offen offset:128\n"
"s_waitcnt vmcnt(0)\n"
"v_mfma_f32_16x16x32_bf16 v[156:159], v[244:247], v[104:107], v[156:159]\n"
"v_mfma_f32_16x16x32_bf16 v[160:163], v[244:247], v[120:123], v[160:163]\n"
"v_mfma_f32_16x16x32_bf16 v[164:167], v[244:247], v[136:139], v[164:167]\n"
"s_nop 7\n"
"buffer_load_dwordx4 v[244:247], v243, s[20:23], s70 offen offset:192\n"
"s_waitcnt vmcnt(0)\n"
"v_mfma_f32_16x16x32_bf16 v[156:159], v[244:247], v[108:111], v[156:159]\n"
"v_mfma_f32_16x16x32_bf16 v[160:163], v[244:247], v[124:127], v[160:163]\n"
"v_mfma_f32_16x16x32_bf16 v[164:167], v[244:247], v[140:143], v[164:167]\n"
"s_nop 7\n"
"s_nop 7\n"
"s_nop 7\n"
"v_max3_f32 v242, v144, v145, v146\n"
"v_max3_f32 v242, v242, v147, v156\n"
"v_max3_f32 v242, v242, v157, v158\n"
"v_max_f32 v242, v242, v159\n"
"v_mov_b32 v243, v242\n"
"s_nop 1\n"
"v_permlane32_swap_b32 v242, v243\n"
"v_max_f32 v242, v242, v243\n"
"v_mov_b32 v243, v242\n"
"s_nop 1\n"
"v_permlane16_swap_b32 v242, v243\n"
"v_max_f32 v242, v242, v243\n"
"v_max_f32 v242, 0, v242\n"
"v_sub_f32 v144, v144, v242\n"
"v_sub_f32 v145, v145, v242\n"
"v_sub_f32 v146, v146, v242\n"
"v_sub_f32 v147, v147, v242\n"
"v_sub_f32 v156, v156, v242\n"
"v_sub_f32 v157, v157, v242\n"
"v_sub_f32 v158, v158, v242\n"
"v_sub_f32 v159, v159, v242\n"
"v_sub_f32 v192, v192, v242\n"
"v_sub_f32 v193, v193, v242\n"
"v_sub_f32 v194, v194, v242\n"
"v_sub_f32 v195, v195, v242\n"
"v_sub_f32 v168, v168, v242\n"
"v_sub_f32 v169, v169, v242\n"
"v_sub_f32 v170, v170, v242\n"
"v_sub_f32 v171, v171, v242\n"
"v_sub_f32 v180, v180, v242\n"
"v_sub_f32 v181, v181, v242\n"
"v_sub_f32 v182, v182, v242\n"
"v_sub_f32 v183, v183, v242\n"
"v_exp_f32 v243, -v242\n"
"s_nop 1\n"
"v_mul_f32 v236, v236, v243\n"
"v_mul_f32 v0, v0, v243\n"
"v_mul_f32 v1, v1, v243\n"
"v_mul_f32 v2, v2, v243\n"
"v_mul_f32 v3, v3, v243\n"
"v_mul_f32 v12, v12, v243\n"
"v_mul_f32 v13, v13, v243\n"
"v_mul_f32 v14, v14, v243\n"
"v_mul_f32 v15, v15, v243\n"
"v_mul_f32 v24, v24, v243\n"
"v_mul_f32 v25, v25, v243\n"
"v_mul_f32 v26, v26, v243\n"
"v_mul_f32 v27, v27, v243\n"
"v_mul_f32 v36, v36, v243\n"
"v_mul_f32 v37, v37, v243\n"
"v_mul_f32 v38, v38, v243\n"
"v_mul_f32 v39, v39, v243\n"
"v_mul_f32 v48, v48, v243\n"
"v_mul_f32 v49, v49, v243\n"
"v_mul_f32 v50, v50, v243\n"
"v_mul_f32 v51, v51, v243\n"
"v_mul_f32 v60, v60, v243\n"
"v_mul_f32 v61, v61, v243\n"
"v_mul_f32 v62, v62, v243\n"
"v_mul_f32 v63, v63, v243\n"
"v_mul_f32 v72, v72, v243\n"
"v_mul_f32 v73, v73, v243\n"
"v_mul_f32 v74, v74, v243\n"
"v_mul_f32 v75, v75, v243\n"
"v_mul_f32 v84, v84, v243\n"
"v_mul_f32 v85, v85, v243\n"
"v_mul_f32 v86, v86, v243\n"
"v_mul_f32 v87, v87, v243\n"
"v_exp_f32 v144, v144\n"
"v_exp_f32 v145, v145\n"
"v_exp_f32 v146, v146\n"
"v_exp_f32 v147, v147\n"
"v_exp_f32 v156, v156\n"
"v_exp_f32 v157, v157\n"
"v_exp_f32 v158, v158\n"
"v_exp_f32 v159, v159\n"
"s_nop 1\n"
"v_cvt_pk_bf16_f32 v144, v144, v145\n"
"v_cvt_pk_bf16_f32 v145, v146, v147\n"
"v_cvt_pk_bf16_f32 v146, v156, v157\n"
"v_cvt_pk_bf16_f32 v147, v158, v159\n"
"v_max3_f32 v242, v148, v149, v150\n"
"v_max3_f32 v242, v242, v151, v160\n"
"v_max3_f32 v242, v242, v161, v162\n"
"v_max_f32 v242, v242, v163\n"
"v_mov_b32 v243, v242\n"
"s_nop 1\n"
"v_permlane32_swap_b32 v242, v243\n"
"v_max_f32 v242, v242, v243\n"
"v_mov_b32 v243, v242\n"
"s_nop 1\n"
"v_permlane16_swap_b32 v242, v243\n"
"v_max_f32 v242, v242, v243\n"
"v_max_f32 v242, 0, v242\n"
"v_sub_f32 v148, v148, v242\n"
"v_sub_f32 v149, v149, v242\n"
"v_sub_f32 v150, v150, v242\n"
"v_sub_f32 v151, v151, v242\n"
"v_sub_f32 v160, v160, v242\n"
"v_sub_f32 v161, v161, v242\n"
"v_sub_f32 v162, v162, v242\n"
"v_sub_f32 v163, v163, v242\n"
"v_sub_f32 v196, v196, v242\n"
"v_sub_f32 v197, v197, v242\n"
"v_sub_f32 v198, v198, v242\n"
"v_sub_f32 v199, v199, v242\n"
"v_sub_f32 v172, v172, v242\n"
"v_sub_f32 v173, v173, v242\n"
"v_sub_f32 v174, v174, v242\n"
"v_sub_f32 v175, v175, v242\n"
"v_sub_f32 v184, v184, v242\n"
"v_sub_f32 v185, v185, v242\n"
"v_sub_f32 v186, v186, v242\n"
"v_sub_f32 v187, v187, v242\n"
"v_exp_f32 v243, -v242\n"
"s_nop 1\n"
"v_mul_f32 v237, v237, v243\n"
"v_mul_f32 v4, v4, v243\n"
"v_mul_f32 v5, v5, v243\n"
"v_mul_f32 v6, v6, v243\n"
"v_mul_f32 v7, v7, v243\n"
"v_mul_f32 v16, v16, v243\n"
"v_mul_f32 v17, v17, v243\n"
"v_mul_f32 v18, v18, v243\n"
"v_mul_f32 v19, v19, v243\n"
"v_mul_f32 v28, v28, v243\n"
"v_mul_f32 v29, v29, v243\n"
"v_mul_f32 v30, v30, v243\n"
"v_mul_f32 v31, v31, v243\n"
"v_mul_f32 v40, v40, v243\n"
"v_mul_f32 v41, v41, v243\n"
"v_mul_f32 v42, v42, v243\n"
"v_mul_f32 v43, v43, v243\n"
"v_mul_f32 v52, v52, v243\n"
"v_mul_f32 v53, v53, v243\n"
"v_mul_f32 v54, v54, v243\n"
"v_mul_f32 v55, v55, v243\n"
"v_mul_f32 v64, v64, v243\n"
"v_mul_f32 v65, v65, v243\n"
"v_mul_f32 v66, v66, v243\n"
"v_mul_f32 v67, v67, v243\n"
"v_mul_f32 v76, v76, v243\n"
"v_mul_f32 v77, v77, v243\n"
"v_mul_f32 v78, v78, v243\n"
"v_mul_f32 v79, v79, v243\n"
"v_mul_f32 v88, v88, v243\n"
"v_mul_f32 v89, v89, v243\n"
"v_mul_f32 v90, v90, v243\n"
"v_mul_f32 v91, v91, v243\n"
"v_exp_f32 v148, v148\n"
"v_exp_f32 v149, v149\n"
"v_exp_f32 v150, v150\n"
"v_exp_f32 v151, v151\n"
"v_exp_f32 v160, v160\n"
"v_exp_f32 v161, v161\n"
"v_exp_f32 v162, v162\n"
"v_exp_f32 v163, v163\n"
"s_nop 1\n"
"v_cvt_pk_bf16_f32 v148, v148, v149\n"
"v_cvt_pk_bf16_f32 v149, v150, v151\n"
"v_cvt_pk_bf16_f32 v150, v160, v161\n"
"v_cvt_pk_bf16_f32 v151, v162, v163\n"
"v_max3_f32 v242, v152, v153, v154\n"
"v_max3_f32 v242, v242, v155, v164\n"
"v_max3_f32 v242, v242, v165, v166\n"
"v_max_f32 v242, v242, v167\n"
"v_mov_b32 v243, v242\n"
"s_nop 1\n"
"v_permlane32_swap_b32 v242, v243\n"
"v_max_f32 v242, v242, v243\n"
"v_mov_b32 v243, v242\n"
"s_nop 1\n"
"v_permlane16_swap_b32 v242, v243\n"
"v_max_f32 v242, v242, v243\n"
"v_max_f32 v242, 0, v242\n"
"v_sub_f32 v152, v152, v242\n"
"v_sub_f32 v153, v153, v242\n"
"v_sub_f32 v154, v154, v242\n"
"v_sub_f32 v155, v155, v242\n"
"v_sub_f32 v164, v164, v242\n"
"v_sub_f32 v165, v165, v242\n"
"v_sub_f32 v166, v166, v242\n"
"v_sub_f32 v167, v167, v242\n"
"v_sub_f32 v200, v200, v242\n"
"v_sub_f32 v201, v201, v242\n"
"v_sub_f32 v202, v202, v242\n"
"v_sub_f32 v203, v203, v242\n"
"v_sub_f32 v176, v176, v242\n"
"v_sub_f32 v177, v177, v242\n"
"v_sub_f32 v178, v178, v242\n"
"v_sub_f32 v179, v179, v242\n"
"v_sub_f32 v188, v188, v242\n"
"v_sub_f32 v189, v189, v242\n"
"v_sub_f32 v190, v190, v242\n"
"v_sub_f32 v191, v191, v242\n"
"v_exp_f32 v243, -v242\n"
"s_nop 1\n"
"v_mul_f32 v238, v238, v243\n"
"v_mul_f32 v8, v8, v243\n"
"v_mul_f32 v9, v9, v243\n"
"v_mul_f32 v10, v10, v243\n"
"v_mul_f32 v11, v11, v243\n"
"v_mul_f32 v20, v20, v243\n"
"v_mul_f32 v21, v21, v243\n"
"v_mul_f32 v22, v22, v243\n"
"v_mul_f32 v23, v23, v243\n"
"v_mul_f32 v32, v32, v243\n"
"v_mul_f32 v33, v33, v243\n"
"v_mul_f32 v34, v34, v243\n"
"v_mul_f32 v35, v35, v243\n"
"v_mul_f32 v44, v44, v243\n"
"v_mul_f32 v45, v45, v243\n"
"v_mul_f32 v46, v46, v243\n"
"v_mul_f32 v47, v47, v243\n"
"v_mul_f32 v56, v56, v243\n"
"v_mul_f32 v57, v57, v243\n"
"v_mul_f32 v58, v58, v243\n"
"v_mul_f32 v59, v59, v243\n"
"v_mul_f32 v68, v68, v243\n"
"v_mul_f32 v69, v69, v243\n"
"v_mul_f32 v70, v70, v243\n"
"v_mul_f32 v71, v71, v243\n"
"v_mul_f32 v80, v80, v243\n"
"v_mul_f32 v81, v81, v243\n"
"v_mul_f32 v82, v82, v243\n"
"v_mul_f32 v83, v83, v243\n"
"v_mul_f32 v92, v92, v243\n"
"v_mul_f32 v93, v93, v243\n"
"v_mul_f32 v94, v94, v243\n"
"v_mul_f32 v95, v95, v243\n"
"v_exp_f32 v152, v152\n"
"v_exp_f32 v153, v153\n"
"v_exp_f32 v154, v154\n"
"v_exp_f32 v155, v155\n"
"v_exp_f32 v164, v164\n"
"v_exp_f32 v165, v165\n"
"v_exp_f32 v166, v166\n"
"v_exp_f32 v167, v167\n"
"s_nop 1\n"
"v_cvt_pk_bf16_f32 v152, v152, v153\n"
"v_cvt_pk_bf16_f32 v153, v154, v155\n"
"v_cvt_pk_bf16_f32 v154, v164, v165\n"
"v_cvt_pk_bf16_f32 v155, v166, v167\n"
"v_mov_b32 v244, 0x3f803f80\n"
"v_mov_b32 v245, 0x3f803f80\n"
"v_mov_b32 v246, 0x3f803f80\n"
"v_mov_b32 v247, 0x3f803f80\n"
"s_nop 1\n"
"v_mfma_f32_16x16x32_bf16 v[156:159], v[244:247], v[144:147], 0\n"
"v_mfma_f32_16x16x32_bf16 v[160:163], v[244:247], v[148:151], 0\n"
"v_mfma_f32_16x16x32_bf16 v[164:167], v[244:247], v[152:155], 0\n"
"s_nop 7\n"
"s_nop 7\n"
"s_nop 3\n"
"s_branch .LrAby41\n"
".LrAy42:\n"
"s_nop 7\n"
"s_nop 7\n"
"v_bfe_u32 v243, v253, 4, 3\n"
"v_lshlrev_b32 v243, 8, v243\n"
"v_bfe_u32 v242, v253, 7, 1\n"
"v_lshl_add_u32 v243, v242, 11, v243\n"
"v_lshrrev_b32 v242, 8, v253\n"
"v_lshl_add_u32 v243, v242, 4, v243\n"
"s_and_b32 s70, s69, 15\n"
"s_lshr_b32 s71, s70, 0\n"
"s_lshl_b32 s71, s71, 2\n"
"s_and_b32 s70, s70, 0\n"
"s_or_b32 s71, s71, s70\n"
"s_lshl_b32 s71, s71, 11\n"
"s_lshr_b32 s70, s69, 4\n"
"s_lshl_b32 s70, s70, 17\n"
"s_add_u32 s71, s71, s70\n"
"s_add_u32 s70, s71, 0\n"
"buffer_load_dwordx4 v[244:247], v243, s[20:23], s70 offen offset:0\n"
"s_waitcnt vmcnt(0)\n"
"v_mfma_f32_16x16x32_bf16 v[168:171], v[244:247], v[96:99], v[192:195]\n"
"v_mfma_f32_16x16x32_bf16 v[172:175], v[244:247], v[112:115], v[196:199]\n"
"v_mfma_f32_16x16x32_bf16 v[176:179], v[244:247], v[128:131], v[200:203]\n"
"s_nop 7\n"
"buffer_load_dwordx4 v[244:247], v243, s[20:23], s70 offen offset:64\n"
"s_waitcnt vmcnt(0)\n"
"v_mfma_f32_16x16x32_bf16 v[168:171], v[244:247], v[100:103], v[168:171]\n"
"v_mfma_f32_16x16x32_bf16 v[172:175], v[244:247], v[116:119], v[172:175]\n"
"v_mfma_f32_16x16x32_bf16 v[176:179], v[244:247], v[132:135], v[176:179]\n"
"s_nop 7\n"
"buffer_load_dwordx4 v[244:247], v243, s[20:23], s70 offen offset:128\n"
"s_waitcnt vmcnt(0)\n"
"v_mfma_f32_16x16x32_bf16 v[168:171], v[244:247], v[104:107], v[168:171]\n"
"v_mfma_f32_16x16x32_bf16 v[172:175], v[244:247], v[120:123], v[172:175]\n"
"v_mfma_f32_16x16x32_bf16 v[176:179], v[244:247], v[136:139], v[176:179]\n"
"s_nop 7\n"
"buffer_load_dwordx4 v[244:247], v243, s[20:23], s70 offen offset:192\n"
"s_waitcnt vmcnt(0)\n"
"v_mfma_f32_16x16x32_bf16 v[168:171], v[244:247], v[108:111], v[168:171]\n"
"v_mfma_f32_16x16x32_bf16 v[172:175], v[244:247], v[124:127], v[172:175]\n"
"v_mfma_f32_16x16x32_bf16 v[176:179], v[244:247], v[140:143], v[176:179]\n"
"s_nop 7\n"
"s_add_u32 s70, s71, 4096\n"
"buffer_load_dwordx4 v[244:247], v243, s[20:23], s70 offen offset:0\n"
"s_waitcnt vmcnt(0)\n"
"v_mfma_f32_16x16x32_bf16 v[180:183], v[244:247], v[96:99], v[192:195]\n"
"v_mfma_f32_16x16x32_bf16 v[184:187], v[244:247], v[112:115], v[196:199]\n"
"v_mfma_f32_16x16x32_bf16 v[188:191], v[244:247], v[128:131], v[200:203]\n"
"s_nop 7\n"
"buffer_load_dwordx4 v[244:247], v243, s[20:23], s70 offen offset:64\n"
"s_waitcnt vmcnt(0)\n"
"v_mfma_f32_16x16x32_bf16 v[180:183], v[244:247], v[100:103], v[180:183]\n"
"v_mfma_f32_16x16x32_bf16 v[184:187], v[244:247], v[116:119], v[184:187]\n"
"v_mfma_f32_16x16x32_bf16 v[188:191], v[244:247], v[132:135], v[188:191]\n"
"s_nop 7\n"
"buffer_load_dwordx4 v[244:247], v243, s[20:23], s70 offen offset:128\n"
"s_waitcnt vmcnt(0)\n"
"v_mfma_f32_16x16x32_bf16 v[180:183], v[244:247], v[104:107], v[180:183]\n"
"v_mfma_f32_16x16x32_bf16 v[184:187], v[244:247], v[120:123], v[184:187]\n"
"v_mfma_f32_16x16x32_bf16 v[188:191], v[244:247], v[136:139], v[188:191]\n"
"s_nop 7\n"
"buffer_load_dwordx4 v[244:247], v243, s[20:23], s70 offen offset:192\n"
"s_waitcnt vmcnt(0)\n"
"v_mfma_f32_16x16x32_bf16 v[180:183], v[244:247], v[108:111], v[180:183]\n"
"v_mfma_f32_16x16x32_bf16 v[184:187], v[244:247], v[124:127], v[184:187]\n"
"v_mfma_f32_16x16x32_bf16 v[188:191], v[244:247], v[140:143], v[188:191]\n"
"s_nop 7\n"
"s_nop 7\n"
"s_nop 7\n"
"v_max3_f32 v242, v168, v169, v170\n"
"v_max3_f32 v242, v242, v171, v180\n"
"v_max3_f32 v242, v242, v181, v182\n"
"v_max_f32 v242, v242, v183\n"
"v_mov_b32 v243, v242\n"
"s_nop 1\n"
"v_permlane32_swap_b32 v242, v243\n"
"v_max_f32 v242, v242, v243\n"
"v_mov_b32 v243, v242\n"
"s_nop 1\n"
"v_permlane16_swap_b32 v242, v243\n"
"v_max_f32 v242, v242, v243\n"
"v_max_f32 v242, 0, v242\n"
"v_sub_f32 v168, v168, v242\n"
"v_sub_f32 v169, v169, v242\n"
"v_sub_f32 v170, v170, v242\n"
"v_sub_f32 v171, v171, v242\n"
"v_sub_f32 v180, v180, v242\n"
"v_sub_f32 v181, v181, v242\n"
"v_sub_f32 v182, v182, v242\n"
"v_sub_f32 v183, v183, v242\n"
"v_sub_f32 v192, v192, v242\n"
"v_sub_f32 v193, v193, v242\n"
"v_sub_f32 v194, v194, v242\n"
"v_sub_f32 v195, v195, v242\n"
"v_exp_f32 v243, -v242\n"
"s_nop 1\n"
"v_mul_f32 v236, v236, v243\n"
"v_mul_f32 v0, v0, v243\n"
"v_mul_f32 v1, v1, v243\n"
"v_mul_f32 v2, v2, v243\n"
"v_mul_f32 v3, v3, v243\n"
"v_mul_f32 v12, v12, v243\n"
"v_mul_f32 v13, v13, v243\n"
"v_mul_f32 v14, v14, v243\n"
"v_mul_f32 v15, v15, v243\n"
"v_mul_f32 v24, v24, v243\n"
"v_mul_f32 v25, v25, v243\n"
"v_mul_f32 v26, v26, v243\n"
"v_mul_f32 v27, v27, v243\n"
"v_mul_f32 v36, v36, v243\n"
"v_mul_f32 v37, v37, v243\n"
"v_mul_f32 v38, v38, v243\n"
"v_mul_f32 v39, v39, v243\n"
"v_mul_f32 v48, v48, v243\n"
"v_mul_f32 v49, v49, v243\n"
"v_mul_f32 v50, v50, v243\n"
"v_mul_f32 v51, v51, v243\n"
"v_mul_f32 v60, v60, v243\n"
"v_mul_f32 v61, v61, v243\n"
"v_mul_f32 v62, v62, v243\n"
"v_mul_f32 v63, v63, v243\n"
"v_mul_f32 v72, v72, v243\n"
"v_mul_f32 v73, v73, v243\n"
"v_mul_f32 v74, v74, v243\n"
"v_mul_f32 v75, v75, v243\n"
"v_mul_f32 v84, v84, v243\n"
"v_mul_f32 v85, v85, v243\n"
"v_mul_f32 v86, v86, v243\n"
"v_mul_f32 v87, v87, v243\n"
"v_exp_f32 v168, v168\n"
"v_exp_f32 v169, v169\n"
"v_exp_f32 v170, v170\n"
"v_exp_f32 v171, v171\n"
"v_exp_f32 v180, v180\n"
"v_exp_f32 v181, v181\n"
"v_exp_f32 v182, v182\n"
"v_exp_f32 v183, v183\n"
"s_nop 1\n"
"v_cvt_pk_bf16_f32 v168, v168, v169\n"
"v_cvt_pk_bf16_f32 v169, v170, v171\n"
"v_cvt_pk_bf16_f32 v170, v180, v181\n"
"v_cvt_pk_bf16_f32 v171, v182, v183\n"
"v_max3_f32 v242, v172, v173, v174\n"
"v_max3_f32 v242, v242, v175, v184\n"
"v_max3_f32 v242, v242, v185, v186\n"
"v_max_f32 v242, v242, v187\n"
"v_mov_b32 v243, v242\n"
"s_nop 1\n"
"v_permlane32_swap_b32 v242, v243\n"
"v_max_f32 v242, v242, v243\n"
"v_mov_b32 v243, v242\n"
"s_nop 1\n"
"v_permlane16_swap_b32 v242, v243\n"
"v_max_f32 v242, v242, v243\n"
"v_max_f32 v242, 0, v242\n"
"v_sub_f32 v172, v172, v242\n"
"v_sub_f32 v173, v173, v242\n"
"v_sub_f32 v174, v174, v242\n"
"v_sub_f32 v175, v175, v242\n"
"v_sub_f32 v184, v184, v242\n"
"v_sub_f32 v185, v185, v242\n"
"v_sub_f32 v186, v186, v242\n"
"v_sub_f32 v187, v187, v242\n"
"v_sub_f32 v196, v196, v242\n"
"v_sub_f32 v197, v197, v242\n"
"v_sub_f32 v198, v198, v242\n"
"v_sub_f32 v199, v199, v242\n"
"v_exp_f32 v243, -v242\n"
"s_nop 1\n"
"v_mul_f32 v237, v237, v243\n"
"v_mul_f32 v4, v4, v243\n"
"v_mul_f32 v5, v5, v243\n"
"v_mul_f32 v6, v6, v243\n"
"v_mul_f32 v7, v7, v243\n"
"v_mul_f32 v16, v16, v243\n"
"v_mul_f32 v17, v17, v243\n"
"v_mul_f32 v18, v18, v243\n"
"v_mul_f32 v19, v19, v243\n"
"v_mul_f32 v28, v28, v243\n"
"v_mul_f32 v29, v29, v243\n"
"v_mul_f32 v30, v30, v243\n"
"v_mul_f32 v31, v31, v243\n"
"v_mul_f32 v40, v40, v243\n"
"v_mul_f32 v41, v41, v243\n"
"v_mul_f32 v42, v42, v243\n"
"v_mul_f32 v43, v43, v243\n"
"v_mul_f32 v52, v52, v243\n"
"v_mul_f32 v53, v53, v243\n"
"v_mul_f32 v54, v54, v243\n"
"v_mul_f32 v55, v55, v243\n"
"v_mul_f32 v64, v64, v243\n"
"v_mul_f32 v65, v65, v243\n"
"v_mul_f32 v66, v66, v243\n"
"v_mul_f32 v67, v67, v243\n"
"v_mul_f32 v76, v76, v243\n"
"v_mul_f32 v77, v77, v243\n"
"v_mul_f32 v78, v78, v243\n"
"v_mul_f32 v79, v79, v243\n"
"v_mul_f32 v88, v88, v243\n"
"v_mul_f32 v89, v89, v243\n"
"v_mul_f32 v90, v90, v243\n"
"v_mul_f32 v91, v91, v243\n"
"v_exp_f32 v172, v172\n"
"v_exp_f32 v173, v173\n"
"v_exp_f32 v174, v174\n"
"v_exp_f32 v175, v175\n"
"v_exp_f32 v184, v184\n"
"v_exp_f32 v185, v185\n"
"v_exp_f32 v186, v186\n"
"v_exp_f32 v187, v187\n"
"s_nop 1\n"
"v_cvt_pk_bf16_f32 v172, v172, v173\n"
"v_cvt_pk_bf16_f32 v173, v174, v175\n"
"v_cvt_pk_bf16_f32 v174, v184, v185\n"
"v_cvt_pk_bf16_f32 v175, v186, v187\n"
"v_max3_f32 v242, v176, v177, v178\n"
"v_max3_f32 v242, v242, v179, v188\n"
"v_max3_f32 v242, v242, v189, v190\n"
"v_max_f32 v242, v242, v191\n"
"v_mov_b32 v243, v242\n"
"s_nop 1\n"
"v_permlane32_swap_b32 v242, v243\n"
"v_max_f32 v242, v242, v243\n"
"v_mov_b32 v243, v242\n"
"s_nop 1\n"
"v_permlane16_swap_b32 v242, v243\n"
"v_max_f32 v242, v242, v243\n"
"v_max_f32 v242, 0, v242\n"
"v_sub_f32 v176, v176, v242\n"
"v_sub_f32 v177, v177, v242\n"
"v_sub_f32 v178, v178, v242\n"
"v_sub_f32 v179, v179, v242\n"
"v_sub_f32 v188, v188, v242\n"
"v_sub_f32 v189, v189, v242\n"
"v_sub_f32 v190, v190, v242\n"
"v_sub_f32 v191, v191, v242\n"
"v_sub_f32 v200, v200, v242\n"
"v_sub_f32 v201, v201, v242\n"
"v_sub_f32 v202, v202, v242\n"
"v_sub_f32 v203, v203, v242\n"
"v_exp_f32 v243, -v242\n"
"s_nop 1\n"
"v_mul_f32 v238, v238, v243\n"
"v_mul_f32 v8, v8, v243\n"
"v_mul_f32 v9, v9, v243\n"
"v_mul_f32 v10, v10, v243\n"
"v_mul_f32 v11, v11, v243\n"
"v_mul_f32 v20, v20, v243\n"
"v_mul_f32 v21, v21, v243\n"
"v_mul_f32 v22, v22, v243\n"
"v_mul_f32 v23, v23, v243\n"
"v_mul_f32 v32, v32, v243\n"
"v_mul_f32 v33, v33, v243\n"
"v_mul_f32 v34, v34, v243\n"
"v_mul_f32 v35, v35, v243\n"
"v_mul_f32 v44, v44, v243\n"
"v_mul_f32 v45, v45, v243\n"
"v_mul_f32 v46, v46, v243\n"
"v_mul_f32 v47, v47, v243\n"
"v_mul_f32 v56, v56, v243\n"
"v_mul_f32 v57, v57, v243\n"
"v_mul_f32 v58, v58, v243\n"
"v_mul_f32 v59, v59, v243\n"
"v_mul_f32 v68, v68, v243\n"
"v_mul_f32 v69, v69, v243\n"
"v_mul_f32 v70, v70, v243\n"
"v_mul_f32 v71, v71, v243\n"
"v_mul_f32 v80, v80, v243\n"
"v_mul_f32 v81, v81, v243\n"
"v_mul_f32 v82, v82, v243\n"
"v_mul_f32 v83, v83, v243\n"
"v_mul_f32 v92, v92, v243\n"
"v_mul_f32 v93, v93, v243\n"
"v_mul_f32 v94, v94, v243\n"
"v_mul_f32 v95, v95, v243\n"
"v_exp_f32 v176, v176\n"
"v_exp_f32 v177, v177\n"
"v_exp_f32 v178, v178\n"
"v_exp_f32 v179, v179\n"
"v_exp_f32 v188, v188\n"
"v_exp_f32 v189, v189\n"
"v_exp_f32 v190, v190\n"
"v_exp_f32 v191, v191\n"
"s_nop 1\n"
"v_cvt_pk_bf16_f32 v176, v176, v177\n"
"v_cvt_pk_bf16_f32 v177, v178, v179\n"
"v_cvt_pk_bf16_f32 v178, v188, v189\n"
"v_cvt_pk_bf16_f32 v179, v190, v191\n"
"v_mov_b32 v244, 0x3f803f80\n"
"v_mov_b32 v245, 0x3f803f80\n"
"v_mov_b32 v246, 0x3f803f80\n"
"v_mov_b32 v247, 0x3f803f80\n"
"s_nop 1\n"
"v_mfma_f32_16x16x32_bf16 v[180:183], v[244:247], v[168:171], 0\n"
"v_mfma_f32_16x16x32_bf16 v[184:187], v[244:247], v[172:175], 0\n"
"v_mfma_f32_16x16x32_bf16 v[188:191], v[244:247], v[176:179], 0\n"
"s_nop 7\n"
"s_nop 7\n"
"s_nop 3\n"
"s_branch .LrAby42\n"
".Lend:\n"
  : "+v"(tid)
  : "s"(blockIdx.x), "s"(__builtin_amdgcn_kernarg_segment_ptr()), "s"((unsigned)(size_t)lds)
  : "v1","v2","v3","v4","v5","v6","v7","v8","v9","v10","v11","v12","v13","v14","v15","v16","v17","v18","v19","v20","v21","v22","v23","v24","v25","v26","v27","v28","v29","v30","v31","v32","v33","v34","v35","v36","v37","v38","v39","v40","v41","v42","v43","v44","v45","v46","v47","v48","v49","v50","v51","v52","v53","v54","v55","v56","v57","v58","v59","v60","v61","v62","v63","v64","v65","v66","v67","v68","v69","v70","v71","v72","v73","v74","v75","v76","v77","v78","v79","v80","v81","v82","v83","v84","v85","v86","v87","v88","v89","v90","v91","v92","v93","v94","v95","v96","v97","v98","v99","v100","v101","v102","v103","v104","v105","v106","v107","v108","v109","v110","v111","v112","v113","v114","v115","v116","v117","v118","v119","v120","v121","v122","v123","v124","v125","v126","v127","v128","v129","v130","v131","v132","v133","v134","v135","v136","v137","v138","v139","v140","v141","v142","v143","v144","v145","v146","v147","v148","v149","v150","v151","v152","v153","v154","v155","v156","v157","v158","v159","v160","v161","v162","v163","v164","v165","v166","v167","v168","v169","v170","v171","v172","v173","v174","v175","v176","v177","v178","v179","v180","v181","v182","v183","v184","v185","v186","v187","v188","v189","v190","v191","v192","v193","v194","v195","v196","v197","v198","v199","v200","v201","v202","v203","v204","v205","v206","v207","v208","v209","v210","v211","v212","v213","v214","v215","v216","v217","v218","v219","v220","v221","v222","v223","v224","v225","v226","v227","v228","v229","v230","v231","v232","v233","v234","v235","v236","v237","v238","v239","v240","v241","v242","v243","v244","v245","v246","v247","v248","v249","v250","v251","v252","v253","v254","v255","s4","s5","s6","s7","s8","s9","s10","s11","s12","s13","s14","s15","s16","s17","s18","s19","s20","s21","s22","s23","s24","s25","s26","s27","s28","s29","s30","s31","s33","s34","s35","s36","s37","s38","s39","s40","s41","s42","s43","s44","s45","s46","s47","s48","s49","s50","s51","s52","s53","s54","s55","s56","s57","s58","s59","s60","s61","s62","s63","s64","s65","s66","s67","s68","s69","s70","s71","s72","s73","s74","s75","s76","s77","s78","s79","s80","s81","s82","s83","s84","s85","s86","s87","s88","s89","s90","s91","s92","s93","s94","s95","s96","s97","s98","s99","vcc","memory");
}

extern "C" void kernel_launch(void* const* d_in, const int* in_sizes, int n_in, void* d_out, int out_size, void* d_ws, size_t ws_size, hipStream_t stream) {
  fused_attn<<<256, 512, 0, stream>>>(d_in[0], d_in[1], d_in[2], d_in[3], d_in[4], d_out, d_ws);
}
```
